```python
import torch

def _softmax(x, axis=-1):
    return torch.softmax(x, dim=axis)
def _gelu(x):
    return torch.nn.functional.gelu(x, approximate="tanh")
def _silu(x):
    return torch.nn.functional.silu(x)
def _sigmoid(x):
    return torch.sigmoid(x)
def _layernorm(x, axis=-1, eps=1e-5):
    m = x.mean(dim=axis, keepdim=True)
    v = x.var(dim=axis, keepdim=True, unbiased=False)
    return (x - m) / torch.sqrt(v + eps)
def _scatter_add(tgt, idx, src, axis=0):
    return tgt.clone().index_add_(axis, idx, src)
def _segment_sum(src, seg, n):
    out = torch.zeros((n,) + src.shape[1:], dtype=src.dtype)
    return out.index_add_(0, seg, src)
def _pad(x, pw):
    flat = tuple(v for pair in reversed(pw) for v in pair)
    return torch.nn.functional.pad(x, flat)

kernel_name = 'parallel_sum__sliding_block_cached__dense_activation_relu__decode'

def setup_inputs():

    _w = (lambda a: (lambda t: t.to(torch.bfloat16) if t.is_floating_point() else t)(torch.as_tensor(a)))
    def _A_setup():
        B, Sq, D = 1, 1, 640
        Hq, Hkv, Sk, Dh = 32, 32, 32768, 192
        assert Hq % Hkv == 0, (Hq, Hkv)
        return {
            "x": _w((lambda *s: torch.randn(*s, dtype=torch.bfloat16) * 0.05)(B, Sq, D)),
            "k_cache": _w((lambda *s: torch.randn(*s, dtype=torch.bfloat16) * 3.2)(B, Hkv, Sk, Dh)),
            "v_cache": _w((lambda *s: torch.randn(*s, dtype=torch.bfloat16) * 1.6)(B, Hkv, Sk, Dh)),
            "wq": _w((lambda *s: torch.randn(*s, dtype=torch.bfloat16) * 3.2)(D, Hq * Dh) * (D ** -0.5)),
            "wo": _w((lambda *s: torch.randn(*s, dtype=torch.bfloat16) * 1.6)(Hq * Dh, D) * ((Hq * Dh) ** -0.5)),
        }
    _A_in = _A_setup()

    def _B_setup():
        B, S, D, Dff = 1, 1, 640, 1536
        return {
            "x": _w((lambda *s: torch.randn(*s, dtype=torch.bfloat16) * 0.05)(B, S, D)),
            "w1": _w((lambda *s: torch.randn(*s, dtype=torch.bfloat16) * 0.05)(D, Dff)),
            "w2": _w((lambda *s: torch.randn(*s, dtype=torch.bfloat16) * 0.05)(Dff, D)),
        }
    _B_in = _B_setup()

    return {
        "A_x": _A_in["x"],
        "A_k_cache": _A_in["k_cache"],
        "A_v_cache": _A_in["v_cache"],
        "A_wq": _A_in["wq"],
        "A_wo": _A_in["wo"],
        "B_x": _B_in["x"],
        "B_w1": _B_in["w1"],
        "B_w2": _B_in["w2"],
    }

def reference(A_x, A_k_cache, A_v_cache, A_wq, A_wo, B_x, B_w1, B_w2):
    def _A_ref(x, k_cache, v_cache, wq, wo):
        B, Sq, D = x.shape
        Hq, Dh = 32, 192
        Hkv, Sk = k_cache.shape[1], k_cache.shape[2]
        W = min(4096, max(256, Sk // 4))
        rep = Hq // Hkv
        q = (lambda x, *d: x.permute(*d))((x @ wq).reshape(B, Sq, Hq, Dh), 0, 2, 1, 3)
        k = (lambda x, n, axis: torch.repeat_interleave(x, n, dim=axis))(k_cache[:, :, Sk - W:, :], rep, axis=1)
        v = (lambda x, n, axis: torch.repeat_interleave(x, n, dim=axis))(v_cache[:, :, Sk - W:, :], rep, axis=1)
        scores = torch.einsum("bhqd,bhkd->bhqk", q, k) * (Dh ** -0.5)
        p = _softmax(scores, axis=-1)
        o = torch.einsum("bhqk,bhkd->bhqd", p, v)
        return (lambda x, *d: x.permute(*d))(o, 0, 2, 1, 3).reshape(B, Sq, Hq * Dh) @ wo

    def _B_ref(x, w1, w2):
        h = torch.einsum("bsd,df->bsf", x, w1)
        zero = torch.zeros_like(h)
        h = torch.maximum(h, zero)
        return torch.einsum("bsf,fd->bsd", h, w2)

    x = A_x
    xn = _layernorm(x, axis=-1)
    return x + _A_ref(xn, A_k_cache, A_v_cache, A_wq, A_wo) + _B_ref(xn, B_w1, B_w2)
```

```cpp
#include <hip/hip_runtime.h>
#include <hip/hip_bf16.h>
#include <stdint.h>
#include <stdio.h>

#define KV_AUX 2
#define WIN 16
#define WO_EARLY 24
#define NRED 80

typedef unsigned int u32;
typedef unsigned short u16;
using u32x2 = __attribute__((ext_vector_type(2))) unsigned;
using u32x3 = __attribute__((ext_vector_type(3))) unsigned;
using u32x4 = __attribute__((ext_vector_type(4))) unsigned;
using f32x2 = __attribute__((ext_vector_type(2))) float;
using f32x4 = __attribute__((ext_vector_type(4))) float;
using bf16x2 = __attribute__((ext_vector_type(2))) __bf16;
using u16x2 = __attribute__((ext_vector_type(2))) unsigned short;

constexpr int D = 640, NH = 32, DH = 192, SK = 32768, WLEN = 4096;
constexpr int CPR = D / NRED;
constexpr int UPP = CPR / 2;
constexpr u32 WS_ML = 4096, WS_Y = 65536;
constexpr u32 WS_BYTES = WS_Y + 320u * 256u * 16u;
constexpr u32 TAGX = 0x9E3779B9u;
constexpr int SPIN_CAP = 400000;
constexpr float LOG2E = 1.4426950408889634f;

__device__ __forceinline__ float dot2(u32 a, u32 b, float acc) {
  return __builtin_amdgcn_fdot2_f32_bf16(__builtin_bit_cast(bf16x2, a), __builtin_bit_cast(bf16x2, b), acc, false);
}
template <int CTRL> __device__ __forceinline__ float dpp(float x) {
  return __builtin_bit_cast(float, __builtin_amdgcn_mov_dpp(__builtin_bit_cast(int, x), CTRL, 0xf, 0xf, true));
}
constexpr int XOR1 = 0xB1, XOR2 = 0x4E, XOR7 = 0x141, ROR8 = 0x128, ROR4 = 0x124;
__device__ __forceinline__ float sum8(float x) {
  x += dpp<XOR1>(x); x += dpp<XOR2>(x); x += dpp<XOR7>(x); return x;
}
__device__ __forceinline__ float sum16(float x) { x = sum8(x); x += dpp<ROR8>(x); return x; }
__device__ __forceinline__ float xrow_sum(float x) {
  auto s = __builtin_amdgcn_permlane16_swap(__float_as_uint(x), __float_as_uint(x), false, false);
  x = __uint_as_float(s[0]) + __uint_as_float(s[1]);
  auto t = __builtin_amdgcn_permlane32_swap(__float_as_uint(x), __float_as_uint(x), false, false);
  return __uint_as_float(t[0]) + __uint_as_float(t[1]);
}
__device__ __forceinline__ float xrow_max(float x) {
  auto s = __builtin_amdgcn_permlane16_swap(__float_as_uint(x), __float_as_uint(x), false, false);
  x = fmaxf(__uint_as_float(s[0]), __uint_as_float(s[1]));
  auto t = __builtin_amdgcn_permlane32_swap(__float_as_uint(x), __float_as_uint(x), false, false);
  return fmaxf(__uint_as_float(t[0]), __uint_as_float(t[1]));
}
__device__ __forceinline__ float wave_sum(float x) { return xrow_sum(sum16(x)); }
__device__ __forceinline__ float wave_max(float x) {
  x = fmaxf(x, dpp<XOR1>(x)); x = fmaxf(x, dpp<XOR2>(x)); x = fmaxf(x, dpp<XOR7>(x)); x = fmaxf(x, dpp<ROR8>(x));
  return xrow_max(x);
}
__device__ __forceinline__ u32 bfbits(float f) { return (u32)__builtin_bit_cast(u16, (__bf16)f); }
__device__ __forceinline__ float bfr(float f) { return __uint_as_float(bfbits(f) << 16); }
__device__ __forceinline__ float blo(u32 v) { return __uint_as_float(v << 16); }
__device__ __forceinline__ float bhi(u32 v) { return __uint_as_float(v & 0xffff0000u); }
__device__ __forceinline__ u32 plo(u32 a, u32 b) { return __builtin_amdgcn_perm(b, a, 0x05040100u); }
__device__ __forceinline__ u32 phi(u32 a, u32 b) { return __builtin_amdgcn_perm(b, a, 0x07060302u); }
#define BAR() asm volatile("s_waitcnt lgkmcnt(0)\n\ts_barrier" ::: "memory")
#define PIN() __builtin_amdgcn_sched_barrier(0)
#define KEEP(v) asm volatile("" : "+v"(v))
#define RSRC(p, n) __builtin_amdgcn_make_buffer_rsrc((void*)(p), 0, (n), 0x00020000)

constexpr int PART_W = 1568;
constexpr int L_PART = 0;
constexpr int L_YP = L_PART + 8 * PART_W;
constexpr int L_SC = L_YP + 8 * 480;
constexpr int L_PT = L_SC + 8 * 192;
constexpr int L_XS = L_PT + 8 * 64;
constexpr int L_QS = L_XS + 320;
constexpr int L_OP = L_QS + 96;
constexpr int L_HP = L_OP + 96;
constexpr int L_HS = L_HP + 256;
constexpr int L_FAC = L_HS + 8;
constexpr int L_RED = L_FAC + 8;
constexpr int L_END = L_RED + 128;

__global__ void __launch_bounds__(512, 2)
fused_decode_kernel(const u16* __restrict__ x, const u16* __restrict__ kc, const u16* __restrict__ vc,
                    const u16* __restrict__ wq, const u16* __restrict__ wo, const u16* __restrict__ w1,
                    const u16* __restrict__ w2, u16* __restrict__ out, u32* __restrict__ ws) {
  __shared__ __attribute__((aligned(16))) float lds[L_END];
  const int tid = threadIdx.x;
  const int lane = tid & 63;
  const int w = __builtin_amdgcn_readfirstlane(tid >> 6);
  const int b = blockIdx.x;
  const int h = (b & 7) * 4 + (b >> 6);
  const int s = (b >> 3) & 7;
  const int j = h * 8 + s;

  const auto rs_ws = RSRC(ws, WS_BYTES);
  const auto rs_x = RSRC(x, D * 2);
  const size_t kvoff = ((size_t)h * SK + (SK - WLEN) + s * 512 + w * 64) * DH;
  const auto rs_k = RSRC(kc + kvoff, 64 * DH * 2);
  const auto rs_v = RSRC(vc + kvoff, 64 * DH * 2);
  const auto rs_wq = RSRC(wq + (size_t)(80 * w) * (NH * DH) + h * DH, 79 * 12288 + 384);
  const auto rs_wo = RSRC(wo + (size_t)(h * DH) * D + 80 * w, 191 * 1280 + 160);
  const auto rs_w1 = RSRC(w1 + (size_t)(80 * w) * 1536 + 6 * j, 79 * 3072 + 12);
  const auto rs_w2 = RSRC(w2 + (size_t)(6 * j) * D + 80 * w, 5 * 1280 + 160);

  int rho[3], c8[3];
#pragma unroll
  for (int u = 0; u < 3; ++u) { const int g = 64 * u + lane; rho[u] = g / 24; c8[u] = g % 24; }
  const int r6 = (lane / 10) > 5 ? 5 : (lane / 10);
  const int ch = lane % 10;
  const int wo_voff = r6 * 1280 + ch * 16;

  const u32 E = __builtin_amdgcn_raw_buffer_load_b32(rs_ws, 0, 0, 16);
  const u32x4 xa = __builtin_amdgcn_raw_buffer_load_b128(rs_x, lane * 20, 0, 0);
  const u32 xb = __builtin_amdgcn_raw_buffer_load_b32(rs_x, lane * 20 + 16, 0, 0);
  PIN();
  u32x4 wqv[30];
#pragma unroll
  for (int i = 0; i < 30; ++i)
    wqv[i] = __builtin_amdgcn_raw_buffer_load_b128(rs_wq, rho[i % 3] * 12288 + c8[i % 3] * 16, (i / 3) * 8 * 12288, 0);
  const u32x3 w1a = __builtin_amdgcn_raw_buffer_load_b96(rs_w1, lane * 3072, 0, 0);
  const u32x3 w1b = __builtin_amdgcn_raw_buffer_load_b96(rs_w1, (64 + (lane & 15)) * 3072, 0, 0);
  const u32x4 w2v = __builtin_amdgcn_raw_buffer_load_b128(rs_w2, wo_voff, 0, 0);
  PIN();
  u32x4 kv[WIN];
#pragma unroll
  for (int i = 0; i < WIN; ++i) {
    kv[i] = __builtin_amdgcn_raw_buffer_load_b128(rs_k, lane * 16, i * 1024, KV_AUX);
    PIN();
  }

  u16* xs = reinterpret_cast<u16*>(lds + L_XS);
  {
    float xv[10];
    xv[0] = blo(xa.x); xv[1] = bhi(xa.x); xv[2] = blo(xa.y); xv[3] = bhi(xa.y);
    xv[4] = blo(xa.z); xv[5] = bhi(xa.z); xv[6] = blo(xa.w); xv[7] = bhi(xa.w);
    xv[8] = blo(xb); xv[9] = bhi(xb);
    float sm = 0.f;
#pragma unroll
    for (int e = 0; e < 10; ++e) sm += xv[e];
    const float mean = wave_sum(sm) * (1.0f / D);
    float sq = 0.f;
#pragma unroll
    for (int e = 0; e < 10; ++e) { const float d = xv[e] - mean; sq += d * d; }
    const float var = wave_sum(sq) * (1.0f / D);
    const float mb = bfr(mean);
    const float den = bfr(sqrtf(bfr(bfr(var) + 1e-5f)));
    u32 xn[10];
#pragma unroll
    for (int e = 0; e < 10; ++e) xn[e] = bfbits(bfr(xv[e] - mb) / den);
    u32* xs32 = reinterpret_cast<u32*>(xs) + lane * 5;
#pragma unroll
    for (int e = 0; e < 5; ++e) xs32[e] = xn[2 * e] | (xn[2 * e + 1] << 16);
  }
  u32 xp[5][3];
#pragma unroll
  for (int t2 = 0; t2 < 5; ++t2)
#pragma unroll
    for (int u = 0; u < 3; ++u) {
      u16x2 pr = {xs[80 * w + 16 * t2 + rho[u]], xs[80 * w + 16 * t2 + 8 + rho[u]]};
      xp[t2][u] = __builtin_bit_cast(u32, pr);
    }
  u32 xh;
  {
    u16x2 pr = {xs[80 * w + lane], xs[80 * w + 64 + (lane & 15)]};
    xh = __builtin_bit_cast(u32, pr);
    if (lane >= 16) xh &= 0xffffu;
  }

  float acc[3][8];
#pragma unroll
  for (int u = 0; u < 3; ++u)
#pragma unroll
    for (int e = 0; e < 8; ++e) acc[u][e] = 0.f;
#pragma unroll
  for (int t2 = 0; t2 < 5; ++t2) {
#pragma unroll
    for (int u = 0; u < 3; ++u) {
      const u32x4 A = wqv[6 * t2 + u], B = wqv[6 * t2 + 3 + u];
#pragma unroll
      for (int d = 0; d < 4; ++d) {
        acc[u][2 * d] = dot2(plo(A[d], B[d]), xp[t2][u], acc[u][2 * d]);
        acc[u][2 * d + 1] = dot2(phi(A[d], B[d]), xp[t2][u], acc[u][2 * d + 1]);
      }
    }
    PIN();
  }
  {
    float* pw = lds + L_PART + w * PART_W + lane * 8;
#pragma unroll
    for (int u = 0; u < 3; ++u) {
      *reinterpret_cast<f32x4*>(pw + u * 512) = f32x4{acc[u][0], acc[u][1], acc[u][2], acc[u][3]};
      *reinterpret_cast<f32x4*>(pw + u * 512 + 4) = f32x4{acc[u][4], acc[u][5], acc[u][6], acc[u][7]};
    }
  }
  {
    float hp[6];
#pragma unroll
    for (int d = 0; d < 3; ++d) {
      hp[2 * d] = sum16(dot2(plo(w1a[d], w1b[d]), xh, 0.f));
      hp[2 * d + 1] = sum16(dot2(phi(w1a[d], w1b[d]), xh, 0.f));
    }
    if ((lane & 15) == 0) {
      float* p = lds + L_HP + (w * 4 + (lane >> 4)) * 8;
      *reinterpret_cast<f32x4*>(p) = f32x4{hp[0], hp[1], hp[2], hp[3]};
      *reinterpret_cast<f32x2*>(p + 4) = f32x2{hp[4], hp[5]};
    }
  }
  PIN();
  u32x4 wov[32];
#pragma unroll
  for (int i = 0; i < WO_EARLY; ++i)
    wov[i] = __builtin_amdgcn_raw_buffer_load_b128(rs_wo, wo_voff, i * 7680, 0);
  PIN();
  BAR();
  if (w < 6) {
    const int col4 = tid >> 3, rg = tid & 7;
    const float* p = lds + L_PART + rg * PART_W + col4 * 4;
    f32x4 a = *reinterpret_cast<const f32x4*>(p);
#pragma unroll
    for (int r = 1; r < 8; ++r) a += *reinterpret_cast<const f32x4*>(p + r * 192);
    a.x = sum8(a.x); a.y = sum8(a.y); a.z = sum8(a.z); a.w = sum8(a.w);
    if (rg == 0) {
      u32x2 o;
      o.x = bfbits(a.x) | (bfbits(a.y) << 16);
      o.y = bfbits(a.z) | (bfbits(a.w) << 16);
      *reinterpret_cast<u32x2*>(lds + L_QS + col4 * 2) = o;
    }
  } else if (w == 6) {
    const int i = (lane >> 3) > 5 ? 5 : (lane >> 3), c = lane & 7;
    const float* p = lds + L_HP + c * 32 + i;
    float a = (p[0] + p[8]) + (p[16] + p[24]);
    a = sum8(a);
    a = fmaxf(bfr(a), 0.f);
    if (c == 0) reinterpret_cast<u32*>(lds + L_HS)[i] = __float_as_uint(a);
  }
  BAR();
  u32x4 q[3];
#pragma unroll
  for (int u = 0; u < 3; ++u) q[u] = *reinterpret_cast<const u32x4*>(lds + L_QS + c8[u] * 4);
  float y[8];
  {
    const u32 hhi = reinterpret_cast<const u32*>(lds + L_HS)[r6];
    const u32 hlo = hhi >> 16;
#pragma unroll
    for (int d = 0; d < 4; ++d) {
      y[2 * d] = dot2(w2v[d], hlo, 0.f);
      y[2 * d + 1] = dot2(w2v[d], hhi, 0.f);
      KEEP(y[2 * d]); KEEP(y[2 * d + 1]);
    }
  }
  PIN();

  float sv[3] = {0.f, 0.f, 0.f};
#pragma unroll
  for (int i = 0; i < 24; ++i) {
    const u32x4 d = kv[i % WIN];
    const int u = i % 3;
    float a = dot2(d.x, q[u].x, 0.f);
    float c = dot2(d.z, q[u].z, 0.f);
    a = dot2(d.y, q[u].y, a);
    c = dot2(d.w, q[u].w, c);
    const int n = i + WIN;
    if (n < 24) kv[i % WIN] = __builtin_amdgcn_raw_buffer_load_b128(rs_k, lane * 16, n * 1024, KV_AUX);
    else kv[i % WIN] = __builtin_amdgcn_raw_buffer_load_b128(rs_v, lane * 16, (n - 24) * 1024, KV_AUX);
    a = sum8(a + c);
    sv[i / 8] = ((lane & 7) == (i & 7)) ? a : sv[i / 8];
    PIN();
  }
  const u32 tagA = E + 1u, tagB = tagA ^ TAGX;
  float mw;
  {
    float* sc = lds + L_SC + w * 192;
#pragma unroll
    for (int a = 0; a < 3; ++a) sc[(8 * a + (lane & 7)) * 8 + (lane >> 3)] = sv[a];
    const float raw = (sc[3 * lane] + sc[3 * lane + 1]) + sc[3 * lane + 2];
    const float sco = bfr(bfr(raw) * 0.07216878364870322f);
    mw = wave_max(sco);
    const float p = __builtin_amdgcn_exp2f((sco - mw) * LOG2E);
    lds[L_PT + w * 64 + (lane & 7) * 8 + (lane >> 3)] = p;
    const float lw = wave_sum(p);
    if (lane == 0) {
      const u32x4 un = {__float_as_uint(mw), tagA, __float_as_uint(lw), tagB};
      __builtin_amdgcn_raw_buffer_store_b128(un, rs_ws, WS_ML + h * 1024 + (s * 8 + w) * 16, 0, 16);
    }
  }
  PIN();

#pragma unroll
  for (int u = 0; u < 3; ++u)
#pragma unroll
    for (int e = 0; e < 8; ++e) acc[u][e] = 0.f;
  f32x4 pv[3];
  u32x4 ml;
  constexpr int POLL_AT = 24 - WIN - 2 < 0 ? 0 : 24 - WIN - 2;
  constexpr int EVAL_AT = POLL_AT + WIN - 1;
#pragma unroll
  for (int i = 0; i < 24; ++i) {
    const int u = i % 3, t = i / 3;
    if (i % 12 == 0) {
#pragma unroll
      for (int uu = 0; uu < 3; ++uu)
        pv[uu] = *reinterpret_cast<const f32x4*>(lds + L_PT + w * 64 + rho[uu] * 8 + (i / 12) * 4);
    }
    const u32x4 d = kv[(24 + i) % WIN];
    const float pp = pv[u][t % 4];
#pragma unroll
    for (int c = 0; c < 4; ++c) {
      f32x2 a2 = {acc[u][2 * c], acc[u][2 * c + 1]};
      const f32x2 v2 = {blo(d[c]), bhi(d[c])};
      a2 = __builtin_elementwise_fma(v2, f32x2{pp, pp}, a2);
      KEEP(a2);
      acc[u][2 * c] = a2.x; acc[u][2 * c + 1] = a2.y;
    }
    if (i == POLL_AT) ml = __builtin_amdgcn_raw_buffer_load_b128(rs_ws, WS_ML + h * 1024 + lane * 16, 0, 16);
    const int n = i + WIN;
    if (n < 24) kv[(24 + i) % WIN] = __builtin_amdgcn_raw_buffer_load_b128(rs_v, lane * 16, n * 1024, KV_AUX);
    else if (WO_EARLY + (n - 24) < 32)
      wov[WO_EARLY + (n - 24)] = __builtin_amdgcn_raw_buffer_load_b128(rs_wo, wo_voff, (WO_EARLY + (n - 24)) * 7680, 0);
    if (i < WO_EARLY / 2) {
      const u32x4 A = wov[2 * i], B = wov[2 * i + 1];
      u32x4 L, H;
#pragma unroll
      for (int dd = 0; dd < 4; ++dd) { L[dd] = plo(A[dd], B[dd]); H[dd] = phi(A[dd], B[dd]); }
      KEEP(L); KEEP(H);
      wov[2 * i] = L; wov[2 * i + 1] = H;
    }
    if (i == EVAL_AT) {
      if (!__all(ml.y == tagA && ml.w == tagB)) {
        int spins = 0;
        do {
          __builtin_amdgcn_s_sleep(1);
          asm volatile("" ::: "memory");
          ml = __builtin_amdgcn_raw_buffer_load_b128(rs_ws, WS_ML + h * 1024 + lane * 16, 0, 16);
        } while (!__all(ml.y == tagA && ml.w == tagB) && ++spins < SPIN_CAP);
      }
      const float mi = __uint_as_float(ml.x), li = __uint_as_float(ml.z);
      const float M = wave_max(mi);
      const float Ls = wave_sum(__builtin_amdgcn_exp2f((mi - M) * LOG2E) * li);
      const float f = __builtin_amdgcn_exp2f((mw - M) * LOG2E) / Ls;
      if (lane == 0) lds[L_FAC + w] = f;
    }
    PIN();
  }
  {
    float* pw = lds + L_PART + w * PART_W + lane * 8;
#pragma unroll
    for (int u = 0; u < 3; ++u) {
      *reinterpret_cast<f32x4*>(pw + u * 512) = f32x4{acc[u][0], acc[u][1], acc[u][2], acc[u][3]};
      *reinterpret_cast<f32x4*>(pw + u * 512 + 4) = f32x4{acc[u][4], acc[u][5], acc[u][6], acc[u][7]};
    }
  }
  PIN();
#pragma unroll
  for (int i = WO_EARLY / 2; i < 16; ++i) {
    const u32x4 A = wov[2 * i], B = wov[2 * i + 1];
    u32x4 L, H;
#pragma unroll
    for (int dd = 0; dd < 4; ++dd) { L[dd] = plo(A[dd], B[dd]); H[dd] = phi(A[dd], B[dd]); }
    KEEP(L); KEEP(H);
    wov[2 * i] = L; wov[2 * i + 1] = H;
  }
  PIN();
  BAR();
  if (w < 6) {
    const int col4 = tid >> 3, rg = tid & 7;
    const float* p = lds + L_PART + rg * PART_W + col4 * 4;
    const float f = lds[L_FAC + rg];
    f32x4 a = *reinterpret_cast<const f32x4*>(p);
#pragma unroll
    for (int r = 1; r < 8; ++r) a += *reinterpret_cast<const f32x4*>(p + r * 192);
    a *= f;
    a.x = sum8(a.x); a.y = sum8(a.y); a.z = sum8(a.z); a.w = sum8(a.w);
    if (rg < 4) {
      const float v = rg == 0 ? a.x : rg == 1 ? a.y : rg == 2 ? a.z : a.w;
      const int c = col4 * 4 + rg;
      const int jj = c / 12, rem = c % 12;
      const int rr = rem >= 6 ? rem - 6 : rem, half = rem >= 6 ? 1 : 0;
      reinterpret_cast<u16*>(lds + L_OP)[(rr * 16 + jj) * 2 + half] = (u16)bfbits(v);
    }
  }
  BAR();
  {
    u32x4 op[4];
#pragma unroll
    for (int a = 0; a < 4; ++a) op[a] = *reinterpret_cast<const u32x4*>(lds + L_OP + r6 * 16 + a * 4);
#pragma unroll
    for (int i = 0; i < 16; ++i) {
      const u32 o2 = op[i / 4][i % 4];
#pragma unroll
      for (int dd = 0; dd < 4; ++dd) {
        y[2 * dd] = dot2(wov[2 * i][dd], o2, y[2 * dd]);
        y[2 * dd + 1] = dot2(wov[2 * i + 1][dd], o2, y[2 * dd + 1]);
      }
    }
#pragma unroll
    for (int e = 0; e < 8; ++e) KEEP(y[e]);
  }
  {
    float* yp = lds + L_YP + w * 480;
    if (lane < 60) {
      *reinterpret_cast<f32x4*>(yp + lane * 8) = f32x4{y[0], y[1], y[2], y[3]};
      *reinterpret_cast<f32x4*>(yp + lane * 8 + 4) = f32x4{y[4], y[5], y[6], y[7]};
    }
    if (lane < 40) {
      f32x2 a = *reinterpret_cast<const f32x2*>(yp + lane * 2);
#pragma unroll
      for (int r = 1; r < 6; ++r) a += *reinterpret_cast<const f32x2*>(yp + r * 80 + lane * 2);
      const int n2 = 40 * w + lane;
      const int r = n2 / UPP, ui = n2 % UPP;
      const u32x4 un = {__float_as_uint(a.x), tagA, __float_as_uint(a.y), tagB};
      __builtin_amdgcn_raw_buffer_store_b128(un, rs_ws, WS_Y + ((r * 256 + j) * UPP + ui) * 16, 0, 16);
    }
  }

  if (b < NRED) {
    constexpr int NU = 256 * UPP / 512;
    u32x4 un[NU];
    const int base = WS_Y + b * 256 * UPP * 16 + tid * 16;
#pragma unroll
    for (int k = 0; k < NU; ++k) un[k] = __builtin_amdgcn_raw_buffer_load_b128(rs_ws, base + k * 8192, 0, 16);
    int spins = 0;
    for (;;) {
      bool ok = true;
#pragma unroll
      for (int k = 0; k < NU; ++k) ok &= (un[k].y == tagA && un[k].w == tagB);
      if (__all(ok)) break;
      if (++spins > SPIN_CAP) break;
      asm volatile("" ::: "memory");
#pragma unroll
      for (int k = 0; k < NU; ++k)
        if (!(un[k].y == tagA && un[k].w == tagB))
          un[k] = __builtin_amdgcn_raw_buffer_load_b128(rs_ws, base + k * 8192, 0, 16);
    }
    float v0 = 0.f, v1 = 0.f;
#pragma unroll
    for (int k = 0; k < NU; ++k) { v0 += __uint_as_float(un[k].x); v1 += __uint_as_float(un[k].z); }
    if (UPP <= 2) { v0 += dpp<XOR2>(v0); v1 += dpp<XOR2>(v1); }
    if (UPP <= 4) { v0 += dpp<ROR4>(v0); v1 += dpp<ROR4>(v1); }
    v0 += dpp<ROR8>(v0); v1 += dpp<ROR8>(v1);
    v0 = xrow_sum(v0); v1 = xrow_sum(v1);
    if (lane < UPP) *reinterpret_cast<f32x2*>(lds + L_RED + w * 16 + lane * 2) = f32x2{v0, v1};
    BAR();
    if (tid < CPR) {
      float a = 0.f;
#pragma unroll
      for (int k = 0; k < 8; ++k) a += lds[L_RED + k * 16 + tid];
      const int col = b * CPR + tid;
      a += blo((u32)x[col]);
      out[col] = (u16)bfbits(a);
    }
    if (b == 0 && tid == 0) __builtin_amdgcn_raw_buffer_store_b32(tagA, rs_ws, 0, 0, 16);
  }
}

extern "C" void kernel_launch(void* const* d_in, const int* in_sizes, int n_in,
                              void* d_out, int out_size, void* d_ws, size_t ws_size,
                              hipStream_t stream) {
  fprintf(stderr, "ws_size=%zu\n", ws_size);
  if (ws_size < WS_BYTES) return;
  fused_decode_kernel<<<256, 512, 0, stream>>>(
      (const u16*)d_in[0], (const u16*)d_in[1], (const u16*)d_in[2], (const u16*)d_in[3],
      (const u16*)d_in[4], (const u16*)d_in[6], (const u16*)d_in[7], (u16*)d_out, (u32*)d_ws);
}
```

```cpp
#include <hip/hip_runtime.h>
#include <hip/hip_bf16.h>
#include <stdint.h>

#define KE 4
#define PACE 40
#define VW 8
#define BAR0 0
#define QFLAG 1
#define NRED 160
#define CODE_KB 0

typedef unsigned int u32;
typedef unsigned short u16;
typedef unsigned long long u64;
using u32x2 = __attribute__((ext_vector_type(2))) unsigned;
using u32x4 = __attribute__((ext_vector_type(4))) unsigned;
using f32x2 = __attribute__((ext_vector_type(2))) float;
using f32x4 = __attribute__((ext_vector_type(4))) float;
using bf16x2 = __attribute__((ext_vector_type(2))) __bf16;

constexpr int D = 640, NH = 32, DH = 192, SK = 32768, WLEN = 4096;
constexpr int CPR = D / NRED;
constexpr int UPP = CPR / 2;
constexpr u32 WS_Q = 4096;
constexpr u32 WS_O = WS_Q + 256u * 2048u;
constexpr u32 WS_Y = WS_O + 256u * 2048u;
constexpr u32 WS_BYTES = WS_Y + 320u * 256u * 16u;
constexpr int SPIN_CAP = 200000;
constexpr float LOG2E = 1.4426950408889634f;

__device__ __forceinline__ float dot2(u32 a, u32 b, float acc) {
  return __builtin_amdgcn_fdot2_f32_bf16(__builtin_bit_cast(bf16x2, a), __builtin_bit_cast(bf16x2, b), acc, false);
}
template <int CTRL> __device__ __forceinline__ float dpp(float x) {
  return __builtin_bit_cast(float, __builtin_amdgcn_mov_dpp(__builtin_bit_cast(int, x), CTRL, 0xf, 0xf, true));
}
constexpr int XOR1 = 0xB1, XOR2 = 0x4E, XOR7 = 0x141, ROR8 = 0x128, ROR4 = 0x124;
__device__ __forceinline__ float sum8(float x) {
  x += dpp<XOR1>(x); x += dpp<XOR2>(x); x += dpp<XOR7>(x); return x;
}
__device__ __forceinline__ float max8(float x) {
  x = fmaxf(x, dpp<XOR1>(x)); x = fmaxf(x, dpp<XOR2>(x)); x = fmaxf(x, dpp<XOR7>(x)); return x;
}
__device__ __forceinline__ float xrow_sum(float x) {
  auto s = __builtin_amdgcn_permlane16_swap(__float_as_uint(x), __float_as_uint(x), false, false);
  x = __uint_as_float(s[0]) + __uint_as_float(s[1]);
  auto t = __builtin_amdgcn_permlane32_swap(__float_as_uint(x), __float_as_uint(x), false, false);
  return __uint_as_float(t[0]) + __uint_as_float(t[1]);
}
__device__ __forceinline__ float xrow_max(float x) {
  auto s = __builtin_amdgcn_permlane16_swap(__float_as_uint(x), __float_as_uint(x), false, false);
  x = fmaxf(__uint_as_float(s[0]), __uint_as_float(s[1]));
  auto t = __builtin_amdgcn_permlane32_swap(__float_as_uint(x), __float_as_uint(x), false, false);
  return fmaxf(__uint_as_float(t[0]), __uint_as_float(t[1]));
}
__device__ __forceinline__ float hi_sum(float x) { x += dpp<ROR8>(x); return xrow_sum(x); }
__device__ __forceinline__ float hi_max(float x) { x = fmaxf(x, dpp<ROR8>(x)); return xrow_max(x); }
__device__ __forceinline__ float wave_sum(float x) { return hi_sum(sum8(x)); }
__device__ __forceinline__ float wave_max(float x) { return hi_max(max8(x)); }
__device__ __forceinline__ u32 bfbits(float f) { return (u32)__builtin_bit_cast(u16, (__bf16)f); }
__device__ __forceinline__ float bfr(float f) { return __uint_as_float(bfbits(f) << 16); }
__device__ __forceinline__ u32 pack2(float a, float b) {
  bf16x2 p = {(__bf16)a, (__bf16)b};
  return __builtin_bit_cast(u32, p);
}
__device__ __forceinline__ float blo(u32 v) { return __uint_as_float(v << 16); }
__device__ __forceinline__ float bhi(u32 v) { return __uint_as_float(v & 0xffff0000u); }
#define BAR() asm volatile("s_waitcnt lgkmcnt(0)\n\ts_barrier" ::: "memory")
#define PIN() __builtin_amdgcn_sched_barrier(0)
#define STAMP(i)
#define KEEP(v) asm volatile("" : "+v"(v))
#define RSRC(p, n) __builtin_amdgcn_make_buffer_rsrc((void*)(p), 0, (n), 0x00020000)
#define WAITVM(n) __builtin_amdgcn_s_waitcnt(0x0F70 | ((n) & 15) | (((n) >> 4) << 14))

constexpr int PART_W = 1568;
constexpr int L_PART = 0;
constexpr int L_SC = L_PART + 8 * PART_W;
constexpr int L_PT = L_SC + 8 * 192;
constexpr int L_QS = L_PT + 8 * 64;
constexpr int L_ML = L_QS + 128;
constexpr int L_OW = L_ML + 16;
constexpr int L_RED = L_OW + 128;
constexpr int L_YP = L_RED + 128;
constexpr int L_END = L_YP + 8 * 480;

__global__ void __launch_bounds__(512, 2)
fused_decode_kernel(const u16* __restrict__ x, const u16* __restrict__ kc, const u16* __restrict__ vc,
                    const u16* __restrict__ wq, const u16* __restrict__ wo, const u16* __restrict__ w1,
                    const u16* __restrict__ w2, u16* __restrict__ out, u32* __restrict__ ws) {
  __shared__ __attribute__((aligned(16))) float lds[L_END];
  const int tid = threadIdx.x;
  const int lane = tid & 63;
  const int w = __builtin_amdgcn_readfirstlane(tid >> 6);
  const int b = blockIdx.x;
  const int h = (b & 7) * 4 + (b >> 6);
  const int s = (b >> 3) & 7;
  const int j = h * 8 + s;

  const auto rs_ws = RSRC(ws, WS_BYTES);
  const auto rs_x = RSRC(x, D * 2);
  const u32x4 xa = __builtin_amdgcn_raw_buffer_load_b128(rs_x, lane * 20, 0, 0);
  const u32 xb = __builtin_amdgcn_raw_buffer_load_b32(rs_x, lane * 20 + 16, 0, 0);
  const int row0 = 80 * s + 10 * w;
  const bool isq = lane < 48;
  const int l1 = lane < 60 ? lane - 48 : 11;
  const int r2 = isq ? (lane >= 24) : (l1 >= 6);
  const int cch = isq ? lane - 24 * r2 : l1 - 6 * r2;
  u32x4 wv[5];
  {
    const char* p = isq ? (const char*)wq + (size_t)(row0 + r2) * 12288 + h * 384 + cch * 16
                        : (const char*)w1 + (size_t)(row0 + r2) * 3072 + h * 96 + cch * 16;
    const u32 stp = isq ? 24576u : 6144u;
#pragma unroll
    for (int i = 0; i < 5; ++i) {
      wv[i] = __builtin_nontemporal_load((const u32x4*)p);
      p += stp;
      PIN();
    }
  }
  const u32 E = __builtin_amdgcn_raw_buffer_load_b32(rs_ws, 0, 0, 16);
  const size_t kvoff = ((size_t)h * SK + (SK - WLEN) + s * 512 + w * 64) * DH;
  const auto rs_k = RSRC(kc + kvoff, 64 * DH * 2);
  const auto rs_v = RSRC(vc + kvoff, 64 * DH * 2);
  u32 cpf = 0;
  if (CODE_KB > 0 && w == 7) {
    const u64 pc = __builtin_amdgcn_s_getpc() & ~(u64)255;
    cpf = __builtin_amdgcn_raw_buffer_load_b32(RSRC(pc, CODE_KB * 1024), lane * 64, ((b >> 3) % ((CODE_KB + 3) / 4)) * 4096, 0);
  }
  PIN();
  u32x4 kv[24 + VW];
#pragma unroll
  for (int i = 0; i < KE; ++i) {
    kv[i] = __builtin_amdgcn_raw_buffer_load_b128(rs_k, lane * 16, i * 1024, 2);
    PIN();
  }
  u32 xr[5];
#pragma unroll
  for (int i = 0; i < 5; ++i) xr[i] = reinterpret_cast<const u32*>(x)[row0 / 2 + i];

  {
    float xv[10];
    xv[0] = blo(xa.x); xv[1] = bhi(xa.x); xv[2] = blo(xa.y); xv[3] = bhi(xa.y);
    xv[4] = blo(xa.z); xv[5] = bhi(xa.z); xv[6] = blo(xa.w); xv[7] = bhi(xa.w);
    xv[8] = blo(xb); xv[9] = bhi(xb);
    float sm = ((xv[0] + xv[1]) + (xv[2] + xv[3])) + ((xv[4] + xv[5]) + (xv[6] + xv[7])) + (xv[8] + xv[9]);
    const float mean = wave_sum(sm) * (1.0f / D);
    float sq = 0.f;
#pragma unroll
    for (int e = 0; e < 10; ++e) { const float d = xv[e] - mean; sq += d * d; }
    const float var = wave_sum(sq) * (1.0f / D);
    const float mb = bfr(mean);
    const float den = bfr(__builtin_amdgcn_sqrtf(bfr(bfr(var) + 1e-5f)));
    const float rinv = 1.0f / den;
    STAMP(1);
#pragma unroll
    for (int i = 0; i < 5; ++i) asm volatile("" : "+s"(xr[i]));
    float acc[8];
#pragma unroll
    for (int e = 0; e < 8; ++e) acc[e] = 0.f;
#pragma unroll
    for (int i = 0; i < 5; ++i) {
      const float v = r2 ? bhi(xr[i]) : blo(xr[i]);
      const u32 xl = bfbits(bfr(v - mb) * rinv), xh = xl << 16;
#pragma unroll
      for (int d = 0; d < 4; ++d) {
        acc[2 * d] = dot2(wv[i][d], xl, acc[2 * d]);
        acc[2 * d + 1] = dot2(wv[i][d], xh, acc[2 * d + 1]);
      }
    }
    if (lane < 60) {
      float* p = lds + L_PART + (2 * w + r2) * 244 + (isq ? 0 : 192) + cch * 8;
      *reinterpret_cast<f32x4*>(p) = f32x4{acc[0], acc[1], acc[2], acc[3]};
      *reinterpret_cast<f32x4*>(p + 4) = f32x4{acc[4], acc[5], acc[6], acc[7]};
    }
  }
  const u32 tag = E + 1u;
  volatile u32* qflag = reinterpret_cast<volatile u32*>(lds + L_QS + 124);
  if (QFLAG && tid == 64) *qflag = 0u;
  KEEP(cpf);
  STAMP(2);
  BAR();

  if (w == 0) {
    __builtin_amdgcn_s_setprio(3);
    const int t = lane < 60 ? lane : 59;
    const float* p = lds + L_PART + t * 4;
    f32x4 a = *reinterpret_cast<const f32x4*>(p);
#pragma unroll
    for (int r = 1; r < 16; ++r) a += *reinterpret_cast<const f32x4*>(p + r * 244);
    if (lane < 60) {
      const u32x4 u0 = {__float_as_uint(a.x), tag, __float_as_uint(a.y), tag};
      const u32x4 u1 = {__float_as_uint(a.z), tag, __float_as_uint(a.w), tag};
      __builtin_amdgcn_raw_buffer_store_b128(u0, rs_ws, lane * 32, WS_Q + j * 2048, 16);
      __builtin_amdgcn_raw_buffer_store_b128(u1, rs_ws, lane * 32 + 16, WS_Q + j * 2048, 16);
    }
    STAMP(3);
    f32x4 sum;
    int spins = 0;
    bool ok;
    do {
      asm volatile("" ::: "memory");
      u32x4 un[16];
#pragma unroll
      for (int k = 0; k < 16; ++k)
        un[k] = __builtin_amdgcn_raw_buffer_load_b128(rs_ws, t * 16 + (k & 1) * 960 + (k >> 1) * 2048, WS_Q + h * 16384, 16);
      u32 mn = tag, mx = tag;
      sum = f32x4{0.f, 0.f, 0.f, 0.f};
#pragma unroll
      for (int k = 0; k < 16; ++k) {
        mn = min(mn, min(un[k].y, un[k].w));
        mx = max(mx, max(un[k].y, un[k].w));
        sum[2 * (k & 1)] += __uint_as_float(un[k].x);
        sum[2 * (k & 1) + 1] += __uint_as_float(un[k].z);
      }
      ok = __all(mn == mx);
    } while (!ok && ++spins < SPIN_CAP);
    if (t >= 36) { sum.z = fmaxf(sum.z, 0.f); sum.w = fmaxf(sum.w, 0.f); }
    u32* qs = reinterpret_cast<u32*>(lds + L_QS);
    qs[t] = pack2(sum.x, sum.y);
    qs[60 + t] = pack2(sum.z, sum.w);
    STAMP(4);
    asm volatile("s_waitcnt lgkmcnt(0)" ::: "memory");
    *qflag = 1u;
  }
#pragma unroll
  for (int i = KE; i < 24 + VW; ++i) {
    if (w != 0) WAITVM(PACE - 1);
    if (i < 24) kv[i] = __builtin_amdgcn_raw_buffer_load_b128(rs_k, lane * 16, i * 1024, 2);
    else kv[i] = __builtin_amdgcn_raw_buffer_load_b128(rs_v, lane * 16, (i - 24) * 1024, 2);
    PIN();
  }
  int rho[3], c8[3];
#pragma unroll
  for (int u = 0; u < 3; ++u) { const int g = 64 * u + lane; rho[u] = g / 24; c8[u] = g % 24; }
  const int r6 = (lane / 10) > 5 ? 5 : (lane / 10);
  const int ch = lane - 10 * (lane / 10);
  const auto rs_wo = RSRC(wo + (size_t)(h * DH + 24 * s) * D + 80 * w, 23 * 1280 + 160);
  const auto rs_w2 = RSRC(w2 + (size_t)(48 * h + 6 * s) * D + 80 * w, 5 * 1280 + 160);
  const int wo_voff = r6 * 1280 + ch * 16;
  if (w != 0) {
    STAMP(3);
    int spins = 0;
    while (*qflag == 0u && ++spins < 100 * SPIN_CAP) __builtin_amdgcn_s_sleep(1);
    asm volatile("" ::: "memory");
    STAMP(4);
  }
  u32x4 q[3];
#pragma unroll
  for (int u = 0; u < 3; ++u) q[u] = *reinterpret_cast<const u32x4*>(lds + L_QS + c8[u] * 4);

  u32x4 wov[5];
  float sv[3] = {0.f, 0.f, 0.f};
#pragma unroll
  for (int i = 0; i < 24; ++i) {
    const u32x4 d = kv[i];
    const int u = i % 3;
    float a = dot2(d.x, q[u].x, 0.f);
    float c = dot2(d.z, q[u].z, 0.f);
    a = dot2(d.y, q[u].y, a);
    c = dot2(d.w, q[u].w, c);
    const int n = i + VW;
    if (n < 24) kv[i] = __builtin_amdgcn_raw_buffer_load_b128(rs_v, lane * 16, n * 1024, 2);
    else if (n < 28) wov[n - 24] = __builtin_amdgcn_raw_buffer_load_b128(rs_wo, wo_voff, (n - 24) * 7680, 2);
    else if (n == 28) wov[4] = __builtin_amdgcn_raw_buffer_load_b128(rs_w2, wo_voff, 0, 2);
    a = sum8(a + c);
    sv[i / 8] = ((lane & 7) == (i & 7)) ? a : sv[i / 8];
    PIN();
  }
  STAMP(5);
  {
    float* sc = lds + L_SC + w * 192;
#pragma unroll
    for (int a = 0; a < 3; ++a) sc[(8 * a + (lane & 7)) * 8 + (lane >> 3)] = sv[a];
    const float raw = (sc[3 * lane] + sc[3 * lane + 1]) + sc[3 * lane + 2];
    const float sco = bfr(bfr(raw) * 0.07216878364870322f);
    const float mw = wave_max(sco);
    const float p = __builtin_amdgcn_exp2f((sco - mw) * LOG2E);
    lds[L_PT + w * 64 + (lane & 7) * 8 + (lane >> 3)] = p;
    const float lw = wave_sum(p);
    if (lane == 0) *reinterpret_cast<f32x2*>(lds + L_ML + 2 * w) = f32x2{mw, lw};
  }
  PIN();

  float acc[3][8];
#pragma unroll
  for (int u = 0; u < 3; ++u)
#pragma unroll
    for (int e = 0; e < 8; ++e) acc[u][e] = 0.f;
  f32x4 pv[3];
#pragma unroll
  for (int i = 0; i < 24; ++i) {
    const int u = i % 3, t = i / 3;
    if (i % 12 == 0) {
#pragma unroll
      for (int uu = 0; uu < 3; ++uu)
        pv[uu] = *reinterpret_cast<const f32x4*>(lds + L_PT + w * 64 + rho[uu] * 8 + (i / 12) * 4);
    }
    const u32x4 d = i < VW ? kv[24 + i] : kv[i - VW];
    const float pp = pv[u][t % 4];
#pragma unroll
    for (int c = 0; c < 4; ++c) {
      f32x2 a2 = {acc[u][2 * c], acc[u][2 * c + 1]};
      const f32x2 v2 = {blo(d[c]), bhi(d[c])};
      a2 = __builtin_elementwise_fma(v2, f32x2{pp, pp}, a2);
      KEEP(a2);
      acc[u][2 * c] = a2.x; acc[u][2 * c + 1] = a2.y;
    }
    PIN();
  }
  STAMP(6);
  {
    float* pw = lds + L_PART + w * PART_W + lane * 8;
#pragma unroll
    for (int u = 0; u < 3; ++u) {
      *reinterpret_cast<f32x4*>(pw + u * 512) = f32x4{acc[u][0], acc[u][1], acc[u][2], acc[u][3]};
      *reinterpret_cast<f32x4*>(pw + u * 512 + 4) = f32x4{acc[u][4], acc[u][5], acc[u][6], acc[u][7]};
    }
  }
  BAR();
  if (w < 7) {
    const int col4 = tid >> 3, rg = tid & 7;
    const f32x2 ml = *reinterpret_cast<const f32x2*>(lds + L_ML + 2 * rg);
    const float M = max8(ml.x);
    const float f = __builtin_amdgcn_exp2f((ml.x - M) * LOG2E);
    if (w < 6) {
      const float* p = lds + L_PART + rg * PART_W + col4 * 4;
      f32x4 a = *reinterpret_cast<const f32x4*>(p);
#pragma unroll
      for (int r = 1; r < 8; ++r) a += *reinterpret_cast<const f32x4*>(p + r * 192);
      a *= f;
      a.x = sum8(a.x); a.y = sum8(a.y); a.z = sum8(a.z); a.w = sum8(a.w);
      if (rg < 2) {
        const int cs = col4 / 6, k = (col4 - 6 * cs) * 2 + rg;
        const u32x4 un = {__float_as_uint(rg ? a.z : a.x), tag, __float_as_uint(rg ? a.w : a.y), tag};
        __builtin_amdgcn_raw_buffer_store_b128(un, rs_ws, cs * 2048 + k * 16, WS_O + h * 16384 + s * 256, 16);
      }
    } else {
      const float L = sum8(f * ml.y);
      if (lane < 8) {
        const u32x4 un = {__float_as_uint(M), tag, __float_as_uint(L), tag};
        __builtin_amdgcn_raw_buffer_store_b128(un, rs_ws, lane * 2048 + 192, WS_O + h * 16384 + s * 256, 16);
      }
    }
  }
  STAMP(7);
  float y[8];
  {
    const u32 hid = reinterpret_cast<const u16*>(lds + L_QS)[192 + 6 * s + r6];
#pragma unroll
    for (int d = 0; d < 4; ++d) {
      y[2 * d] = dot2(wov[4][d], hid, 0.f);
      y[2 * d + 1] = dot2(wov[4][d], hid << 16, 0.f);
    }
    const int kk = lane & 7;
    const int ob = (lane >> 3) * 256;
    u32x4 u0, u1, u2;
    int spins = 0;
    bool ok;
    do {
      asm volatile("" ::: "memory");
      u0 = __builtin_amdgcn_raw_buffer_load_b128(rs_ws, ob + kk * 16, WS_O + j * 2048, 16);
      u1 = __builtin_amdgcn_raw_buffer_load_b128(rs_ws, ob + 128 + (kk & 3) * 16, WS_O + j * 2048, 16);
      u2 = __builtin_amdgcn_raw_buffer_load_b128(rs_ws, ob + 192, WS_O + j * 2048, 16);
      const u32 mn = min(min(min(u0.y, u0.w), min(u1.y, u1.w)), min(min(u2.y, u2.w), tag));
      const u32 mx = max(max(max(u0.y, u0.w), max(u1.y, u1.w)), max(max(u2.y, u2.w), tag));
      ok = __all(mn == mx);
    } while (!ok && ++spins < SPIN_CAP);
    STAMP(8);
    const float mp = __uint_as_float(u2.x);
    const float M = hi_max(mp);
    float f = __builtin_amdgcn_exp2f((mp - M) * LOG2E);
    const float L = hi_sum(f * __uint_as_float(u2.z));
    f *= __builtin_amdgcn_rcpf(L);
    const float o0 = hi_sum(f * __uint_as_float(u0.x)), o1 = hi_sum(f * __uint_as_float(u0.z));
    const float o2 = hi_sum(f * __uint_as_float(u1.x)), o3 = hi_sum(f * __uint_as_float(u1.z));
    u32* ow = reinterpret_cast<u32*>(lds + L_OW + w * 16);
    if (lane < 12) ow[lane] = lane < 8 ? pack2(o0, o1) : pack2(o2, o3);
    const u16* oh = reinterpret_cast<const u16*>(ow);
#pragma unroll
    for (int i = 0; i < 4; ++i) {
      const u32 ov = oh[6 * i + r6];
#pragma unroll
      for (int d = 0; d < 4; ++d) {
        y[2 * d] = dot2(wov[i][d], ov, y[2 * d]);
        y[2 * d + 1] = dot2(wov[i][d], ov << 16, y[2 * d + 1]);
      }
    }
  }
  {
    float* yp = lds + L_YP + w * 480;
    if (lane < 60) {
      *reinterpret_cast<f32x4*>(yp + lane * 8) = f32x4{y[0], y[1], y[2], y[3]};
      *reinterpret_cast<f32x4*>(yp + lane * 8 + 4) = f32x4{y[4], y[5], y[6], y[7]};
    }
    if (lane < 40) {
      f32x2 a = *reinterpret_cast<const f32x2*>(yp + lane * 2);
#pragma unroll
      for (int r = 1; r < 6; ++r) a += *reinterpret_cast<const f32x2*>(yp + r * 80 + lane * 2);
      const int n2 = 40 * w + lane;
      const int r = n2 / UPP, ui = n2 % UPP;
      const u32x4 un = {__float_as_uint(a.x), tag, __float_as_uint(a.y), tag};
      __builtin_amdgcn_raw_buffer_store_b128(un, rs_ws, (r * 256 * UPP + ui) * 16, WS_Y + j * UPP * 16, 16);
    }
  }
  STAMP(9);
  if (b < NRED) {
    constexpr int NU = 256 * UPP / 512;
    const u32 xo = x[b * CPR + (tid < CPR ? tid : 0)];
    float v0, v1;
    int spins = 0;
    bool ok;
    do {
      asm volatile("" ::: "memory");
      u32x4 un[NU];
#pragma unroll
      for (int k = 0; k < NU; ++k)
        un[k] = __builtin_amdgcn_raw_buffer_load_b128(rs_ws, tid * 16 + k * 8192, WS_Y + b * 256 * UPP * 16, 16);
      u32 mn = tag, mx = tag;
      v0 = 0.f; v1 = 0.f;
#pragma unroll
      for (int k = 0; k < NU; ++k) {
        mn = min(mn, min(un[k].y, un[k].w));
        mx = max(mx, max(un[k].y, un[k].w));
        v0 += __uint_as_float(un[k].x); v1 += __uint_as_float(un[k].z);
      }
      ok = __all(mn == mx);
    } while (!ok && ++spins < SPIN_CAP);
    STAMP(10);
    if (UPP <= 1) { v0 += dpp<XOR1>(v0); v1 += dpp<XOR1>(v1); }
    if (UPP <= 2) { v0 += dpp<XOR2>(v0); v1 += dpp<XOR2>(v1); }
    if (UPP <= 4) { v0 += dpp<ROR4>(v0); v1 += dpp<ROR4>(v1); }
    v0 = hi_sum(v0); v1 = hi_sum(v1);
    if (lane < UPP) *reinterpret_cast<f32x2*>(lds + L_RED + w * 16 + lane * 2) = f32x2{v0, v1};
    BAR();
    if (tid < CPR) {
      float a = 0.f;
#pragma unroll
      for (int k = 0; k < 8; ++k) a += lds[L_RED + k * 16 + tid];
      a += blo(xo);
      out[b * CPR + tid] = (u16)bfbits(a);
    }
    if (b == 0 && tid == 0) __builtin_amdgcn_raw_buffer_store_b32(tag, rs_ws, 0, 0, 16);
  }
}

extern "C" void kernel_launch(void* const* d_in, const int* in_sizes, int n_in,
                              void* d_out, int out_size, void* d_ws, size_t ws_size,
                              hipStream_t stream) {
  if (ws_size < WS_BYTES) return;
  fused_decode_kernel<<<256, 512, 0, stream>>>(
      (const u16*)d_in[0], (const u16*)d_in[1], (const u16*)d_in[2], (const u16*)d_in[3],
      (const u16*)d_in[4], (const u16*)d_in[6], (const u16*)d_in[7], (u16*)d_out, (u32*)d_ws);
}
```

```cpp
#define AMPK 0
#define AMPW 1
#include <hip/hip_runtime.h>
#include <hip/hip_bf16.h>
#include <stdint.h>

#define KE 4
#define PACE 8
#define VW 8
#define BAR0 0
#define QFLAG 1
#define NRED 160
#define CODE_KB 0

typedef unsigned int u32;
typedef unsigned short u16;
typedef unsigned long long u64;
using u32x2 = __attribute__((ext_vector_type(2))) unsigned;
using u32x4 = __attribute__((ext_vector_type(4))) unsigned;
using f32x2 = __attribute__((ext_vector_type(2))) float;
using f32x4 = __attribute__((ext_vector_type(4))) float;
using bf16x2 = __attribute__((ext_vector_type(2))) __bf16;

constexpr int D = 640, NH = 32, DH = 192, SK = 32768, WLEN = 4096;
constexpr int CPR = D / NRED;
constexpr int UPP = CPR / 2;
constexpr u32 WS_Q = 4096;
constexpr u32 WS_O = WS_Q + 256u * 2048u;
constexpr u32 WS_Y = WS_O + 256u * 2048u;
constexpr u32 WS_BYTES = WS_Y + 320u * 256u * 16u;
constexpr int SPIN_CAP = 200000;
constexpr float LOG2E = 1.4426950408889634f;

__device__ __forceinline__ float dot2(u32 a, u32 b, float acc) {
  return __builtin_amdgcn_fdot2_f32_bf16(__builtin_bit_cast(bf16x2, a), __builtin_bit_cast(bf16x2, b), acc, false);
}
template <int CTRL> __device__ __forceinline__ float dpp(float x) {
  return __builtin_bit_cast(float, __builtin_amdgcn_mov_dpp(__builtin_bit_cast(int, x), CTRL, 0xf, 0xf, true));
}
constexpr int XOR1 = 0xB1, XOR2 = 0x4E, XOR7 = 0x141, ROR8 = 0x128, ROR4 = 0x124;
__device__ __forceinline__ float sum8(float x) {
  x += dpp<XOR1>(x); x += dpp<XOR2>(x); x += dpp<XOR7>(x); return x;
}
__device__ __forceinline__ float max8(float x) {
  x = fmaxf(x, dpp<XOR1>(x)); x = fmaxf(x, dpp<XOR2>(x)); x = fmaxf(x, dpp<XOR7>(x)); return x;
}
__device__ __forceinline__ float xrow_sum(float x) {
  auto s = __builtin_amdgcn_permlane16_swap(__float_as_uint(x), __float_as_uint(x), false, false);
  x = __uint_as_float(s[0]) + __uint_as_float(s[1]);
  auto t = __builtin_amdgcn_permlane32_swap(__float_as_uint(x), __float_as_uint(x), false, false);
  return __uint_as_float(t[0]) + __uint_as_float(t[1]);
}
__device__ __forceinline__ float xrow_max(float x) {
  auto s = __builtin_amdgcn_permlane16_swap(__float_as_uint(x), __float_as_uint(x), false, false);
  x = fmaxf(__uint_as_float(s[0]), __uint_as_float(s[1]));
  auto t = __builtin_amdgcn_permlane32_swap(__float_as_uint(x), __float_as_uint(x), false, false);
  return fmaxf(__uint_as_float(t[0]), __uint_as_float(t[1]));
}
__device__ __forceinline__ float hi_sum(float x) { x += dpp<ROR8>(x); return xrow_sum(x); }
__device__ __forceinline__ float hi_max(float x) { x = fmaxf(x, dpp<ROR8>(x)); return xrow_max(x); }
__device__ __forceinline__ float wave_sum(float x) { return hi_sum(sum8(x)); }
__device__ __forceinline__ float wave_max(float x) { return hi_max(max8(x)); }
__device__ __forceinline__ u32 bfbits(float f) { return (u32)__builtin_bit_cast(u16, (__bf16)f); }
__device__ __forceinline__ float bfr(float f) { return __uint_as_float(bfbits(f) << 16); }
__device__ __forceinline__ u32 pack2(float a, float b) {
  bf16x2 p = {(__bf16)a, (__bf16)b};
  return __builtin_bit_cast(u32, p);
}
__device__ __forceinline__ float blo(u32 v) { return __uint_as_float(v << 16); }
__device__ __forceinline__ float bhi(u32 v) { return __uint_as_float(v & 0xffff0000u); }
#define BAR() asm volatile("s_waitcnt lgkmcnt(0)\n\ts_barrier" ::: "memory")
#define PIN() __builtin_amdgcn_sched_barrier(0)
#define STAMP(i) do { if ((i) == AMPK) { PIN(); tsel = (u32)__builtin_amdgcn_s_memrealtime(); PIN(); } } while (0)
#define KEEP(v) asm volatile("" : "+v"(v))
#define RSRC(p, n) __builtin_amdgcn_make_buffer_rsrc((void*)(p), 0, (n), 0x00020000)
#define WAITVM(n) __builtin_amdgcn_s_waitcnt(0x0F70 | ((n) & 15) | (((n) >> 4) << 14))

constexpr int PART_W = 1568;
constexpr int L_PART = 0;
constexpr int L_SC = L_PART + 8 * PART_W;
constexpr int L_PT = L_SC + 8 * 192;
constexpr int L_QS = L_PT + 8 * 64;
constexpr int L_ML = L_QS + 128;
constexpr int L_OW = L_ML + 16;
constexpr int L_RED = L_OW + 128;
constexpr int L_YP = L_RED + 128;
constexpr int L_END = L_YP + 8 * 480;

__global__ void __launch_bounds__(512, 2)
fused_decode_kernel(const u16* __restrict__ x, const u16* __restrict__ kc, const u16* __restrict__ vc,
                    const u16* __restrict__ wq, const u16* __restrict__ wo, const u16* __restrict__ w1,
                    const u16* __restrict__ w2, u16* __restrict__ out, u32* __restrict__ ws) {
  __shared__ __attribute__((aligned(16))) float lds[L_END];
  const u32 tst = (u32)__builtin_amdgcn_s_memrealtime();
  u32 tsel = AMPK == 0 ? tst : 0u;
  const int tid = threadIdx.x;
  const int lane = tid & 63;
  const int w = __builtin_amdgcn_readfirstlane(tid >> 6);
  const int b = blockIdx.x;
  const int h = (b & 7) * 4 + (b >> 6);
  const int s = (b >> 3) & 7;
  const int j = h * 8 + s;

  const auto rs_ws = RSRC(ws, WS_BYTES);
  const auto rs_x = RSRC(x, D * 2);
  const u32x4 xa = __builtin_amdgcn_raw_buffer_load_b128(rs_x, lane * 20, 0, 0);
  const u32 xb = __builtin_amdgcn_raw_buffer_load_b32(rs_x, lane * 20 + 16, 0, 0);
  const int row0 = 80 * s + 10 * w;
  const bool isq = lane < 48;
  const int l1 = lane < 60 ? lane - 48 : 11;
  const int r2 = isq ? (lane >= 24) : (l1 >= 6);
  const int cch = isq ? lane - 24 * r2 : l1 - 6 * r2;
  u32x4 wv[5];
  {
    const char* p = isq ? (const char*)wq + (size_t)(row0 + r2) * 12288 + h * 384 + cch * 16
                        : (const char*)w1 + (size_t)(row0 + r2) * 3072 + h * 96 + cch * 16;
    const u32 stp = isq ? 24576u : 6144u;
#pragma unroll
    for (int i = 0; i < 5; ++i) {
      wv[i] = __builtin_nontemporal_load((const u32x4*)p);
      p += stp;
      PIN();
    }
  }
  const u32 E = __builtin_amdgcn_raw_buffer_load_b32(rs_ws, 0, 0, 16);
  const size_t kvoff = ((size_t)h * SK + (SK - WLEN) + s * 512 + w * 64) * DH;
  const auto rs_k = RSRC(kc + kvoff, 64 * DH * 2);
  const auto rs_v = RSRC(vc + kvoff, 64 * DH * 2);
  u32 cpf = 0;
  if (CODE_KB > 0 && w == 7) {
    const u64 pc = __builtin_amdgcn_s_getpc() & ~(u64)255;
    cpf = __builtin_amdgcn_raw_buffer_load_b32(RSRC(pc, CODE_KB * 1024), lane * 64, ((b >> 3) % ((CODE_KB + 3) / 4)) * 4096, 0);
  }
  PIN();
  u32x4 kv[24 + VW];
#pragma unroll
  for (int i = 0; i < KE; ++i) {
    kv[i] = __builtin_amdgcn_raw_buffer_load_b128(rs_k, lane * 16, i * 1024, 2);
    PIN();
  }
  u32 xr[5];
#pragma unroll
  for (int i = 0; i < 5; ++i) xr[i] = reinterpret_cast<const u32*>(x)[row0 / 2 + i];

  {
    float xv[10];
    xv[0] = blo(xa.x); xv[1] = bhi(xa.x); xv[2] = blo(xa.y); xv[3] = bhi(xa.y);
    xv[4] = blo(xa.z); xv[5] = bhi(xa.z); xv[6] = blo(xa.w); xv[7] = bhi(xa.w);
    xv[8] = blo(xb); xv[9] = bhi(xb);
    float sm = ((xv[0] + xv[1]) + (xv[2] + xv[3])) + ((xv[4] + xv[5]) + (xv[6] + xv[7])) + (xv[8] + xv[9]);
    const float mean = wave_sum(sm) * (1.0f / D);
    float sq = 0.f;
#pragma unroll
    for (int e = 0; e < 10; ++e) { const float d = xv[e] - mean; sq += d * d; }
    const float var = wave_sum(sq) * (1.0f / D);
    const float mb = bfr(mean);
    const float den = bfr(__builtin_amdgcn_sqrtf(bfr(bfr(var) + 1e-5f)));
    const float rinv = 1.0f / den;
    STAMP(1);
#pragma unroll
    for (int i = 0; i < 5; ++i) asm volatile("" : "+s"(xr[i]));
    float acc[8];
#pragma unroll
    for (int e = 0; e < 8; ++e) acc[e] = 0.f;
#pragma unroll
    for (int i = 0; i < 5; ++i) {
      const float v = r2 ? bhi(xr[i]) : blo(xr[i]);
      const u32 xl = bfbits(bfr(v - mb) * rinv), xh = xl << 16;
#pragma unroll
      for (int d = 0; d < 4; ++d) {
        acc[2 * d] = dot2(wv[i][d], xl, acc[2 * d]);
        acc[2 * d + 1] = dot2(wv[i][d], xh, acc[2 * d + 1]);
      }
    }
    if (lane < 60) {
      float* p = lds + L_PART + (2 * w + r2) * 244 + (isq ? 0 : 192) + cch * 8;
      *reinterpret_cast<f32x4*>(p) = f32x4{acc[0], acc[1], acc[2], acc[3]};
      *reinterpret_cast<f32x4*>(p + 4) = f32x4{acc[4], acc[5], acc[6], acc[7]};
    }
  }
  const u32 tag = E + 1u;
  volatile u32* qflag = reinterpret_cast<volatile u32*>(lds + L_QS + 124);
  if (QFLAG && tid == 64) *qflag = 0u;
  KEEP(cpf);
  if (b == 0 && tid == 0) { u32x2 g = {tst, tag}; __builtin_amdgcn_raw_buffer_store_b64(g, rs_ws, 64, 0, 16); }
  STAMP(2);
  BAR();

  if (w == 0) {
    __builtin_amdgcn_s_setprio(3);
    const int t = lane < 60 ? lane : 59;
    const float* p = lds + L_PART + t * 4;
    f32x4 a = *reinterpret_cast<const f32x4*>(p);
#pragma unroll
    for (int r = 1; r < 16; ++r) a += *reinterpret_cast<const f32x4*>(p + r * 244);
    if (lane < 60) {
      const u32x4 u0 = {__float_as_uint(a.x), tag, __float_as_uint(a.y), tag};
      const u32x4 u1 = {__float_as_uint(a.z), tag, __float_as_uint(a.w), tag};
      __builtin_amdgcn_raw_buffer_store_b128(u0, rs_ws, lane * 32, WS_Q + j * 2048, 16);
      __builtin_amdgcn_raw_buffer_store_b128(u1, rs_ws, lane * 32 + 16, WS_Q + j * 2048, 16);
    }
    STAMP(3);
    f32x4 sum;
    int spins = 0;
    bool ok;
    do {
      asm volatile("" ::: "memory");
      u32x4 un[16];
#pragma unroll
      for (int k = 0; k < 16; ++k)
        un[k] = __builtin_amdgcn_raw_buffer_load_b128(rs_ws, t * 16 + (k & 1) * 960 + (k >> 1) * 2048, WS_Q + h * 16384, 16);
      u32 mn = tag, mx = tag;
      sum = f32x4{0.f, 0.f, 0.f, 0.f};
#pragma unroll
      for (int k = 0; k < 16; ++k) {
        mn = min(mn, min(un[k].y, un[k].w));
        mx = max(mx, max(un[k].y, un[k].w));
        sum[2 * (k & 1)] += __uint_as_float(un[k].x);
        sum[2 * (k & 1) + 1] += __uint_as_float(un[k].z);
      }
      ok = __all(mn == mx);
    } while (!ok && ++spins < SPIN_CAP);
    if (t >= 36) { sum.z = fmaxf(sum.z, 0.f); sum.w = fmaxf(sum.w, 0.f); }
    u32* qs = reinterpret_cast<u32*>(lds + L_QS);
    qs[t] = pack2(sum.x, sum.y);
    qs[60 + t] = pack2(sum.z, sum.w);
    STAMP(4);
    asm volatile("s_waitcnt lgkmcnt(0)" ::: "memory");
    *qflag = 1u;
  }
#pragma unroll
  for (int i = KE; i < 24 + VW; ++i) {
    if (w != 0) WAITVM(PACE - 1);
    if (i < 24) kv[i] = __builtin_amdgcn_raw_buffer_load_b128(rs_k, lane * 16, i * 1024, 2);
    else kv[i] = __builtin_amdgcn_raw_buffer_load_b128(rs_v, lane * 16, (i - 24) * 1024, 2);
    PIN();
  }
  int rho[3], c8[3];
#pragma unroll
  for (int u = 0; u < 3; ++u) { const int g = 64 * u + lane; rho[u] = g / 24; c8[u] = g % 24; }
  const int r6 = (lane / 10) > 5 ? 5 : (lane / 10);
  const int ch = lane - 10 * (lane / 10);
  const auto rs_wo = RSRC(wo + (size_t)(h * DH + 24 * s) * D + 80 * w, 23 * 1280 + 160);
  const auto rs_w2 = RSRC(w2 + (size_t)(48 * h + 6 * s) * D + 80 * w, 5 * 1280 + 160);
  const int wo_voff = r6 * 1280 + ch * 16;
  if (w != 0) {
    STAMP(3);
    int spins = 0;
    while (*qflag == 0u && ++spins < 100 * SPIN_CAP) __builtin_amdgcn_s_sleep(1);
    asm volatile("" ::: "memory");
    STAMP(4);
  }
  u32x4 q[3];
#pragma unroll
  for (int u = 0; u < 3; ++u) q[u] = *reinterpret_cast<const u32x4*>(lds + L_QS + c8[u] * 4);

  u32x4 wov[5];
  float sv[3] = {0.f, 0.f, 0.f};
#pragma unroll
  for (int i = 0; i < 24; ++i) {
    const u32x4 d = kv[i];
    const int u = i % 3;
    float a = dot2(d.x, q[u].x, 0.f);
    float c = dot2(d.z, q[u].z, 0.f);
    a = dot2(d.y, q[u].y, a);
    c = dot2(d.w, q[u].w, c);
    const int n = i + VW;
    if (n < 24) kv[i] = __builtin_amdgcn_raw_buffer_load_b128(rs_v, lane * 16, n * 1024, 2);
    else if (n < 28) wov[n - 24] = __builtin_amdgcn_raw_buffer_load_b128(rs_wo, wo_voff, (n - 24) * 7680, 2);
    else if (n == 28) wov[4] = __builtin_amdgcn_raw_buffer_load_b128(rs_w2, wo_voff, 0, 2);
    a = sum8(a + c);
    sv[i / 8] = ((lane & 7) == (i & 7)) ? a : sv[i / 8];
    PIN();
  }
  STAMP(5);
  {
    float* sc = lds + L_SC + w * 192;
#pragma unroll
    for (int a = 0; a < 3; ++a) sc[(8 * a + (lane & 7)) * 8 + (lane >> 3)] = sv[a];
    const float raw = (sc[3 * lane] + sc[3 * lane + 1]) + sc[3 * lane + 2];
    const float sco = bfr(bfr(raw) * 0.07216878364870322f);
    const float mw = wave_max(sco);
    const float p = __builtin_amdgcn_exp2f((sco - mw) * LOG2E);
    lds[L_PT + w * 64 + (lane & 7) * 8 + (lane >> 3)] = p;
    const float lw = wave_sum(p);
    if (lane == 0) *reinterpret_cast<f32x2*>(lds + L_ML + 2 * w) = f32x2{mw, lw};
  }
  PIN();

  float acc[3][8];
#pragma unroll
  for (int u = 0; u < 3; ++u)
#pragma unroll
    for (int e = 0; e < 8; ++e) acc[u][e] = 0.f;
  f32x4 pv[3];
#pragma unroll
  for (int i = 0; i < 24; ++i) {
    const int u = i % 3, t = i / 3;
    if (i % 12 == 0) {
#pragma unroll
      for (int uu = 0; uu < 3; ++uu)
        pv[uu] = *reinterpret_cast<const f32x4*>(lds + L_PT + w * 64 + rho[uu] * 8 + (i / 12) * 4);
    }
    const u32x4 d = i < VW ? kv[24 + i] : kv[i - VW];
    const float pp = pv[u][t % 4];
#pragma unroll
    for (int c = 0; c < 4; ++c) {
      f32x2 a2 = {acc[u][2 * c], acc[u][2 * c + 1]};
      const f32x2 v2 = {blo(d[c]), bhi(d[c])};
      a2 = __builtin_elementwise_fma(v2, f32x2{pp, pp}, a2);
      KEEP(a2);
      acc[u][2 * c] = a2.x; acc[u][2 * c + 1] = a2.y;
    }
    PIN();
  }
  STAMP(6);
  {
    float* pw = lds + L_PART + w * PART_W + lane * 8;
#pragma unroll
    for (int u = 0; u < 3; ++u) {
      *reinterpret_cast<f32x4*>(pw + u * 512) = f32x4{acc[u][0], acc[u][1], acc[u][2], acc[u][3]};
      *reinterpret_cast<f32x4*>(pw + u * 512 + 4) = f32x4{acc[u][4], acc[u][5], acc[u][6], acc[u][7]};
    }
  }
  BAR();
  if (w < 7) {
    const int col4 = tid >> 3, rg = tid & 7;
    const f32x2 ml = *reinterpret_cast<const f32x2*>(lds + L_ML + 2 * rg);
    const float M = max8(ml.x);
    const float f = __builtin_amdgcn_exp2f((ml.x - M) * LOG2E);
    if (w < 6) {
      const float* p = lds + L_PART + rg * PART_W + col4 * 4;
      f32x4 a = *reinterpret_cast<const f32x4*>(p);
#pragma unroll
      for (int r = 1; r < 8; ++r) a += *reinterpret_cast<const f32x4*>(p + r * 192);
      a *= f;
      a.x = sum8(a.x); a.y = sum8(a.y); a.z = sum8(a.z); a.w = sum8(a.w);
      if (rg < 2) {
        const int cs = col4 / 6, k = (col4 - 6 * cs) * 2 + rg;
        const u32x4 un = {__float_as_uint(rg ? a.z : a.x), tag, __float_as_uint(rg ? a.w : a.y), tag};
        __builtin_amdgcn_raw_buffer_store_b128(un, rs_ws, cs * 2048 + k * 16, WS_O + h * 16384 + s * 256, 16);
      }
    } else {
      const float L = sum8(f * ml.y);
      if (lane < 8) {
        const u32x4 un = {__float_as_uint(M), tag, __float_as_uint(L), tag};
        __builtin_amdgcn_raw_buffer_store_b128(un, rs_ws, lane * 2048 + 192, WS_O + h * 16384 + s * 256, 16);
      }
    }
  }
  STAMP(7);
  float y[8];
  {
    const u32 hid = reinterpret_cast<const u16*>(lds + L_QS)[192 + 6 * s + r6];
#pragma unroll
    for (int d = 0; d < 4; ++d) {
      y[2 * d] = dot2(wov[4][d], hid, 0.f);
      y[2 * d + 1] = dot2(wov[4][d], hid << 16, 0.f);
    }
    const int kk = lane & 7;
    const int ob = (lane >> 3) * 256;
    u32x4 u0, u1, u2;
    int spins = 0;
    bool ok;
    do {
      asm volatile("" ::: "memory");
      u0 = __builtin_amdgcn_raw_buffer_load_b128(rs_ws, ob + kk * 16, WS_O + j * 2048, 16);
      u1 = __builtin_amdgcn_raw_buffer_load_b128(rs_ws, ob + 128 + (kk & 3) * 16, WS_O + j * 2048, 16);
      u2 = __builtin_amdgcn_raw_buffer_load_b128(rs_ws, ob + 192, WS_O + j * 2048, 16);
      const u32 mn = min(min(min(u0.y, u0.w), min(u1.y, u1.w)), min(min(u2.y, u2.w), tag));
      const u32 mx = max(max(max(u0.y, u0.w), max(u1.y, u1.w)), max(max(u2.y, u2.w), tag));
      ok = __all(mn == mx);
    } while (!ok && ++spins < SPIN_CAP);
    STAMP(8);
    const float mp = __uint_as_float(u2.x);
    const float M = hi_max(mp);
    float f = __builtin_amdgcn_exp2f((mp - M) * LOG2E);
    const float L = hi_sum(f * __uint_as_float(u2.z));
    f *= __builtin_amdgcn_rcpf(L);
    const float o0 = hi_sum(f * __uint_as_float(u0.x)), o1 = hi_sum(f * __uint_as_float(u0.z));
    const float o2 = hi_sum(f * __uint_as_float(u1.x)), o3 = hi_sum(f * __uint_as_float(u1.z));
    u32* ow = reinterpret_cast<u32*>(lds + L_OW + w * 16);
    if (lane < 12) ow[lane] = lane < 8 ? pack2(o0, o1) : pack2(o2, o3);
    const u16* oh = reinterpret_cast<const u16*>(ow);
#pragma unroll
    for (int i = 0; i < 4; ++i) {
      const u32 ov = oh[6 * i + r6];
#pragma unroll
      for (int d = 0; d < 4; ++d) {
        y[2 * d] = dot2(wov[i][d], ov, y[2 * d]);
        y[2 * d + 1] = dot2(wov[i][d], ov << 16, y[2 * d + 1]);
      }
    }
  }
  {
    float* yp = lds + L_YP + w * 480;
    if (lane < 60) {
      *reinterpret_cast<f32x4*>(yp + lane * 8) = f32x4{y[0], y[1], y[2], y[3]};
      *reinterpret_cast<f32x4*>(yp + lane * 8 + 4) = f32x4{y[4], y[5], y[6], y[7]};
    }
    if (lane < 40) {
      f32x2 a = *reinterpret_cast<const f32x2*>(yp + lane * 2);
#pragma unroll
      for (int r = 1; r < 6; ++r) a += *reinterpret_cast<const f32x2*>(yp + r * 80 + lane * 2);
      const int n2 = 40 * w + lane;
      const int r = n2 / UPP, ui = n2 % UPP;
      const u32x4 un = {__float_as_uint(a.x), tag, __float_as_uint(a.y), tag};
      __builtin_amdgcn_raw_buffer_store_b128(un, rs_ws, (r * 256 * UPP + ui) * 16, WS_Y + j * UPP * 16, 16);
    }
  }
  STAMP(9);
  if (b < NRED) {
    constexpr int NU = 256 * UPP / 512;
    const u32 xo = x[b * CPR + (tid < CPR ? tid : 0)];
    float v0, v1;
    int spins = 0;
    bool ok;
    do {
      asm volatile("" ::: "memory");
      u32x4 un[NU];
#pragma unroll
      for (int k = 0; k < NU; ++k)
        un[k] = __builtin_amdgcn_raw_buffer_load_b128(rs_ws, tid * 16 + k * 8192, WS_Y + b * 256 * UPP * 16, 16);
      u32 mn = tag, mx = tag;
      v0 = 0.f; v1 = 0.f;
#pragma unroll
      for (int k = 0; k < NU; ++k) {
        mn = min(mn, min(un[k].y, un[k].w));
        mx = max(mx, max(un[k].y, un[k].w));
        v0 += __uint_as_float(un[k].x); v1 += __uint_as_float(un[k].z);
      }
      ok = __all(mn == mx);
    } while (!ok && ++spins < SPIN_CAP);
    STAMP(10);
    if (UPP <= 1) { v0 += dpp<XOR1>(v0); v1 += dpp<XOR1>(v1); }
    if (UPP <= 2) { v0 += dpp<XOR2>(v0); v1 += dpp<XOR2>(v1); }
    if (UPP <= 4) { v0 += dpp<ROR4>(v0); v1 += dpp<ROR4>(v1); }
    v0 = hi_sum(v0); v1 = hi_sum(v1);
    if (lane < UPP) *reinterpret_cast<f32x2*>(lds + L_RED + w * 16 + lane * 2) = f32x2{v0, v1};
    BAR();
    if (tid < CPR) {
      float a = 0.f;
#pragma unroll
      for (int k = 0; k < 8; ++k) a += lds[L_RED + k * 16 + tid];
      a += blo(xo);
      out[b * CPR + tid] = (u16)bfbits(a);
    }
    if (b == 0 && tid == 0) __builtin_amdgcn_raw_buffer_store_b32(tag, rs_ws, 0, 0, 16);
  }
  STAMP(11);
  if (tsel != 0u && (AMPW)) {
    u32x2 g; int sp = 0;
    do { asm volatile("" ::: "memory"); g = __builtin_amdgcn_raw_buffer_load_b64(rs_ws, 64, 0, 16); } while (g.y != tag && ++sp < SPIN_CAP);
    const u32 t0 = __builtin_amdgcn_readfirstlane(g.x);
    int d = (int)(tsel - t0) + 200;
    if (d < 0) d = 0;
    if (d > 10000) d = 10000;
    const u32 ta = (u32)__builtin_amdgcn_s_memrealtime();
    while ((u32)__builtin_amdgcn_s_memrealtime() - ta < (u32)d * 16u) __builtin_amdgcn_s_sleep(2);
  }
}

extern "C" void kernel_launch(void* const* d_in, const int* in_sizes, int n_in,
                              void* d_out, int out_size, void* d_ws, size_t ws_size,
                              hipStream_t stream) {
  if (ws_size < WS_BYTES) return;
  fused_decode_kernel<<<256, 512, 0, stream>>>(
      (const u16*)d_in[0], (const u16*)d_in[1], (const u16*)d_in[2], (const u16*)d_in[3],
      (const u16*)d_in[4], (const u16*)d_in[6], (const u16*)d_in[7], (u16*)d_out, (u32*)d_ws);
}
```

```cpp
#include <hip/hip_runtime.h>
#include <hip/hip_bf16.h>
#include <stdint.h>

#define KE 4
#define PACE 8
#define VW 8
#define BAR0 0
#define QFLAG 1
#define NRED 160
#define CODE_KB 0

typedef unsigned int u32;
typedef unsigned short u16;
typedef unsigned long long u64;
using u32x2 = __attribute__((ext_vector_type(2))) unsigned;
using u32x4 = __attribute__((ext_vector_type(4))) unsigned;
using f32x2 = __attribute__((ext_vector_type(2))) float;
using f32x4 = __attribute__((ext_vector_type(4))) float;
using bf16x2 = __attribute__((ext_vector_type(2))) __bf16;

constexpr int D = 640, NH = 32, DH = 192, SK = 32768, WLEN = 4096;
constexpr int CPR = D / NRED;
constexpr int UPP = CPR / 2;
constexpr u32 WS_Q = 4096;
constexpr u32 WS_O = WS_Q + 256u * 2048u;
constexpr u32 WS_Y = WS_O + 256u * 2048u;
constexpr u32 WS_BYTES = WS_Y + 320u * 256u * 16u;
constexpr int SPIN_CAP = 200000;
constexpr float LOG2E = 1.4426950408889634f;

__device__ __forceinline__ float dot2(u32 a, u32 b, float acc) {
  return __builtin_amdgcn_fdot2_f32_bf16(__builtin_bit_cast(bf16x2, a), __builtin_bit_cast(bf16x2, b), acc, false);
}
template <int CTRL> __device__ __forceinline__ float dpp(float x) {
  return __builtin_bit_cast(float, __builtin_amdgcn_mov_dpp(__builtin_bit_cast(int, x), CTRL, 0xf, 0xf, true));
}
constexpr int XOR1 = 0xB1, XOR2 = 0x4E, XOR7 = 0x141, ROR8 = 0x128, ROR4 = 0x124;
__device__ __forceinline__ float sum8(float x) {
  x += dpp<XOR1>(x); x += dpp<XOR2>(x); x += dpp<XOR7>(x); return x;
}
__device__ __forceinline__ float max8(float x) {
  x = fmaxf(x, dpp<XOR1>(x)); x = fmaxf(x, dpp<XOR2>(x)); x = fmaxf(x, dpp<XOR7>(x)); return x;
}
__device__ __forceinline__ float xrow_sum(float x) {
  auto s = __builtin_amdgcn_permlane16_swap(__float_as_uint(x), __float_as_uint(x), false, false);
  x = __uint_as_float(s[0]) + __uint_as_float(s[1]);
  auto t = __builtin_amdgcn_permlane32_swap(__float_as_uint(x), __float_as_uint(x), false, false);
  return __uint_as_float(t[0]) + __uint_as_float(t[1]);
}
__device__ __forceinline__ float xrow_max(float x) {
  auto s = __builtin_amdgcn_permlane16_swap(__float_as_uint(x), __float_as_uint(x), false, false);
  x = fmaxf(__uint_as_float(s[0]), __uint_as_float(s[1]));
  auto t = __builtin_amdgcn_permlane32_swap(__float_as_uint(x), __float_as_uint(x), false, false);
  return fmaxf(__uint_as_float(t[0]), __uint_as_float(t[1]));
}
__device__ __forceinline__ float hi_sum(float x) { x += dpp<ROR8>(x); return xrow_sum(x); }
__device__ __forceinline__ float hi_max(float x) { x = fmaxf(x, dpp<ROR8>(x)); return xrow_max(x); }
__device__ __forceinline__ float wave_sum(float x) { return hi_sum(sum8(x)); }
__device__ __forceinline__ float wave_max(float x) { return hi_max(max8(x)); }
__device__ __forceinline__ u32 bfbits(float f) { return (u32)__builtin_bit_cast(u16, (__bf16)f); }
__device__ __forceinline__ float bfr(float f) { return __uint_as_float(bfbits(f) << 16); }
__device__ __forceinline__ u32 pack2(float a, float b) {
  bf16x2 p = {(__bf16)a, (__bf16)b};
  return __builtin_bit_cast(u32, p);
}
__device__ __forceinline__ float blo(u32 v) { return __uint_as_float(v << 16); }
__device__ __forceinline__ float bhi(u32 v) { return __uint_as_float(v & 0xffff0000u); }
#define BAR() asm volatile("s_waitcnt lgkmcnt(0)\n\ts_barrier" ::: "memory")
#define PIN() __builtin_amdgcn_sched_barrier(0)
#define STAMP(i)
#define KEEP(v) asm volatile("" : "+v"(v))
#define RSRC(p, n) __builtin_amdgcn_make_buffer_rsrc((void*)(p), 0, (n), 0x00020000)
#define WAITVM(n) __builtin_amdgcn_s_waitcnt(0x0F70 | ((n) & 15) | (((n) >> 4) << 14))

constexpr int PART_W = 1568;
constexpr int L_PART = 0;
constexpr int L_SC = L_PART + 8 * PART_W;
constexpr int L_PT = L_SC + 8 * 192;
constexpr int L_QS = L_PT + 8 * 64;
constexpr int L_ML = L_QS + 128;
constexpr int L_OW = L_ML + 16;
constexpr int L_RED = L_OW + 128;
constexpr int L_YP = L_RED + 128;
constexpr int L_END = L_YP + 8 * 480;

__global__ void __launch_bounds__(512, 2)
fused_decode_kernel(const u16* __restrict__ x, const u16* __restrict__ kc, const u16* __restrict__ vc,
                    const u16* __restrict__ wq, const u16* __restrict__ wo, const u16* __restrict__ w1,
                    const u16* __restrict__ w2, u16* __restrict__ out, u32* __restrict__ ws) {
  __shared__ __attribute__((aligned(16))) float lds[L_END];
  const int tid = threadIdx.x;
  const int lane = tid & 63;
  const int w = __builtin_amdgcn_readfirstlane(tid >> 6);
  const int b = blockIdx.x;
  const int h = (b & 7) * 4 + (b >> 6);
  const int s = (b >> 3) & 7;
  const int j = h * 8 + s;

  const auto rs_ws = RSRC(ws, WS_BYTES);
  const auto rs_x = RSRC(x, D * 2);
  const u32x4 xa = __builtin_amdgcn_raw_buffer_load_b128(rs_x, lane * 20, 0, 0);
  const u32 xb = __builtin_amdgcn_raw_buffer_load_b32(rs_x, lane * 20 + 16, 0, 0);
  const int row0 = 80 * s + 10 * w;
  const bool isq = lane < 48;
  const int l1 = lane < 60 ? lane - 48 : 11;
  const int r2 = isq ? (lane >= 24) : (l1 >= 6);
  const int cch = isq ? lane - 24 * r2 : l1 - 6 * r2;
  u32x4 wv[5];
  {
    const char* p = isq ? (const char*)wq + (size_t)(row0 + r2) * 12288 + h * 384 + cch * 16
                        : (const char*)w1 + (size_t)(row0 + r2) * 3072 + h * 96 + cch * 16;
    const u32 stp = isq ? 24576u : 6144u;
#pragma unroll
    for (int i = 0; i < 5; ++i) {
      wv[i] = __builtin_nontemporal_load((const u32x4*)p);
      p += stp;
      PIN();
    }
  }
  const u32 E = __builtin_amdgcn_raw_buffer_load_b32(rs_ws, 0, 0, 16);
  const size_t kvoff = ((size_t)h * SK + (SK - WLEN) + s * 512 + w * 64) * DH;
  const auto rs_k = RSRC(kc + kvoff, 64 * DH * 2);
  const auto rs_v = RSRC(vc + kvoff, 64 * DH * 2);
  u32 cpf = 0;
  if (CODE_KB > 0 && w == 7) {
    const u64 pc = __builtin_amdgcn_s_getpc() & ~(u64)255;
    cpf = __builtin_amdgcn_raw_buffer_load_b32(RSRC(pc, CODE_KB * 1024), lane * 64, ((b >> 3) % ((CODE_KB + 3) / 4)) * 4096, 0);
  }
  PIN();
  u32x4 kv[24 + VW];
#pragma unroll
  for (int i = 0; i < KE; ++i) {
    kv[i] = __builtin_amdgcn_raw_buffer_load_b128(rs_k, lane * 16, i * 1024, 2);
    PIN();
  }
  u32 xr[5];
#pragma unroll
  for (int i = 0; i < 5; ++i) xr[i] = reinterpret_cast<const u32*>(x)[row0 / 2 + i];

  {
    float xv[10];
    xv[0] = blo(xa.x); xv[1] = bhi(xa.x); xv[2] = blo(xa.y); xv[3] = bhi(xa.y);
    xv[4] = blo(xa.z); xv[5] = bhi(xa.z); xv[6] = blo(xa.w); xv[7] = bhi(xa.w);
    xv[8] = blo(xb); xv[9] = bhi(xb);
    float sm = ((xv[0] + xv[1]) + (xv[2] + xv[3])) + ((xv[4] + xv[5]) + (xv[6] + xv[7])) + (xv[8] + xv[9]);
    const float mean = wave_sum(sm) * (1.0f / D);
    float sq = 0.f;
#pragma unroll
    for (int e = 0; e < 10; ++e) { const float d = xv[e] - mean; sq += d * d; }
    const float var = wave_sum(sq) * (1.0f / D);
    const float mb = bfr(mean);
    const float den = bfr(__builtin_amdgcn_sqrtf(bfr(bfr(var) + 1e-5f)));
    const float rinv = 1.0f / den;
    STAMP(1);
#pragma unroll
    for (int i = 0; i < 5; ++i) asm volatile("" : "+s"(xr[i]));
    float acc[8];
#pragma unroll
    for (int e = 0; e < 8; ++e) acc[e] = 0.f;
#pragma unroll
    for (int i = 0; i < 5; ++i) {
      const float v = r2 ? bhi(xr[i]) : blo(xr[i]);
      const u32 xl = bfbits(bfr(v - mb) * rinv), xh = xl << 16;
#pragma unroll
      for (int d = 0; d < 4; ++d) {
        acc[2 * d] = dot2(wv[i][d], xl, acc[2 * d]);
        acc[2 * d + 1] = dot2(wv[i][d], xh, acc[2 * d + 1]);
      }
    }
    if (lane < 60) {
      float* p = lds + L_PART + (2 * w + r2) * 244 + (isq ? 0 : 192) + cch * 8;
      *reinterpret_cast<f32x4*>(p) = f32x4{acc[0], acc[1], acc[2], acc[3]};
      *reinterpret_cast<f32x4*>(p + 4) = f32x4{acc[4], acc[5], acc[6], acc[7]};
    }
  }
  const u32 tag = E + 1u;
  volatile u32* qflag = reinterpret_cast<volatile u32*>(lds + L_QS + 124);
  if (QFLAG && tid == 64) *qflag = 0u;
  KEEP(cpf);
  STAMP(2);
  BAR();

  if (w == 0) {
    __builtin_amdgcn_s_setprio(3);
    const int t = lane < 60 ? lane : 59;
    const float* p = lds + L_PART + t * 4;
    f32x4 a = *reinterpret_cast<const f32x4*>(p);
#pragma unroll
    for (int r = 1; r < 16; ++r) a += *reinterpret_cast<const f32x4*>(p + r * 244);
    if (lane < 60) {
      const u32x4 u0 = {__float_as_uint(a.x), tag, __float_as_uint(a.y), tag};
      const u32x4 u1 = {__float_as_uint(a.z), tag, __float_as_uint(a.w), tag};
      __builtin_amdgcn_raw_buffer_store_b128(u0, rs_ws, lane * 32, WS_Q + j * 2048, 16);
      __builtin_amdgcn_raw_buffer_store_b128(u1, rs_ws, lane * 32 + 16, WS_Q + j * 2048, 16);
    }
    STAMP(3);
    f32x4 sum;
    int spins = 0;
    bool ok;
    do {
      asm volatile("" ::: "memory");
      u32x4 un[16];
#pragma unroll
      for (int k = 0; k < 16; ++k)
        un[k] = __builtin_amdgcn_raw_buffer_load_b128(rs_ws, t * 16 + (k & 1) * 960 + (k >> 1) * 2048, WS_Q + h * 16384, 16);
      u32 mn = tag, mx = tag;
      sum = f32x4{0.f, 0.f, 0.f, 0.f};
#pragma unroll
      for (int k = 0; k < 16; ++k) {
        mn = min(mn, min(un[k].y, un[k].w));
        mx = max(mx, max(un[k].y, un[k].w));
        sum[2 * (k & 1)] += __uint_as_float(un[k].x);
        sum[2 * (k & 1) + 1] += __uint_as_float(un[k].z);
      }
      ok = __all(mn == mx);
    } while (!ok && ++spins < SPIN_CAP);
    if (t >= 36) { sum.z = fmaxf(sum.z, 0.f); sum.w = fmaxf(sum.w, 0.f); }
    u32* qs = reinterpret_cast<u32*>(lds + L_QS);
    qs[t] = pack2(sum.x, sum.y);
    qs[60 + t] = pack2(sum.z, sum.w);
    STAMP(4);
    asm volatile("s_waitcnt lgkmcnt(0)" ::: "memory");
    *qflag = 1u;
  }
#pragma unroll
  for (int i = KE; i < 24 + VW; ++i) {
    if (w != 0) WAITVM(PACE - 1);
    if (i < 24) kv[i] = __builtin_amdgcn_raw_buffer_load_b128(rs_k, lane * 16, i * 1024, 2);
    else kv[i] = __builtin_amdgcn_raw_buffer_load_b128(rs_v, lane * 16, (i - 24) * 1024, 2);
    PIN();
  }
  int rho[3], c8[3];
#pragma unroll
  for (int u = 0; u < 3; ++u) { const int g = 64 * u + lane; rho[u] = g / 24; c8[u] = g % 24; }
  const int r6 = (lane / 10) > 5 ? 5 : (lane / 10);
  const int ch = lane - 10 * (lane / 10);
  const auto rs_wo = RSRC(wo + (size_t)(h * DH + 24 * s) * D + 80 * w, 23 * 1280 + 160);
  const auto rs_w2 = RSRC(w2 + (size_t)(48 * h + 6 * s) * D + 80 * w, 5 * 1280 + 160);
  const int wo_voff = r6 * 1280 + ch * 16;
  if (w != 0) {
    STAMP(3);
    int spins = 0;
    while (*qflag == 0u && ++spins < 100 * SPIN_CAP) __builtin_amdgcn_s_sleep(1);
    asm volatile("" ::: "memory");
    STAMP(4);
  }
  u32x4 q[3];
#pragma unroll
  for (int u = 0; u < 3; ++u) q[u] = *reinterpret_cast<const u32x4*>(lds + L_QS + c8[u] * 4);

  u32x4 wov[5];
  float sv[3] = {0.f, 0.f, 0.f};
#pragma unroll
  for (int i = 0; i < 24; ++i) {
    const u32x4 d = kv[i];
    const int u = i % 3;
    float a = dot2(d.x, q[u].x, 0.f);
    float c = dot2(d.z, q[u].z, 0.f);
    a = dot2(d.y, q[u].y, a);
    c = dot2(d.w, q[u].w, c);
    const int n = i + VW;
    if (n < 24) kv[i] = __builtin_amdgcn_raw_buffer_load_b128(rs_v, lane * 16, n * 1024, 2);
    else if (n < 28) wov[n - 24] = __builtin_amdgcn_raw_buffer_load_b128(rs_wo, wo_voff, (n - 24) * 7680, 2);
    else if (n == 28) wov[4] = __builtin_amdgcn_raw_buffer_load_b128(rs_w2, wo_voff, 0, 2);
    a = sum8(a + c);
    sv[i / 8] = ((lane & 7) == (i & 7)) ? a : sv[i / 8];
    PIN();
  }
  STAMP(5);
  {
    float* sc = lds + L_SC + w * 192;
#pragma unroll
    for (int a = 0; a < 3; ++a) sc[(8 * a + (lane & 7)) * 8 + (lane >> 3)] = sv[a];
    const float raw = (sc[3 * lane] + sc[3 * lane + 1]) + sc[3 * lane + 2];
    const float sco = bfr(bfr(raw) * 0.07216878364870322f);
    const float mw = wave_max(sco);
    const float p = __builtin_amdgcn_exp2f((sco - mw) * LOG2E);
    lds[L_PT + w * 64 + (lane & 7) * 8 + (lane >> 3)] = p;
    const float lw = wave_sum(p);
    if (lane == 0) *reinterpret_cast<f32x2*>(lds + L_ML + 2 * w) = f32x2{mw, lw};
  }
  PIN();

  float acc[3][8];
#pragma unroll
  for (int u = 0; u < 3; ++u)
#pragma unroll
    for (int e = 0; e < 8; ++e) acc[u][e] = 0.f;
  f32x4 pv[3];
#pragma unroll
  for (int i = 0; i < 24; ++i) {
    const int u = i % 3, t = i / 3;
    if (i % 12 == 0) {
#pragma unroll
      for (int uu = 0; uu < 3; ++uu)
        pv[uu] = *reinterpret_cast<const f32x4*>(lds + L_PT + w * 64 + rho[uu] * 8 + (i / 12) * 4);
    }
    const u32x4 d = i < VW ? kv[24 + i] : kv[i - VW];
    const float pp = pv[u][t % 4];
#pragma unroll
    for (int c = 0; c < 4; ++c) {
      f32x2 a2 = {acc[u][2 * c], acc[u][2 * c + 1]};
      const f32x2 v2 = {blo(d[c]), bhi(d[c])};
      a2 = __builtin_elementwise_fma(v2, f32x2{pp, pp}, a2);
      KEEP(a2);
      acc[u][2 * c] = a2.x; acc[u][2 * c + 1] = a2.y;
    }
    PIN();
  }
  STAMP(6);
  {
    float* pw = lds + L_PART + w * PART_W + lane * 8;
#pragma unroll
    for (int u = 0; u < 3; ++u) {
      *reinterpret_cast<f32x4*>(pw + u * 512) = f32x4{acc[u][0], acc[u][1], acc[u][2], acc[u][3]};
      *reinterpret_cast<f32x4*>(pw + u * 512 + 4) = f32x4{acc[u][4], acc[u][5], acc[u][6], acc[u][7]};
    }
  }
  BAR();
  if (w < 7) {
    const int col4 = tid >> 3, rg = tid & 7;
    const f32x2 ml = *reinterpret_cast<const f32x2*>(lds + L_ML + 2 * rg);
    const float M = max8(ml.x);
    const float f = __builtin_amdgcn_exp2f((ml.x - M) * LOG2E);
    if (w < 6) {
      const float* p = lds + L_PART + rg * PART_W + col4 * 4;
      f32x4 a = *reinterpret_cast<const f32x4*>(p);
#pragma unroll
      for (int r = 1; r < 8; ++r) a += *reinterpret_cast<const f32x4*>(p + r * 192);
      a *= f;
      a.x = sum8(a.x); a.y = sum8(a.y); a.z = sum8(a.z); a.w = sum8(a.w);
      if (rg < 2) {
        const int cs = col4 / 6, k = (col4 - 6 * cs) * 2 + rg;
        const u32x4 un = {__float_as_uint(rg ? a.z : a.x), tag, __float_as_uint(rg ? a.w : a.y), tag};
        __builtin_amdgcn_raw_buffer_store_b128(un, rs_ws, cs * 2048 + k * 16, WS_O + h * 16384 + s * 256, 16);
      }
    } else {
      const float L = sum8(f * ml.y);
      if (lane < 8) {
        const u32x4 un = {__float_as_uint(M), tag, __float_as_uint(L), tag};
        __builtin_amdgcn_raw_buffer_store_b128(un, rs_ws, lane * 2048 + 192, WS_O + h * 16384 + s * 256, 16);
      }
    }
  }
  STAMP(7);
  float y[8];
  {
    const u32 hid = reinterpret_cast<const u16*>(lds + L_QS)[192 + 6 * s + r6];
#pragma unroll
    for (int d = 0; d < 4; ++d) {
      y[2 * d] = dot2(wov[4][d], hid, 0.f);
      y[2 * d + 1] = dot2(wov[4][d], hid << 16, 0.f);
    }
    const int kk = lane & 7;
    const int ob = (lane >> 3) * 256;
    u32x4 u0, u1, u2;
    int spins = 0;
    bool ok;
    do {
      asm volatile("" ::: "memory");
      u0 = __builtin_amdgcn_raw_buffer_load_b128(rs_ws, ob + kk * 16, WS_O + j * 2048, 16);
      u1 = __builtin_amdgcn_raw_buffer_load_b128(rs_ws, ob + 128 + (kk & 3) * 16, WS_O + j * 2048, 16);
      u2 = __builtin_amdgcn_raw_buffer_load_b128(rs_ws, ob + 192, WS_O + j * 2048, 16);
      const u32 mn = min(min(min(u0.y, u0.w), min(u1.y, u1.w)), min(min(u2.y, u2.w), tag));
      const u32 mx = max(max(max(u0.y, u0.w), max(u1.y, u1.w)), max(max(u2.y, u2.w), tag));
      ok = __all(mn == mx);
    } while (!ok && ++spins < SPIN_CAP);
    STAMP(8);
    const float mp = __uint_as_float(u2.x);
    const float M = hi_max(mp);
    float f = __builtin_amdgcn_exp2f((mp - M) * LOG2E);
    const float L = hi_sum(f * __uint_as_float(u2.z));
    f *= __builtin_amdgcn_rcpf(L);
    const float o0 = hi_sum(f * __uint_as_float(u0.x)), o1 = hi_sum(f * __uint_as_float(u0.z));
    const float o2 = hi_sum(f * __uint_as_float(u1.x)), o3 = hi_sum(f * __uint_as_float(u1.z));
    u32* ow = reinterpret_cast<u32*>(lds + L_OW + w * 16);
    if (lane < 12) ow[lane] = lane < 8 ? pack2(o0, o1) : pack2(o2, o3);
    const u16* oh = reinterpret_cast<const u16*>(ow);
#pragma unroll
    for (int i = 0; i < 4; ++i) {
      const u32 ov = oh[6 * i + r6];
#pragma unroll
      for (int d = 0; d < 4; ++d) {
        y[2 * d] = dot2(wov[i][d], ov, y[2 * d]);
        y[2 * d + 1] = dot2(wov[i][d], ov << 16, y[2 * d + 1]);
      }
    }
  }
  {
    float* yp = lds + L_YP + w * 480;
    if (lane < 60) {
      *reinterpret_cast<f32x4*>(yp + lane * 8) = f32x4{y[0], y[1], y[2], y[3]};
      *reinterpret_cast<f32x4*>(yp + lane * 8 + 4) = f32x4{y[4], y[5], y[6], y[7]};
    }
    if (lane < 40) {
      f32x2 a = *reinterpret_cast<const f32x2*>(yp + lane * 2);
#pragma unroll
      for (int r = 1; r < 6; ++r) a += *reinterpret_cast<const f32x2*>(yp + r * 80 + lane * 2);
      const int n2 = 40 * w + lane;
      const int r = n2 / UPP, ui = n2 % UPP;
      const u32x4 un = {__float_as_uint(a.x), tag, __float_as_uint(a.y), tag};
      __builtin_amdgcn_raw_buffer_store_b128(un, rs_ws, (r * 256 * UPP + ui) * 16, WS_Y + j * UPP * 16, 16);
    }
  }
  STAMP(9);
  if (b < NRED) {
    constexpr int NU = 256 * UPP / 512;
    const u32 xo = x[b * CPR + (tid < CPR ? tid : 0)];
    float v0, v1;
    int spins = 0;
    bool ok;
    do {
      asm volatile("" ::: "memory");
      u32x4 un[NU];
#pragma unroll
      for (int k = 0; k < NU; ++k)
        un[k] = __builtin_amdgcn_raw_buffer_load_b128(rs_ws, tid * 16 + k * 8192, WS_Y + b * 256 * UPP * 16, 16);
      u32 mn = tag, mx = tag;
      v0 = 0.f; v1 = 0.f;
#pragma unroll
      for (int k = 0; k < NU; ++k) {
        mn = min(mn, min(un[k].y, un[k].w));
        mx = max(mx, max(un[k].y, un[k].w));
        v0 += __uint_as_float(un[k].x); v1 += __uint_as_float(un[k].z);
      }
      ok = __all(mn == mx);
    } while (!ok && ++spins < SPIN_CAP);
    STAMP(10);
    if (UPP <= 1) { v0 += dpp<XOR1>(v0); v1 += dpp<XOR1>(v1); }
    if (UPP <= 2) { v0 += dpp<XOR2>(v0); v1 += dpp<XOR2>(v1); }
    if (UPP <= 4) { v0 += dpp<ROR4>(v0); v1 += dpp<ROR4>(v1); }
    v0 = hi_sum(v0); v1 = hi_sum(v1);
    if (lane < UPP) *reinterpret_cast<f32x2*>(lds + L_RED + w * 16 + lane * 2) = f32x2{v0, v1};
    BAR();
    if (tid < CPR) {
      float a = 0.f;
#pragma unroll
      for (int k = 0; k < 8; ++k) a += lds[L_RED + k * 16 + tid];
      a += blo(xo);
      out[b * CPR + tid] = (u16)bfbits(a);
    }
    if (b == 0 && tid == 0) __builtin_amdgcn_raw_buffer_store_b32(tag, rs_ws, 0, 0, 16);
  }
}

__global__ void pre_kernel(u32* ws) { if (threadIdx.x == 0) ws[32] = (u32)__builtin_amdgcn_s_memrealtime(); }
extern "C" void kernel_launch(void* const* d_in, const int* in_sizes, int n_in,
                              void* d_out, int out_size, void* d_ws, size_t ws_size,
                              hipStream_t stream) {
  if (ws_size < WS_BYTES) return;
  pre_kernel<<<1, 64, 0, stream>>>((u32*)d_ws);
  fused_decode_kernel<<<256, 512, 0, stream>>>(
      (const u16*)d_in[0], (const u16*)d_in[1], (const u16*)d_in[2], (const u16*)d_in[3],
      (const u16*)d_in[4], (const u16*)d_in[6], (const u16*)d_in[7], (u16*)d_out, (u32*)d_ws);
}
```

```cpp
#include <hip/hip_runtime.h>
#include <hip/hip_bf16.h>
#include <stdint.h>

#define KE 4
#define PACE 8
#define VW 8
#define BAR0 0
#define QFLAG 1
#define NRED 160
#define CODE_KB 0

typedef unsigned int u32;
typedef unsigned short u16;
typedef unsigned long long u64;
using u32x2 = __attribute__((ext_vector_type(2))) unsigned;
using u32x4 = __attribute__((ext_vector_type(4))) unsigned;
using f32x2 = __attribute__((ext_vector_type(2))) float;
using f32x4 = __attribute__((ext_vector_type(4))) float;
using bf16x2 = __attribute__((ext_vector_type(2))) __bf16;

constexpr int D = 640, NH = 32, DH = 192, SK = 32768, WLEN = 4096;
constexpr int CPR = D / NRED;
constexpr int UPP = CPR / 2;
constexpr u32 WS_Q = 16384;
constexpr u32 WS_O = WS_Q + 256u * 2048u;
constexpr u32 WS_Y = WS_O + 256u * 2048u;
constexpr u32 WS_BYTES = WS_Y + 320u * 256u * 16u;
constexpr int SPIN_CAP = 200000;
constexpr float LOG2E = 1.4426950408889634f;

__device__ __forceinline__ float dot2(u32 a, u32 b, float acc) {
  return __builtin_amdgcn_fdot2_f32_bf16(__builtin_bit_cast(bf16x2, a), __builtin_bit_cast(bf16x2, b), acc, false);
}
template <int CTRL> __device__ __forceinline__ float dpp(float x) {
  return __builtin_bit_cast(float, __builtin_amdgcn_mov_dpp(__builtin_bit_cast(int, x), CTRL, 0xf, 0xf, true));
}
constexpr int XOR1 = 0xB1, XOR2 = 0x4E, XOR7 = 0x141, ROR8 = 0x128, ROR4 = 0x124;
__device__ __forceinline__ float sum8(float x) {
  x += dpp<XOR1>(x); x += dpp<XOR2>(x); x += dpp<XOR7>(x); return x;
}
__device__ __forceinline__ float max8(float x) {
  x = fmaxf(x, dpp<XOR1>(x)); x = fmaxf(x, dpp<XOR2>(x)); x = fmaxf(x, dpp<XOR7>(x)); return x;
}
__device__ __forceinline__ float xrow_sum(float x) {
  auto s = __builtin_amdgcn_permlane16_swap(__float_as_uint(x), __float_as_uint(x), false, false);
  x = __uint_as_float(s[0]) + __uint_as_float(s[1]);
  auto t = __builtin_amdgcn_permlane32_swap(__float_as_uint(x), __float_as_uint(x), false, false);
  return __uint_as_float(t[0]) + __uint_as_float(t[1]);
}
__device__ __forceinline__ float xrow_max(float x) {
  auto s = __builtin_amdgcn_permlane16_swap(__float_as_uint(x), __float_as_uint(x), false, false);
  x = fmaxf(__uint_as_float(s[0]), __uint_as_float(s[1]));
  auto t = __builtin_amdgcn_permlane32_swap(__float_as_uint(x), __float_as_uint(x), false, false);
  return fmaxf(__uint_as_float(t[0]), __uint_as_float(t[1]));
}
__device__ __forceinline__ float hi_sum(float x) { x += dpp<ROR8>(x); return xrow_sum(x); }
__device__ __forceinline__ float hi_max(float x) { x = fmaxf(x, dpp<ROR8>(x)); return xrow_max(x); }
__device__ __forceinline__ float wave_sum(float x) { return hi_sum(sum8(x)); }
__device__ __forceinline__ float wave_max(float x) { return hi_max(max8(x)); }
__device__ __forceinline__ u32 bfbits(float f) { return (u32)__builtin_bit_cast(u16, (__bf16)f); }
__device__ __forceinline__ float bfr(float f) { return __uint_as_float(bfbits(f) << 16); }
__device__ __forceinline__ u32 pack2(float a, float b) {
  bf16x2 p = {(__bf16)a, (__bf16)b};
  return __builtin_bit_cast(u32, p);
}
__device__ __forceinline__ float blo(u32 v) { return __uint_as_float(v << 16); }
__device__ __forceinline__ float bhi(u32 v) { return __uint_as_float(v & 0xffff0000u); }
#define BAR() asm volatile("s_waitcnt lgkmcnt(0)\n\ts_barrier" ::: "memory")
#define PIN() __builtin_amdgcn_sched_barrier(0)
#define STAMP(i)
#define KEEP(v) asm volatile("" : "+v"(v))
#define RSRC(p, n) __builtin_amdgcn_make_buffer_rsrc((void*)(p), 0, (n), 0x00020000)
#define WAITVM(n) __builtin_amdgcn_s_waitcnt(0x0F70 | ((n) & 15) | (((n) >> 4) << 14))

constexpr int PART_W = 1568;
constexpr int L_PART = 0;
constexpr int L_SC = L_PART + 8 * PART_W;
constexpr int L_PT = L_SC + 8 * 192;
constexpr int L_QS = L_PT + 8 * 64;
constexpr int L_ML = L_QS + 128;
constexpr int L_OW = L_ML + 16;
constexpr int L_RED = L_OW + 128;
constexpr int L_YP = L_RED + 128;
constexpr int L_END = L_YP + 8 * 480;

__global__ void __launch_bounds__(512, 2)
fused_decode_kernel(const u16* __restrict__ x, const u16* __restrict__ kc, const u16* __restrict__ vc,
                    const u16* __restrict__ wq, const u16* __restrict__ wo, const u16* __restrict__ w1,
                    const u16* __restrict__ w2, u16* __restrict__ out, u32* __restrict__ ws) {
  __shared__ __attribute__((aligned(16))) float lds[L_END];
  const int tid = threadIdx.x;
  const int lane = tid & 63;
  const int w = __builtin_amdgcn_readfirstlane(tid >> 6);
  const int b = blockIdx.x;
  const int h = (b & 7) * 4 + (b >> 6);
  const int s = (b >> 3) & 7;
  const int j = h * 8 + s;

  const auto rs_ws = RSRC(ws, WS_BYTES);
  const auto rs_x = RSRC(x, D * 2);
  const u32x4 xa = __builtin_amdgcn_raw_buffer_load_b128(rs_x, lane * 20, 0, 0);
  const u32 xb = __builtin_amdgcn_raw_buffer_load_b32(rs_x, lane * 20 + 16, 0, 0);
  const int row0 = 80 * s + 10 * w;
  const bool isq = lane < 48;
  const int l1 = lane < 60 ? lane - 48 : 11;
  const int r2 = isq ? (lane >= 24) : (l1 >= 6);
  const int cch = isq ? lane - 24 * r2 : l1 - 6 * r2;
  u32x4 wv[5];
  {
    const char* p = isq ? (const char*)wq + (size_t)(row0 + r2) * 12288 + h * 384 + cch * 16
                        : (const char*)w1 + (size_t)(row0 + r2) * 3072 + h * 96 + cch * 16;
    const u32 stp = isq ? 24576u : 6144u;
#pragma unroll
    for (int i = 0; i < 5; ++i) {
      wv[i] = __builtin_nontemporal_load((const u32x4*)p);
      p += stp;
      PIN();
    }
  }
  const u32 E = __builtin_amdgcn_raw_buffer_load_b32(rs_ws, 0, 0, 16);
  const size_t kvoff = ((size_t)h * SK + (SK - WLEN) + s * 512 + w * 64) * DH;
  const auto rs_k = RSRC(kc + kvoff, 64 * DH * 2);
  const auto rs_v = RSRC(vc + kvoff, 64 * DH * 2);
  u32 cpf = 0;
  if (CODE_KB > 0 && w == 7) {
    const u64 pc = __builtin_amdgcn_s_getpc() & ~(u64)255;
    cpf = __builtin_amdgcn_raw_buffer_load_b32(RSRC(pc, CODE_KB * 1024), lane * 64, ((b >> 3) % ((CODE_KB + 3) / 4)) * 4096, 0);
  }
  PIN();
  u32x4 kv[24 + VW];
#pragma unroll
  for (int i = 0; i < KE; ++i) {
    kv[i] = __builtin_amdgcn_raw_buffer_load_b128(rs_k, lane * 16, i * 1024, 2);
    PIN();
  }
  u32 xr[5];
#pragma unroll
  for (int i = 0; i < 5; ++i) xr[i] = reinterpret_cast<const u32*>(x)[row0 / 2 + i];

  {
    float xv[10];
    xv[0] = blo(xa.x); xv[1] = bhi(xa.x); xv[2] = blo(xa.y); xv[3] = bhi(xa.y);
    xv[4] = blo(xa.z); xv[5] = bhi(xa.z); xv[6] = blo(xa.w); xv[7] = bhi(xa.w);
    xv[8] = blo(xb); xv[9] = bhi(xb);
    float sm = ((xv[0] + xv[1]) + (xv[2] + xv[3])) + ((xv[4] + xv[5]) + (xv[6] + xv[7])) + (xv[8] + xv[9]);
    const float mean = wave_sum(sm) * (1.0f / D);
    float sq = 0.f;
#pragma unroll
    for (int e = 0; e < 10; ++e) { const float d = xv[e] - mean; sq += d * d; }
    const float var = wave_sum(sq) * (1.0f / D);
    const float mb = bfr(mean);
    const float den = bfr(__builtin_amdgcn_sqrtf(bfr(bfr(var) + 1e-5f)));
    const float rinv = 1.0f / den;
    STAMP(1);
#pragma unroll
    for (int i = 0; i < 5; ++i) asm volatile("" : "+s"(xr[i]));
    float acc[8];
#pragma unroll
    for (int e = 0; e < 8; ++e) acc[e] = 0.f;
#pragma unroll
    for (int i = 0; i < 5; ++i) {
      const float v = r2 ? bhi(xr[i]) : blo(xr[i]);
      const u32 xl = bfbits(bfr(v - mb) * rinv), xh = xl << 16;
#pragma unroll
      for (int d = 0; d < 4; ++d) {
        acc[2 * d] = dot2(wv[i][d], xl, acc[2 * d]);
        acc[2 * d + 1] = dot2(wv[i][d], xh, acc[2 * d + 1]);
      }
    }
    if (lane < 60) {
      float* p = lds + L_PART + (2 * w + r2) * 244 + (isq ? 0 : 192) + cch * 8;
      *reinterpret_cast<f32x4*>(p) = f32x4{acc[0], acc[1], acc[2], acc[3]};
      *reinterpret_cast<f32x4*>(p + 4) = f32x4{acc[4], acc[5], acc[6], acc[7]};
    }
  }
  const u32 tag = E + 1u;
  volatile u32* qflag = reinterpret_cast<volatile u32*>(lds + L_QS + 124);
  if (QFLAG && tid == 64) *qflag = 0u;
  KEEP(cpf);
  STAMP(2);
  BAR();

  if (w == 0) {
    __builtin_amdgcn_s_setprio(3);
    const int t = lane < 60 ? lane : 59;
    const float* p = lds + L_PART + t * 4;
    f32x4 a = *reinterpret_cast<const f32x4*>(p);
#pragma unroll
    for (int r = 1; r < 16; ++r) a += *reinterpret_cast<const f32x4*>(p + r * 244);
    if (lane < 60) {
      const u32x4 u0 = {__float_as_uint(a.x), tag, __float_as_uint(a.y), tag};
      const u32x4 u1 = {__float_as_uint(a.z), tag, __float_as_uint(a.w), tag};
      __builtin_amdgcn_raw_buffer_store_b128(u0, rs_ws, lane * 32, WS_Q + j * 2048, 16);
      __builtin_amdgcn_raw_buffer_store_b128(u1, rs_ws, lane * 32 + 16, WS_Q + j * 2048, 16);
    }
    STAMP(3);
    f32x4 sum;
    int spins = 0;
    bool ok;
    do {
      asm volatile("" ::: "memory");
      u32x4 un[16];
#pragma unroll
      for (int k = 0; k < 16; ++k)
        un[k] = __builtin_amdgcn_raw_buffer_load_b128(rs_ws, t * 16 + (k & 1) * 960 + (k >> 1) * 2048, WS_Q + h * 16384, 16);
      u32 mn = tag, mx = tag;
      sum = f32x4{0.f, 0.f, 0.f, 0.f};
#pragma unroll
      for (int k = 0; k < 16; ++k) {
        mn = min(mn, min(un[k].y, un[k].w));
        mx = max(mx, max(un[k].y, un[k].w));
        sum[2 * (k & 1)] += __uint_as_float(un[k].x);
        sum[2 * (k & 1) + 1] += __uint_as_float(un[k].z);
      }
      ok = __all(mn == mx);
    } while (!ok && ++spins < SPIN_CAP);
    if (t >= 36) { sum.z = fmaxf(sum.z, 0.f); sum.w = fmaxf(sum.w, 0.f); }
    u32* qs = reinterpret_cast<u32*>(lds + L_QS);
    qs[t] = pack2(sum.x, sum.y);
    qs[60 + t] = pack2(sum.z, sum.w);
    STAMP(4);
    asm volatile("s_waitcnt lgkmcnt(0)" ::: "memory");
    *qflag = 1u;
  }
#pragma unroll
  for (int i = KE; i < 24 + VW; ++i) {
    if (w != 0) WAITVM(PACE - 1);
    if (i < 24) kv[i] = __builtin_amdgcn_raw_buffer_load_b128(rs_k, lane * 16, i * 1024, 2);
    else kv[i] = __builtin_amdgcn_raw_buffer_load_b128(rs_v, lane * 16, (i - 24) * 1024, 2);
    PIN();
  }
  int rho[3], c8[3];
#pragma unroll
  for (int u = 0; u < 3; ++u) { const int g = 64 * u + lane; rho[u] = g / 24; c8[u] = g % 24; }
  const int r6 = (lane / 10) > 5 ? 5 : (lane / 10);
  const int ch = lane - 10 * (lane / 10);
  const auto rs_wo = RSRC(wo + (size_t)(h * DH + 24 * s) * D + 80 * w, 23 * 1280 + 160);
  const auto rs_w2 = RSRC(w2 + (size_t)(48 * h + 6 * s) * D + 80 * w, 5 * 1280 + 160);
  const int wo_voff = r6 * 1280 + ch * 16;
  if (w != 0) {
    STAMP(3);
    int spins = 0;
    while (*qflag == 0u && ++spins < 100 * SPIN_CAP) __builtin_amdgcn_s_sleep(1);
    asm volatile("" ::: "memory");
    STAMP(4);
  }
  u32x4 q[3];
#pragma unroll
  for (int u = 0; u < 3; ++u) q[u] = *reinterpret_cast<const u32x4*>(lds + L_QS + c8[u] * 4);

  u32x4 wov[5];
  float sv[3] = {0.f, 0.f, 0.f};
#pragma unroll
  for (int i = 0; i < 24; ++i) {
    const u32x4 d = kv[i];
    const int u = i % 3;
    float a = dot2(d.x, q[u].x, 0.f);
    float c = dot2(d.z, q[u].z, 0.f);
    a = dot2(d.y, q[u].y, a);
    c = dot2(d.w, q[u].w, c);
    const int n = i + VW;
    if (n < 24) kv[i] = __builtin_amdgcn_raw_buffer_load_b128(rs_v, lane * 16, n * 1024, 2);
    else if (n < 28) wov[n - 24] = __builtin_amdgcn_raw_buffer_load_b128(rs_wo, wo_voff, (n - 24) * 7680, 2);
    else if (n == 28) wov[4] = __builtin_amdgcn_raw_buffer_load_b128(rs_w2, wo_voff, 0, 2);
    a = sum8(a + c);
    sv[i / 8] = ((lane & 7) == (i & 7)) ? a : sv[i / 8];
    PIN();
  }
  STAMP(5);
  {
    float* sc = lds + L_SC + w * 192;
#pragma unroll
    for (int a = 0; a < 3; ++a) sc[(8 * a + (lane & 7)) * 8 + (lane >> 3)] = sv[a];
    const float raw = (sc[3 * lane] + sc[3 * lane + 1]) + sc[3 * lane + 2];
    const float sco = bfr(bfr(raw) * 0.07216878364870322f);
    const float mw = wave_max(sco);
    const float p = __builtin_amdgcn_exp2f((sco - mw) * LOG2E);
    lds[L_PT + w * 64 + (lane & 7) * 8 + (lane >> 3)] = p;
    const float lw = wave_sum(p);
    if (lane == 0) *reinterpret_cast<f32x2*>(lds + L_ML + 2 * w) = f32x2{mw, lw};
  }
  PIN();

  float acc[3][8];
#pragma unroll
  for (int u = 0; u < 3; ++u)
#pragma unroll
    for (int e = 0; e < 8; ++e) acc[u][e] = 0.f;
  f32x4 pv[3];
#pragma unroll
  for (int i = 0; i < 24; ++i) {
    const int u = i % 3, t = i / 3;
    if (i % 12 == 0) {
#pragma unroll
      for (int uu = 0; uu < 3; ++uu)
        pv[uu] = *reinterpret_cast<const f32x4*>(lds + L_PT + w * 64 + rho[uu] * 8 + (i / 12) * 4);
    }
    const u32x4 d = i < VW ? kv[24 + i] : kv[i - VW];
    const float pp = pv[u][t % 4];
#pragma unroll
    for (int c = 0; c < 4; ++c) {
      f32x2 a2 = {acc[u][2 * c], acc[u][2 * c + 1]};
      const f32x2 v2 = {blo(d[c]), bhi(d[c])};
      a2 = __builtin_elementwise_fma(v2, f32x2{pp, pp}, a2);
      KEEP(a2);
      acc[u][2 * c] = a2.x; acc[u][2 * c + 1] = a2.y;
    }
    PIN();
  }
  STAMP(6);
  {
    float* pw = lds + L_PART + w * PART_W + lane * 8;
#pragma unroll
    for (int u = 0; u < 3; ++u) {
      *reinterpret_cast<f32x4*>(pw + u * 512) = f32x4{acc[u][0], acc[u][1], acc[u][2], acc[u][3]};
      *reinterpret_cast<f32x4*>(pw + u * 512 + 4) = f32x4{acc[u][4], acc[u][5], acc[u][6], acc[u][7]};
    }
  }
  BAR();
  if (w < 7) {
    const int col4 = tid >> 3, rg = tid & 7;
    const f32x2 ml = *reinterpret_cast<const f32x2*>(lds + L_ML + 2 * rg);
    const float M = max8(ml.x);
    const float f = __builtin_amdgcn_exp2f((ml.x - M) * LOG2E);
    if (w < 6) {
      const float* p = lds + L_PART + rg * PART_W + col4 * 4;
      f32x4 a = *reinterpret_cast<const f32x4*>(p);
#pragma unroll
      for (int r = 1; r < 8; ++r) a += *reinterpret_cast<const f32x4*>(p + r * 192);
      a *= f;
      a.x = sum8(a.x); a.y = sum8(a.y); a.z = sum8(a.z); a.w = sum8(a.w);
      if (rg < 2) {
        const int cs = col4 / 6, k = (col4 - 6 * cs) * 2 + rg;
        const u32x4 un = {__float_as_uint(rg ? a.z : a.x), tag, __float_as_uint(rg ? a.w : a.y), tag};
        __builtin_amdgcn_raw_buffer_store_b128(un, rs_ws, cs * 2048 + k * 16, WS_O + h * 16384 + s * 256, 16);
      }
    } else {
      const float L = sum8(f * ml.y);
      if (lane < 8) {
        const u32x4 un = {__float_as_uint(M), tag, __float_as_uint(L), tag};
        __builtin_amdgcn_raw_buffer_store_b128(un, rs_ws, lane * 2048 + 192, WS_O + h * 16384 + s * 256, 16);
      }
    }
  }
  STAMP(7);
  float y[8];
  {
    const u32 hid = reinterpret_cast<const u16*>(lds + L_QS)[192 + 6 * s + r6];
#pragma unroll
    for (int d = 0; d < 4; ++d) {
      y[2 * d] = dot2(wov[4][d], hid, 0.f);
      y[2 * d + 1] = dot2(wov[4][d], hid << 16, 0.f);
    }
    const int kk = lane & 7;
    const int ob = (lane >> 3) * 256;
    u32x4 u0, u1, u2;
    int spins = 0;
    bool ok;
    do {
      asm volatile("" ::: "memory");
      u0 = __builtin_amdgcn_raw_buffer_load_b128(rs_ws, ob + kk * 16, WS_O + j * 2048, 16);
      u1 = __builtin_amdgcn_raw_buffer_load_b128(rs_ws, ob + 128 + (kk & 3) * 16, WS_O + j * 2048, 16);
      u2 = __builtin_amdgcn_raw_buffer_load_b128(rs_ws, ob + 192, WS_O + j * 2048, 16);
      const u32 mn = min(min(min(u0.y, u0.w), min(u1.y, u1.w)), min(min(u2.y, u2.w), tag));
      const u32 mx = max(max(max(u0.y, u0.w), max(u1.y, u1.w)), max(max(u2.y, u2.w), tag));
      ok = __all(mn == mx);
    } while (!ok && ++spins < SPIN_CAP);
    STAMP(8);
    const float mp = __uint_as_float(u2.x);
    const float M = hi_max(mp);
    float f = __builtin_amdgcn_exp2f((mp - M) * LOG2E);
    const float L = hi_sum(f * __uint_as_float(u2.z));
    f *= __builtin_amdgcn_rcpf(L);
    const float o0 = hi_sum(f * __uint_as_float(u0.x)), o1 = hi_sum(f * __uint_as_float(u0.z));
    const float o2 = hi_sum(f * __uint_as_float(u1.x)), o3 = hi_sum(f * __uint_as_float(u1.z));
    u32* ow = reinterpret_cast<u32*>(lds + L_OW + w * 16);
    if (lane < 12) ow[lane] = lane < 8 ? pack2(o0, o1) : pack2(o2, o3);
    const u16* oh = reinterpret_cast<const u16*>(ow);
#pragma unroll
    for (int i = 0; i < 4; ++i) {
      const u32 ov = oh[6 * i + r6];
#pragma unroll
      for (int d = 0; d < 4; ++d) {
        y[2 * d] = dot2(wov[i][d], ov, y[2 * d]);
        y[2 * d + 1] = dot2(wov[i][d], ov << 16, y[2 * d + 1]);
      }
    }
  }
  {
    float* yp = lds + L_YP + w * 480;
    if (lane < 60) {
      *reinterpret_cast<f32x4*>(yp + lane * 8) = f32x4{y[0], y[1], y[2], y[3]};
      *reinterpret_cast<f32x4*>(yp + lane * 8 + 4) = f32x4{y[4], y[5], y[6], y[7]};
    }
    if (lane < 40) {
      f32x2 a = *reinterpret_cast<const f32x2*>(yp + lane * 2);
#pragma unroll
      for (int r = 1; r < 6; ++r) a += *reinterpret_cast<const f32x2*>(yp + r * 80 + lane * 2);
      const int n2 = 40 * w + lane;
      const int r = n2 / UPP, ui = n2 % UPP;
      const u32x4 un = {__float_as_uint(a.x), tag, __float_as_uint(a.y), tag};
      __builtin_amdgcn_raw_buffer_store_b128(un, rs_ws, (r * 256 * UPP + ui) * 16, WS_Y + j * UPP * 16, 16);
    }
  }
  STAMP(9);
  if (b < NRED) {
    constexpr int NU = 256 * UPP / 512;
    const u32 xo = x[b * CPR + (tid < CPR ? tid : 0)];
    float v0, v1;
    int spins = 0;
    bool ok;
    do {
      asm volatile("" ::: "memory");
      u32x4 un[NU];
#pragma unroll
      for (int k = 0; k < NU; ++k)
        un[k] = __builtin_amdgcn_raw_buffer_load_b128(rs_ws, tid * 16 + k * 8192, WS_Y + b * 256 * UPP * 16, 16);
      u32 mn = tag, mx = tag;
      v0 = 0.f; v1 = 0.f;
#pragma unroll
      for (int k = 0; k < NU; ++k) {
        mn = min(mn, min(un[k].y, un[k].w));
        mx = max(mx, max(un[k].y, un[k].w));
        v0 += __uint_as_float(un[k].x); v1 += __uint_as_float(un[k].z);
      }
      ok = __all(mn == mx);
    } while (!ok && ++spins < SPIN_CAP);
    STAMP(10);
    if (UPP <= 1) { v0 += dpp<XOR1>(v0); v1 += dpp<XOR1>(v1); }
    if (UPP <= 2) { v0 += dpp<XOR2>(v0); v1 += dpp<XOR2>(v1); }
    if (UPP <= 4) { v0 += dpp<ROR4>(v0); v1 += dpp<ROR4>(v1); }
    v0 = hi_sum(v0); v1 = hi_sum(v1);
    if (lane < UPP) *reinterpret_cast<f32x2*>(lds + L_RED + w * 16 + lane * 2) = f32x2{v0, v1};
    BAR();
    if (tid < CPR) {
      float a = 0.f;
#pragma unroll
      for (int k = 0; k < 8; ++k) a += lds[L_RED + k * 16 + tid];
      a += blo(xo);
      out[b * CPR + tid] = (u16)bfbits(a);
    }
    if (b == 0 && tid == 0) __builtin_amdgcn_raw_buffer_store_b32(tag, rs_ws, 0, 0, 16);
  }
  if (lane == 0) ws[64 + b * 8 + w] = (u32)__builtin_amdgcn_s_memrealtime();
}

__global__ void post_kernel(u32* ws) {
  const u32 t = (u32)__builtin_amdgcn_s_memrealtime();
  int m = 1 << 30;
  for (int i = threadIdx.x; i < 2048; i += 64) { int dd = (int)(t - ws[64 + i]); m = dd < m ? dd : m; }
  for (int o = 32; o; o >>= 1) { int v = __shfl_xor(m, o); m = v < m ? v : m; }
  int d = m + 200; if (d < 0) d = 0; if (d > 10000) d = 10000;
  const u32 ta = (u32)__builtin_amdgcn_s_memrealtime();
  while ((u32)__builtin_amdgcn_s_memrealtime() - ta < (u32)d * 0u) __builtin_amdgcn_s_sleep(2);
}
extern "C" void kernel_launch(void* const* d_in, const int* in_sizes, int n_in,
                              void* d_out, int out_size, void* d_ws, size_t ws_size,
                              hipStream_t stream) {
  if (ws_size < WS_BYTES) return;
  fused_decode_kernel<<<256, 512, 0, stream>>>(
      (const u16*)d_in[0], (const u16*)d_in[1], (const u16*)d_in[2], (const u16*)d_in[3],
      (const u16*)d_in[4], (const u16*)d_in[6], (const u16*)d_in[7], (u16*)d_out, (u32*)d_ws);
  post_kernel<<<1, 64, 0, stream>>>((u32*)d_ws);
}
```

```cpp
#define AMPK 0
#define AMPW 1
#define OWN 0
#define SGN -1
#include <hip/hip_runtime.h>
#include <hip/hip_bf16.h>
#include <stdint.h>

#define KE 4
#define PACE 8
#define VW 8
#define BAR0 0
#define QFLAG 1
#define NRED 160
#define CODE_KB 0

typedef unsigned int u32;
typedef unsigned short u16;
typedef unsigned long long u64;
using u32x2 = __attribute__((ext_vector_type(2))) unsigned;
using u32x4 = __attribute__((ext_vector_type(4))) unsigned;
using f32x2 = __attribute__((ext_vector_type(2))) float;
using f32x4 = __attribute__((ext_vector_type(4))) float;
using bf16x2 = __attribute__((ext_vector_type(2))) __bf16;

constexpr int D = 640, NH = 32, DH = 192, SK = 32768, WLEN = 4096;
constexpr int CPR = D / NRED;
constexpr int UPP = CPR / 2;
constexpr u32 WS_Q = 4096;
constexpr u32 WS_O = WS_Q + 256u * 2048u;
constexpr u32 WS_Y = WS_O + 256u * 2048u;
constexpr u32 WS_BYTES = WS_Y + 320u * 256u * 16u;
constexpr int SPIN_CAP = 200000;
constexpr float LOG2E = 1.4426950408889634f;

__device__ __forceinline__ float dot2(u32 a, u32 b, float acc) {
  return __builtin_amdgcn_fdot2_f32_bf16(__builtin_bit_cast(bf16x2, a), __builtin_bit_cast(bf16x2, b), acc, false);
}
template <int CTRL> __device__ __forceinline__ float dpp(float x) {
  return __builtin_bit_cast(float, __builtin_amdgcn_mov_dpp(__builtin_bit_cast(int, x), CTRL, 0xf, 0xf, true));
}
constexpr int XOR1 = 0xB1, XOR2 = 0x4E, XOR7 = 0x141, ROR8 = 0x128, ROR4 = 0x124;
__device__ __forceinline__ float sum8(float x) {
  x += dpp<XOR1>(x); x += dpp<XOR2>(x); x += dpp<XOR7>(x); return x;
}
__device__ __forceinline__ float max8(float x) {
  x = fmaxf(x, dpp<XOR1>(x)); x = fmaxf(x, dpp<XOR2>(x)); x = fmaxf(x, dpp<XOR7>(x)); return x;
}
__device__ __forceinline__ float xrow_sum(float x) {
  auto s = __builtin_amdgcn_permlane16_swap(__float_as_uint(x), __float_as_uint(x), false, false);
  x = __uint_as_float(s[0]) + __uint_as_float(s[1]);
  auto t = __builtin_amdgcn_permlane32_swap(__float_as_uint(x), __float_as_uint(x), false, false);
  return __uint_as_float(t[0]) + __uint_as_float(t[1]);
}
__device__ __forceinline__ float xrow_max(float x) {
  auto s = __builtin_amdgcn_permlane16_swap(__float_as_uint(x), __float_as_uint(x), false, false);
  x = fmaxf(__uint_as_float(s[0]), __uint_as_float(s[1]));
  auto t = __builtin_amdgcn_permlane32_swap(__float_as_uint(x), __float_as_uint(x), false, false);
  return fmaxf(__uint_as_float(t[0]), __uint_as_float(t[1]));
}
__device__ __forceinline__ float hi_sum(float x) { x += dpp<ROR8>(x); return xrow_sum(x); }
__device__ __forceinline__ float hi_max(float x) { x = fmaxf(x, dpp<ROR8>(x)); return xrow_max(x); }
__device__ __forceinline__ float wave_sum(float x) { return hi_sum(sum8(x)); }
__device__ __forceinline__ float wave_max(float x) { return hi_max(max8(x)); }
__device__ __forceinline__ u32 bfbits(float f) { return (u32)__builtin_bit_cast(u16, (__bf16)f); }
__device__ __forceinline__ float bfr(float f) { return __uint_as_float(bfbits(f) << 16); }
__device__ __forceinline__ u32 pack2(float a, float b) {
  bf16x2 p = {(__bf16)a, (__bf16)b};
  return __builtin_bit_cast(u32, p);
}
__device__ __forceinline__ float blo(u32 v) { return __uint_as_float(v << 16); }
__device__ __forceinline__ float bhi(u32 v) { return __uint_as_float(v & 0xffff0000u); }
#define BAR() asm volatile("s_waitcnt lgkmcnt(0)\n\ts_barrier" ::: "memory")
#define PIN() __builtin_amdgcn_sched_barrier(0)
#define STAMP(i) do { if ((i) == AMPK) { PIN(); tsel = (u32)__builtin_amdgcn_s_memrealtime(); PIN(); } } while (0)
#define KEEP(v) asm volatile("" : "+v"(v))
#define RSRC(p, n) __builtin_amdgcn_make_buffer_rsrc((void*)(p), 0, (n), 0x00020000)
#define WAITVM(n) __builtin_amdgcn_s_waitcnt(0x0F70 | ((n) & 15) | (((n) >> 4) << 14))

constexpr int PART_W = 1568;
constexpr int L_PART = 0;
constexpr int L_SC = L_PART + 8 * PART_W;
constexpr int L_PT = L_SC + 8 * 192;
constexpr int L_QS = L_PT + 8 * 64;
constexpr int L_ML = L_QS + 128;
constexpr int L_OW = L_ML + 16;
constexpr int L_RED = L_OW + 128;
constexpr int L_YP = L_RED + 128;
constexpr int L_END = L_YP + 8 * 480;

__global__ void __launch_bounds__(512, 2)
fused_decode_kernel(const u16* __restrict__ x, const u16* __restrict__ kc, const u16* __restrict__ vc,
                    const u16* __restrict__ wq, const u16* __restrict__ wo, const u16* __restrict__ w1,
                    const u16* __restrict__ w2, u16* __restrict__ out, u32* __restrict__ ws) {
  __shared__ __attribute__((aligned(16))) float lds[L_END];
  u64 tst64;
  asm volatile("s_memrealtime %0\n\ts_waitcnt lgkmcnt(0)" : "=s"(tst64) :: "memory");
  PIN();
  const u32 tst = (u32)tst64;
  u32 tsel = AMPK == 0 ? tst : 0u;
  const int tid = threadIdx.x;
  const int lane = tid & 63;
  const int w = __builtin_amdgcn_readfirstlane(tid >> 6);
  const int b = blockIdx.x;
  const int h = (b & 7) * 4 + (b >> 6);
  const int s = (b >> 3) & 7;
  const int j = h * 8 + s;

  const auto rs_ws = RSRC(ws, WS_BYTES);
  const auto rs_x = RSRC(x, D * 2);
  const u32x4 xa = __builtin_amdgcn_raw_buffer_load_b128(rs_x, lane * 20, 0, 0);
  STAMP(12);
  const u32 xb = __builtin_amdgcn_raw_buffer_load_b32(rs_x, lane * 20 + 16, 0, 0);
  const int row0 = 80 * s + 10 * w;
  const bool isq = lane < 48;
  const int l1 = lane < 60 ? lane - 48 : 11;
  const int r2 = isq ? (lane >= 24) : (l1 >= 6);
  const int cch = isq ? lane - 24 * r2 : l1 - 6 * r2;
  u32x4 wv[5];
  {
    const char* p = isq ? (const char*)wq + (size_t)(row0 + r2) * 12288 + h * 384 + cch * 16
                        : (const char*)w1 + (size_t)(row0 + r2) * 3072 + h * 96 + cch * 16;
    const u32 stp = isq ? 24576u : 6144u;
#pragma unroll
    for (int i = 0; i < 5; ++i) {
      wv[i] = __builtin_nontemporal_load((const u32x4*)p);
      p += stp;
      PIN();
    }
  }
  const u32 E = __builtin_amdgcn_raw_buffer_load_b32(rs_ws, 0, 0, 16);
  const size_t kvoff = ((size_t)h * SK + (SK - WLEN) + s * 512 + w * 64) * DH;
  const auto rs_k = RSRC(kc + kvoff, 64 * DH * 2);
  const auto rs_v = RSRC(vc + kvoff, 64 * DH * 2);
  u32 cpf = 0;
  if (CODE_KB > 0 && w == 7) {
    const u64 pc = __builtin_amdgcn_s_getpc() & ~(u64)255;
    cpf = __builtin_amdgcn_raw_buffer_load_b32(RSRC(pc, CODE_KB * 1024), lane * 64, ((b >> 3) % ((CODE_KB + 3) / 4)) * 4096, 0);
  }
  PIN();
  u32x4 kv[24 + VW];
#pragma unroll
  for (int i = 0; i < KE; ++i) {
    kv[i] = __builtin_amdgcn_raw_buffer_load_b128(rs_k, lane * 16, i * 1024, 2);
    PIN();
  }
  u32 xr[5];
#pragma unroll
  for (int i = 0; i < 5; ++i) xr[i] = reinterpret_cast<const u32*>(x)[row0 / 2 + i];

  {
    float xv[10];
    xv[0] = blo(xa.x); xv[1] = bhi(xa.x); xv[2] = blo(xa.y); xv[3] = bhi(xa.y);
    xv[4] = blo(xa.z); xv[5] = bhi(xa.z); xv[6] = blo(xa.w); xv[7] = bhi(xa.w);
    xv[8] = blo(xb); xv[9] = bhi(xb);
    float sm = ((xv[0] + xv[1]) + (xv[2] + xv[3])) + ((xv[4] + xv[5]) + (xv[6] + xv[7])) + (xv[8] + xv[9]);
    const float mean = wave_sum(sm) * (1.0f / D);
    float sq = 0.f;
#pragma unroll
    for (int e = 0; e < 10; ++e) { const float d = xv[e] - mean; sq += d * d; }
    const float var = wave_sum(sq) * (1.0f / D);
    const float mb = bfr(mean);
    const float den = bfr(__builtin_amdgcn_sqrtf(bfr(bfr(var) + 1e-5f)));
    const float rinv = 1.0f / den;
    STAMP(1);
#pragma unroll
    for (int i = 0; i < 5; ++i) asm volatile("" : "+s"(xr[i]));
    float acc[8];
#pragma unroll
    for (int e = 0; e < 8; ++e) acc[e] = 0.f;
#pragma unroll
    for (int i = 0; i < 5; ++i) {
      const float v = r2 ? bhi(xr[i]) : blo(xr[i]);
      const u32 xl = bfbits(bfr(v - mb) * rinv), xh = xl << 16;
#pragma unroll
      for (int d = 0; d < 4; ++d) {
        acc[2 * d] = dot2(wv[i][d], xl, acc[2 * d]);
        acc[2 * d + 1] = dot2(wv[i][d], xh, acc[2 * d + 1]);
      }
    }
    if (lane < 60) {
      float* p = lds + L_PART + (2 * w + r2) * 244 + (isq ? 0 : 192) + cch * 8;
      *reinterpret_cast<f32x4*>(p) = f32x4{acc[0], acc[1], acc[2], acc[3]};
      *reinterpret_cast<f32x4*>(p + 4) = f32x4{acc[4], acc[5], acc[6], acc[7]};
    }
  }
  const u32 tag = E + 1u;
  volatile u32* qflag = reinterpret_cast<volatile u32*>(lds + L_QS + 124);
  if (QFLAG && tid == 64) *qflag = 0u;
  KEEP(cpf);
  if (b == 0 && tid == 0) { u32x2 g = {tst, tag}; __builtin_amdgcn_raw_buffer_store_b64(g, rs_ws, 64, 0, 16); }
  STAMP(2);
  BAR();

  if (w == 0) {
    __builtin_amdgcn_s_setprio(3);
    const int t = lane < 60 ? lane : 59;
    const float* p = lds + L_PART + t * 4;
    f32x4 a = *reinterpret_cast<const f32x4*>(p);
#pragma unroll
    for (int r = 1; r < 16; ++r) a += *reinterpret_cast<const f32x4*>(p + r * 244);
    if (lane < 60) {
      const u32x4 u0 = {__float_as_uint(a.x), tag, __float_as_uint(a.y), tag};
      const u32x4 u1 = {__float_as_uint(a.z), tag, __float_as_uint(a.w), tag};
      __builtin_amdgcn_raw_buffer_store_b128(u0, rs_ws, lane * 32, WS_Q + j * 2048, 16);
      __builtin_amdgcn_raw_buffer_store_b128(u1, rs_ws, lane * 32 + 16, WS_Q + j * 2048, 16);
    }
    STAMP(3);
    f32x4 sum;
    int spins = 0;
    bool ok;
    do {
      asm volatile("" ::: "memory");
      u32x4 un[16];
#pragma unroll
      for (int k = 0; k < 16; ++k)
        un[k] = __builtin_amdgcn_raw_buffer_load_b128(rs_ws, t * 16 + (k & 1) * 960 + (k >> 1) * 2048, WS_Q + h * 16384, 16);
      u32 mn = tag, mx = tag;
      sum = f32x4{0.f, 0.f, 0.f, 0.f};
#pragma unroll
      for (int k = 0; k < 16; ++k) {
        mn = min(mn, min(un[k].y, un[k].w));
        mx = max(mx, max(un[k].y, un[k].w));
        sum[2 * (k & 1)] += __uint_as_float(un[k].x);
        sum[2 * (k & 1) + 1] += __uint_as_float(un[k].z);
      }
      ok = __all(mn == mx);
    } while (!ok && ++spins < SPIN_CAP);
    if (t >= 36) { sum.z = fmaxf(sum.z, 0.f); sum.w = fmaxf(sum.w, 0.f); }
    u32* qs = reinterpret_cast<u32*>(lds + L_QS);
    qs[t] = pack2(sum.x, sum.y);
    qs[60 + t] = pack2(sum.z, sum.w);
    STAMP(4);
    asm volatile("s_waitcnt lgkmcnt(0)" ::: "memory");
    *qflag = 1u;
  }
#pragma unroll
  for (int i = KE; i < 24 + VW; ++i) {
    if (w != 0) WAITVM(PACE - 1);
    if (i < 24) kv[i] = __builtin_amdgcn_raw_buffer_load_b128(rs_k, lane * 16, i * 1024, 2);
    else kv[i] = __builtin_amdgcn_raw_buffer_load_b128(rs_v, lane * 16, (i - 24) * 1024, 2);
    PIN();
  }
  int rho[3], c8[3];
#pragma unroll
  for (int u = 0; u < 3; ++u) { const int g = 64 * u + lane; rho[u] = g / 24; c8[u] = g % 24; }
  const int r6 = (lane / 10) > 5 ? 5 : (lane / 10);
  const int ch = lane - 10 * (lane / 10);
  const auto rs_wo = RSRC(wo + (size_t)(h * DH + 24 * s) * D + 80 * w, 23 * 1280 + 160);
  const auto rs_w2 = RSRC(w2 + (size_t)(48 * h + 6 * s) * D + 80 * w, 5 * 1280 + 160);
  const int wo_voff = r6 * 1280 + ch * 16;
  if (w != 0) {
    STAMP(3);
    int spins = 0;
    while (*qflag == 0u && ++spins < 100 * SPIN_CAP) __builtin_amdgcn_s_sleep(1);
    asm volatile("" ::: "memory");
    STAMP(4);
  }
  u32x4 q[3];
#pragma unroll
  for (int u = 0; u < 3; ++u) q[u] = *reinterpret_cast<const u32x4*>(lds + L_QS + c8[u] * 4);

  u32x4 wov[5];
  float sv[3] = {0.f, 0.f, 0.f};
#pragma unroll
  for (int i = 0; i < 24; ++i) {
    const u32x4 d = kv[i];
    const int u = i % 3;
    float a = dot2(d.x, q[u].x, 0.f);
    float c = dot2(d.z, q[u].z, 0.f);
    a = dot2(d.y, q[u].y, a);
    c = dot2(d.w, q[u].w, c);
    const int n = i + VW;
    if (n < 24) kv[i] = __builtin_amdgcn_raw_buffer_load_b128(rs_v, lane * 16, n * 1024, 2);
    else if (n < 28) wov[n - 24] = __builtin_amdgcn_raw_buffer_load_b128(rs_wo, wo_voff, (n - 24) * 7680, 2);
    else if (n == 28) wov[4] = __builtin_amdgcn_raw_buffer_load_b128(rs_w2, wo_voff, 0, 2);
    a = sum8(a + c);
    sv[i / 8] = ((lane & 7) == (i & 7)) ? a : sv[i / 8];
    PIN();
  }
  STAMP(5);
  {
    float* sc = lds + L_SC + w * 192;
#pragma unroll
    for (int a = 0; a < 3; ++a) sc[(8 * a + (lane & 7)) * 8 + (lane >> 3)] = sv[a];
    const float raw = (sc[3 * lane] + sc[3 * lane + 1]) + sc[3 * lane + 2];
    const float sco = bfr(bfr(raw) * 0.07216878364870322f);
    const float mw = wave_max(sco);
    const float p = __builtin_amdgcn_exp2f((sco - mw) * LOG2E);
    lds[L_PT + w * 64 + (lane & 7) * 8 + (lane >> 3)] = p;
    const float lw = wave_sum(p);
    if (lane == 0) *reinterpret_cast<f32x2*>(lds + L_ML + 2 * w) = f32x2{mw, lw};
  }
  PIN();

  float acc[3][8];
#pragma unroll
  for (int u = 0; u < 3; ++u)
#pragma unroll
    for (int e = 0; e < 8; ++e) acc[u][e] = 0.f;
  f32x4 pv[3];
#pragma unroll
  for (int i = 0; i < 24; ++i) {
    const int u = i % 3, t = i / 3;
    if (i % 12 == 0) {
#pragma unroll
      for (int uu = 0; uu < 3; ++uu)
        pv[uu] = *reinterpret_cast<const f32x4*>(lds + L_PT + w * 64 + rho[uu] * 8 + (i / 12) * 4);
    }
    const u32x4 d = i < VW ? kv[24 + i] : kv[i - VW];
    const float pp = pv[u][t % 4];
#pragma unroll
    for (int c = 0; c < 4; ++c) {
      f32x2 a2 = {acc[u][2 * c], acc[u][2 * c + 1]};
      const f32x2 v2 = {blo(d[c]), bhi(d[c])};
      a2 = __builtin_elementwise_fma(v2, f32x2{pp, pp}, a2);
      KEEP(a2);
      acc[u][2 * c] = a2.x; acc[u][2 * c + 1] = a2.y;
    }
    PIN();
  }
  STAMP(6);
  {
    float* pw = lds + L_PART + w * PART_W + lane * 8;
#pragma unroll
    for (int u = 0; u < 3; ++u) {
      *reinterpret_cast<f32x4*>(pw + u * 512) = f32x4{acc[u][0], acc[u][1], acc[u][2], acc[u][3]};
      *reinterpret_cast<f32x4*>(pw + u * 512 + 4) = f32x4{acc[u][4], acc[u][5], acc[u][6], acc[u][7]};
    }
  }
  BAR();
  if (w < 7) {
    const int col4 = tid >> 3, rg = tid & 7;
    const f32x2 ml = *reinterpret_cast<const f32x2*>(lds + L_ML + 2 * rg);
    const float M = max8(ml.x);
    const float f = __builtin_amdgcn_exp2f((ml.x - M) * LOG2E);
    if (w < 6) {
      const float* p = lds + L_PART + rg * PART_W + col4 * 4;
      f32x4 a = *reinterpret_cast<const f32x4*>(p);
#pragma unroll
      for (int r = 1; r < 8; ++r) a += *reinterpret_cast<const f32x4*>(p + r * 192);
      a *= f;
      a.x = sum8(a.x); a.y = sum8(a.y); a.z = sum8(a.z); a.w = sum8(a.w);
      if (rg < 2) {
        const int cs = col4 / 6, k = (col4 - 6 * cs) * 2 + rg;
        const u32x4 un = {__float_as_uint(rg ? a.z : a.x), tag, __float_as_uint(rg ? a.w : a.y), tag};
        __builtin_amdgcn_raw_buffer_store_b128(un, rs_ws, cs * 2048 + k * 16, WS_O + h * 16384 + s * 256, 16);
      }
    } else {
      const float L = sum8(f * ml.y);
      if (lane < 8) {
        const u32x4 un = {__float_as_uint(M), tag, __float_as_uint(L), tag};
        __builtin_amdgcn_raw_buffer_store_b128(un, rs_ws, lane * 2048 + 192, WS_O + h * 16384 + s * 256, 16);
      }
    }
  }
  STAMP(7);
  float y[8];
  {
    const u32 hid = reinterpret_cast<const u16*>(lds + L_QS)[192 + 6 * s + r6];
#pragma unroll
    for (int d = 0; d < 4; ++d) {
      y[2 * d] = dot2(wov[4][d], hid, 0.f);
      y[2 * d + 1] = dot2(wov[4][d], hid << 16, 0.f);
    }
    const int kk = lane & 7;
    const int ob = (lane >> 3) * 256;
    u32x4 u0, u1, u2;
    int spins = 0;
    bool ok;
    do {
      asm volatile("" ::: "memory");
      u0 = __builtin_amdgcn_raw_buffer_load_b128(rs_ws, ob + kk * 16, WS_O + j * 2048, 16);
      u1 = __builtin_amdgcn_raw_buffer_load_b128(rs_ws, ob + 128 + (kk & 3) * 16, WS_O + j * 2048, 16);
      u2 = __builtin_amdgcn_raw_buffer_load_b128(rs_ws, ob + 192, WS_O + j * 2048, 16);
      const u32 mn = min(min(min(u0.y, u0.w), min(u1.y, u1.w)), min(min(u2.y, u2.w), tag));
      const u32 mx = max(max(max(u0.y, u0.w), max(u1.y, u1.w)), max(max(u2.y, u2.w), tag));
      ok = __all(mn == mx);
    } while (!ok && ++spins < SPIN_CAP);
    STAMP(8);
    const float mp = __uint_as_float(u2.x);
    const float M = hi_max(mp);
    float f = __builtin_amdgcn_exp2f((mp - M) * LOG2E);
    const float L = hi_sum(f * __uint_as_float(u2.z));
    f *= __builtin_amdgcn_rcpf(L);
    const float o0 = hi_sum(f * __uint_as_float(u0.x)), o1 = hi_sum(f * __uint_as_float(u0.z));
    const float o2 = hi_sum(f * __uint_as_float(u1.x)), o3 = hi_sum(f * __uint_as_float(u1.z));
    u32* ow = reinterpret_cast<u32*>(lds + L_OW + w * 16);
    if (lane < 12) ow[lane] = lane < 8 ? pack2(o0, o1) : pack2(o2, o3);
    const u16* oh = reinterpret_cast<const u16*>(ow);
#pragma unroll
    for (int i = 0; i < 4; ++i) {
      const u32 ov = oh[6 * i + r6];
#pragma unroll
      for (int d = 0; d < 4; ++d) {
        y[2 * d] = dot2(wov[i][d], ov, y[2 * d]);
        y[2 * d + 1] = dot2(wov[i][d], ov << 16, y[2 * d + 1]);
      }
    }
  }
  {
    float* yp = lds + L_YP + w * 480;
    if (lane < 60) {
      *reinterpret_cast<f32x4*>(yp + lane * 8) = f32x4{y[0], y[1], y[2], y[3]};
      *reinterpret_cast<f32x4*>(yp + lane * 8 + 4) = f32x4{y[4], y[5], y[6], y[7]};
    }
    if (lane < 40) {
      f32x2 a = *reinterpret_cast<const f32x2*>(yp + lane * 2);
#pragma unroll
      for (int r = 1; r < 6; ++r) a += *reinterpret_cast<const f32x2*>(yp + r * 80 + lane * 2);
      const int n2 = 40 * w + lane;
      const int r = n2 / UPP, ui = n2 % UPP;
      const u32x4 un = {__float_as_uint(a.x), tag, __float_as_uint(a.y), tag};
      __builtin_amdgcn_raw_buffer_store_b128(un, rs_ws, (r * 256 * UPP + ui) * 16, WS_Y + j * UPP * 16, 16);
    }
  }
  STAMP(9);
  if (b < NRED) {
    constexpr int NU = 256 * UPP / 512;
    const u32 xo = x[b * CPR + (tid < CPR ? tid : 0)];
    float v0, v1;
    int spins = 0;
    bool ok;
    do {
      asm volatile("" ::: "memory");
      u32x4 un[NU];
#pragma unroll
      for (int k = 0; k < NU; ++k)
        un[k] = __builtin_amdgcn_raw_buffer_load_b128(rs_ws, tid * 16 + k * 8192, WS_Y + b * 256 * UPP * 16, 16);
      u32 mn = tag, mx = tag;
      v0 = 0.f; v1 = 0.f;
#pragma unroll
      for (int k = 0; k < NU; ++k) {
        mn = min(mn, min(un[k].y, un[k].w));
        mx = max(mx, max(un[k].y, un[k].w));
        v0 += __uint_as_float(un[k].x); v1 += __uint_as_float(un[k].z);
      }
      ok = __all(mn == mx);
    } while (!ok && ++spins < SPIN_CAP);
    STAMP(10);
    if (UPP <= 1) { v0 += dpp<XOR1>(v0); v1 += dpp<XOR1>(v1); }
    if (UPP <= 2) { v0 += dpp<XOR2>(v0); v1 += dpp<XOR2>(v1); }
    if (UPP <= 4) { v0 += dpp<ROR4>(v0); v1 += dpp<ROR4>(v1); }
    v0 = hi_sum(v0); v1 = hi_sum(v1);
    if (lane < UPP) *reinterpret_cast<f32x2*>(lds + L_RED + w * 16 + lane * 2) = f32x2{v0, v1};
    BAR();
    if (tid < CPR) {
      float a = 0.f;
#pragma unroll
      for (int k = 0; k < 8; ++k) a += lds[L_RED + k * 16 + tid];
      a += blo(xo);
      out[b * CPR + tid] = (u16)bfbits(a);
    }
    if (b == 0 && tid == 0) __builtin_amdgcn_raw_buffer_store_b32(tag, rs_ws, 0, 0, 16);
  }
  STAMP(11);
  if (tsel != 0u && (AMPW)) {
    u32x2 g; int sp = 0;
    do { asm volatile("" ::: "memory"); g = __builtin_amdgcn_raw_buffer_load_b64(rs_ws, 64, 0, 16); } while (g.y != tag && ++sp < SPIN_CAP);
    const u32 t0 = OWN ? tst : __builtin_amdgcn_readfirstlane(g.x);
    int d = SGN * (int)(tsel - t0) + 200;
    if (d < 0) d = 0;
    if (d > 10000) d = 10000;
    const u32 ta = (u32)__builtin_amdgcn_s_memrealtime();
    while ((u32)__builtin_amdgcn_s_memrealtime() - ta < (u32)d * 16u) __builtin_amdgcn_s_sleep(2);
  }
}

extern "C" void kernel_launch(void* const* d_in, const int* in_sizes, int n_in,
                              void* d_out, int out_size, void* d_ws, size_t ws_size,
                              hipStream_t stream) {
  if (ws_size < WS_BYTES) return;
  fused_decode_kernel<<<256, 512, 0, stream>>>(
      (const u16*)d_in[0], (const u16*)d_in[1], (const u16*)d_in[2], (const u16*)d_in[3],
      (const u16*)d_in[4], (const u16*)d_in[6], (const u16*)d_in[7], (u16*)d_out, (u32*)d_ws);
}
```

```cpp
#include <hip/hip_runtime.h>
#include <hip/hip_bf16.h>
#include <stdint.h>

#define KE 4
#define PACE 8
#define VW 8
#define BAR0 0
#define QFLAG 1
#define NRED 160
#define CODE_KB 0

typedef unsigned int u32;
typedef unsigned short u16;
typedef unsigned long long u64;
using u32x2 = __attribute__((ext_vector_type(2))) unsigned;
using u32x4 = __attribute__((ext_vector_type(4))) unsigned;
using f32x2 = __attribute__((ext_vector_type(2))) float;
using f32x4 = __attribute__((ext_vector_type(4))) float;
using bf16x2 = __attribute__((ext_vector_type(2))) __bf16;

constexpr int D = 640, NH = 32, DH = 192, SK = 32768, WLEN = 4096;
constexpr int CPR = D / NRED;
constexpr int UPP = CPR / 2;
constexpr u32 WS_Q = 4096;
constexpr u32 WS_O = WS_Q + 256u * 2048u;
constexpr u32 WS_Y = WS_O + 256u * 2048u;
constexpr u32 WS_BYTES = WS_Y + 320u * 256u * 16u;
constexpr int SPIN_CAP = 200000;
constexpr float LOG2E = 1.4426950408889634f;

__device__ __forceinline__ float dot2(u32 a, u32 b, float acc) {
  return __builtin_amdgcn_fdot2_f32_bf16(__builtin_bit_cast(bf16x2, a), __builtin_bit_cast(bf16x2, b), acc, false);
}
template <int CTRL> __device__ __forceinline__ float dpp(float x) {
  return __builtin_bit_cast(float, __builtin_amdgcn_mov_dpp(__builtin_bit_cast(int, x), CTRL, 0xf, 0xf, true));
}
constexpr int XOR1 = 0xB1, XOR2 = 0x4E, XOR7 = 0x141, ROR8 = 0x128, ROR4 = 0x124;
__device__ __forceinline__ float sum8(float x) {
  x += dpp<XOR1>(x); x += dpp<XOR2>(x); x += dpp<XOR7>(x); return x;
}
__device__ __forceinline__ float max8(float x) {
  x = fmaxf(x, dpp<XOR1>(x)); x = fmaxf(x, dpp<XOR2>(x)); x = fmaxf(x, dpp<XOR7>(x)); return x;
}
__device__ __forceinline__ float xrow_sum(float x) {
  auto s = __builtin_amdgcn_permlane16_swap(__float_as_uint(x), __float_as_uint(x), false, false);
  x = __uint_as_float(s[0]) + __uint_as_float(s[1]);
  auto t = __builtin_amdgcn_permlane32_swap(__float_as_uint(x), __float_as_uint(x), false, false);
  return __uint_as_float(t[0]) + __uint_as_float(t[1]);
}
__device__ __forceinline__ float xrow_max(float x) {
  auto s = __builtin_amdgcn_permlane16_swap(__float_as_uint(x), __float_as_uint(x), false, false);
  x = fmaxf(__uint_as_float(s[0]), __uint_as_float(s[1]));
  auto t = __builtin_amdgcn_permlane32_swap(__float_as_uint(x), __float_as_uint(x), false, false);
  return fmaxf(__uint_as_float(t[0]), __uint_as_float(t[1]));
}
__device__ __forceinline__ float hi_sum(float x) { x += dpp<ROR8>(x); return xrow_sum(x); }
__device__ __forceinline__ float hi_max(float x) { x = fmaxf(x, dpp<ROR8>(x)); return xrow_max(x); }
__device__ __forceinline__ float wave_sum(float x) { return hi_sum(sum8(x)); }
__device__ __forceinline__ float wave_max(float x) { return hi_max(max8(x)); }
__device__ __forceinline__ u32 bfbits(float f) { return (u32)__builtin_bit_cast(u16, (__bf16)f); }
__device__ __forceinline__ float bfr(float f) { return __uint_as_float(bfbits(f) << 16); }
__device__ __forceinline__ u32 pack2(float a, float b) {
  bf16x2 p = {(__bf16)a, (__bf16)b};
  return __builtin_bit_cast(u32, p);
}
__device__ __forceinline__ float blo(u32 v) { return __uint_as_float(v << 16); }
__device__ __forceinline__ float bhi(u32 v) { return __uint_as_float(v & 0xffff0000u); }
#define BAR() asm volatile("s_waitcnt lgkmcnt(0)\n\ts_barrier" ::: "memory")
#define PIN() __builtin_amdgcn_sched_barrier(0)
#define STAMP(i)
#define KEEP(v) asm volatile("" : "+v"(v))
#define RSRC(p, n) __builtin_amdgcn_make_buffer_rsrc((void*)(p), 0, (n), 0x00020000)
#define WAITVM(n) __builtin_amdgcn_s_waitcnt(0x0F70 | ((n) & 15) | (((n) >> 4) << 14))

constexpr int PART_W = 1568;
constexpr int L_PART = 0;
constexpr int L_SC = L_PART + 8 * PART_W;
constexpr int L_PT = L_SC + 8 * 192;
constexpr int L_QS = L_PT + 8 * 64;
constexpr int L_ML = L_QS + 128;
constexpr int L_OW = L_ML + 16;
constexpr int L_RED = L_OW + 128;
constexpr int L_YP = L_RED + 128;
constexpr int L_END = L_YP + 8 * 480;

__global__ void __launch_bounds__(512, 2)
fused_decode_kernel(const u16* __restrict__ x, const u16* __restrict__ kc, const u16* __restrict__ vc,
                    const u16* __restrict__ wq, const u16* __restrict__ wo, const u16* __restrict__ w1,
                    const u16* __restrict__ w2, u16* __restrict__ out, u32* __restrict__ ws) {
  __shared__ __attribute__((aligned(16))) float lds[L_END];
  const int tid = threadIdx.x;
  const int lane = tid & 63;
  const int w = __builtin_amdgcn_readfirstlane(tid >> 6);
  const int b = blockIdx.x;
  const int h = (b & 7) * 4 + (b >> 6);
  const int s = (b >> 3) & 7;
  const int j = h * 8 + s;

  const auto rs_ws = RSRC(ws, WS_BYTES);
  const auto rs_x = RSRC(x, D * 2);
  const u32x4 xa = __builtin_amdgcn_raw_buffer_load_b128(rs_x, lane * 20, 0, 0);
  const u32 xb = __builtin_amdgcn_raw_buffer_load_b32(rs_x, lane * 20 + 16, 0, 0);
  const int row0 = 80 * s + 10 * w;
  const bool isq = lane < 48;
  const int l1 = lane < 60 ? lane - 48 : 11;
  const int r2 = isq ? (lane >= 24) : (l1 >= 6);
  const int cch = isq ? lane - 24 * r2 : l1 - 6 * r2;
  u32x4 wv[5];
  {
    const char* p = isq ? (const char*)wq + (size_t)(row0 + r2) * 12288 + h * 384 + cch * 16
                        : (const char*)w1 + (size_t)(row0 + r2) * 3072 + h * 96 + cch * 16;
    const u32 stp = isq ? 24576u : 6144u;
#pragma unroll
    for (int i = 0; i < 5; ++i) {
      wv[i] = __builtin_nontemporal_load((const u32x4*)p);
      p += stp;
      PIN();
    }
  }
  const u32 E = __builtin_amdgcn_raw_buffer_load_b32(rs_ws, 0, 0, 16);
  const size_t kvoff = ((size_t)h * SK + (SK - WLEN) + s * 512 + w * 64) * DH;
  const auto rs_k = RSRC(kc + kvoff, 64 * DH * 2);
  const auto rs_v = RSRC(vc + kvoff, 64 * DH * 2);
  u32 cpf = 0;
  if (CODE_KB > 0 && w == 7) {
    const u64 pc = __builtin_amdgcn_s_getpc() & ~(u64)255;
    cpf = __builtin_amdgcn_raw_buffer_load_b32(RSRC(pc, CODE_KB * 1024), lane * 64, ((b >> 3) % ((CODE_KB + 3) / 4)) * 4096, 0);
  }
  PIN();
  u32x4 kv[24 + VW];
#pragma unroll
  for (int i = 0; i < KE; ++i) {
    kv[i] = __builtin_amdgcn_raw_buffer_load_b128(rs_k, lane * 16, i * 1024, 2);
    PIN();
  }
  u32 xr[5];
#pragma unroll
  for (int i = 0; i < 5; ++i) xr[i] = reinterpret_cast<const u32*>(x)[row0 / 2 + i];

  {
    float xv[10];
    xv[0] = blo(xa.x); xv[1] = bhi(xa.x); xv[2] = blo(xa.y); xv[3] = bhi(xa.y);
    xv[4] = blo(xa.z); xv[5] = bhi(xa.z); xv[6] = blo(xa.w); xv[7] = bhi(xa.w);
    xv[8] = blo(xb); xv[9] = bhi(xb);
    float sm = ((xv[0] + xv[1]) + (xv[2] + xv[3])) + ((xv[4] + xv[5]) + (xv[6] + xv[7])) + (xv[8] + xv[9]);
    const float mean = wave_sum(sm) * (1.0f / D);
    float sq = 0.f;
#pragma unroll
    for (int e = 0; e < 10; ++e) { const float d = xv[e] - mean; sq += d * d; }
    const float var = wave_sum(sq) * (1.0f / D);
    const float mb = bfr(mean);
    const float den = bfr(__builtin_amdgcn_sqrtf(bfr(bfr(var) + 1e-5f)));
    const float rinv = 1.0f / den;
    STAMP(1);
#pragma unroll
    for (int i = 0; i < 5; ++i) asm volatile("" : "+s"(xr[i]));
    float acc[8];
#pragma unroll
    for (int e = 0; e < 8; ++e) acc[e] = 0.f;
#pragma unroll
    for (int i = 0; i < 5; ++i) {
      const float v = r2 ? bhi(xr[i]) : blo(xr[i]);
      const u32 xl = bfbits(bfr(v - mb) * rinv), xh = xl << 16;
#pragma unroll
      for (int d = 0; d < 4; ++d) {
        acc[2 * d] = dot2(wv[i][d], xl, acc[2 * d]);
        acc[2 * d + 1] = dot2(wv[i][d], xh, acc[2 * d + 1]);
      }
    }
    if (lane < 60) {
      float* p = lds + L_PART + (2 * w + r2) * 244 + (isq ? 0 : 192) + cch * 8;
      *reinterpret_cast<f32x4*>(p) = f32x4{acc[0], acc[1], acc[2], acc[3]};
      *reinterpret_cast<f32x4*>(p + 4) = f32x4{acc[4], acc[5], acc[6], acc[7]};
    }
  }
  const u32 tag = E + 1u;
  volatile u32* qflag = reinterpret_cast<volatile u32*>(lds + L_QS + 124);
  if (QFLAG && tid == 64) *qflag = 0u;
  KEEP(cpf);
  STAMP(2);
  BAR();

  if (w == 0) {
    __builtin_amdgcn_s_setprio(3);
    const int t = lane < 60 ? lane : 59;
    const float* p = lds + L_PART + t * 4;
    f32x4 a = *reinterpret_cast<const f32x4*>(p);
#pragma unroll
    for (int r = 1; r < 16; ++r) a += *reinterpret_cast<const f32x4*>(p + r * 244);
    if (lane < 60) {
      const u32x4 u0 = {__float_as_uint(a.x), tag, __float_as_uint(a.y), tag};
      const u32x4 u1 = {__float_as_uint(a.z), tag, __float_as_uint(a.w), tag};
      __builtin_amdgcn_raw_buffer_store_b128(u0, rs_ws, lane * 32, WS_Q + j * 2048, 16);
      __builtin_amdgcn_raw_buffer_store_b128(u1, rs_ws, lane * 32 + 16, WS_Q + j * 2048, 16);
    }
    STAMP(3);
    f32x4 sum;
    int spins = 0;
    bool ok;
    do {
      asm volatile("" ::: "memory");
      u32x4 un[16];
#pragma unroll
      for (int k = 0; k < 16; ++k)
        un[k] = __builtin_amdgcn_raw_buffer_load_b128(rs_ws, t * 16 + (k & 1) * 960 + (k >> 1) * 2048, WS_Q + h * 16384, 16);
      u32 mn = tag, mx = tag;
      sum = f32x4{0.f, 0.f, 0.f, 0.f};
#pragma unroll
      for (int k = 0; k < 16; ++k) {
        mn = min(mn, min(un[k].y, un[k].w));
        mx = max(mx, max(un[k].y, un[k].w));
        sum[2 * (k & 1)] += __uint_as_float(un[k].x);
        sum[2 * (k & 1) + 1] += __uint_as_float(un[k].z);
      }
      ok = __all(mn == mx);
    } while (!ok && ++spins < SPIN_CAP);
    if (t >= 36) { sum.z = fmaxf(sum.z, 0.f); sum.w = fmaxf(sum.w, 0.f); }
    u32* qs = reinterpret_cast<u32*>(lds + L_QS);
    qs[t] = pack2(sum.x, sum.y);
    qs[60 + t] = pack2(sum.z, sum.w);
    STAMP(4);
    asm volatile("s_waitcnt lgkmcnt(0)" ::: "memory");
    *qflag = 1u;
  }
#pragma unroll
  for (int i = KE; i < 24 + VW; ++i) {
    if (w != 0) WAITVM(PACE - 1);
    if (i < 24) kv[i] = __builtin_amdgcn_raw_buffer_load_b128(rs_k, lane * 16, i * 1024, 2);
    else kv[i] = __builtin_amdgcn_raw_buffer_load_b128(rs_v, lane * 16, (i - 24) * 1024, 2);
    PIN();
  }
  int rho[3], c8[3];
#pragma unroll
  for (int u = 0; u < 3; ++u) { const int g = 64 * u + lane; rho[u] = g / 24; c8[u] = g % 24; }
  const int r6 = (lane / 10) > 5 ? 5 : (lane / 10);
  const int ch = lane - 10 * (lane / 10);
  const auto rs_wo = RSRC(wo + (size_t)(h * DH + 24 * s) * D + 80 * w, 23 * 1280 + 160);
  const auto rs_w2 = RSRC(w2 + (size_t)(48 * h + 6 * s) * D + 80 * w, 5 * 1280 + 160);
  const int wo_voff = r6 * 1280 + ch * 16;
  if (w != 0) {
    STAMP(3);
    int spins = 0;
    while (*qflag == 0u && ++spins < 100 * SPIN_CAP) __builtin_amdgcn_s_sleep(1);
    asm volatile("" ::: "memory");
    STAMP(4);
  }
  u32x4 q[3];
#pragma unroll
  for (int u = 0; u < 3; ++u) q[u] = *reinterpret_cast<const u32x4*>(lds + L_QS + c8[u] * 4);

  u32x4 wov[5];
  float sv[3] = {0.f, 0.f, 0.f};
#pragma unroll
  for (int i = 0; i < 24; ++i) {
    const u32x4 d = kv[i];
    const int u = i % 3;
    float a = dot2(d.x, q[u].x, 0.f);
    float c = dot2(d.z, q[u].z, 0.f);
    a = dot2(d.y, q[u].y, a);
    c = dot2(d.w, q[u].w, c);
    const int n = i + VW;
    if (n < 24) kv[i] = __builtin_amdgcn_raw_buffer_load_b128(rs_v, lane * 16, n * 1024, 2);
    else if (n < 28) wov[n - 24] = __builtin_amdgcn_raw_buffer_load_b128(rs_wo, wo_voff, (n - 24) * 7680, 2);
    else if (n == 28) wov[4] = __builtin_amdgcn_raw_buffer_load_b128(rs_w2, wo_voff, 0, 2);
    a = sum8(a + c);
    sv[i / 8] = ((lane & 7) == (i & 7)) ? a : sv[i / 8];
    PIN();
  }
  STAMP(5);
  {
    float* sc = lds + L_SC + w * 192;
#pragma unroll
    for (int a = 0; a < 3; ++a) sc[(8 * a + (lane & 7)) * 8 + (lane >> 3)] = sv[a];
    const float raw = (sc[3 * lane] + sc[3 * lane + 1]) + sc[3 * lane + 2];
    const float sco = bfr(bfr(raw) * 0.07216878364870322f);
    const float mw = wave_max(sco);
    const float p = __builtin_amdgcn_exp2f((sco - mw) * LOG2E);
    lds[L_PT + w * 64 + (lane & 7) * 8 + (lane >> 3)] = p;
    const float lw = wave_sum(p);
    if (lane == 0) *reinterpret_cast<f32x2*>(lds + L_ML + 2 * w) = f32x2{mw, lw};
  }
  PIN();

  float acc[3][8];
#pragma unroll
  for (int u = 0; u < 3; ++u)
#pragma unroll
    for (int e = 0; e < 8; ++e) acc[u][e] = 0.f;
  f32x4 pv[3];
#pragma unroll
  for (int i = 0; i < 24; ++i) {
    const int u = i % 3, t = i / 3;
    if (i % 12 == 0) {
#pragma unroll
      for (int uu = 0; uu < 3; ++uu)
        pv[uu] = *reinterpret_cast<const f32x4*>(lds + L_PT + w * 64 + rho[uu] * 8 + (i / 12) * 4);
    }
    const u32x4 d = i < VW ? kv[24 + i] : kv[i - VW];
    const float pp = pv[u][t % 4];
#pragma unroll
    for (int c = 0; c < 4; ++c) {
      f32x2 a2 = {acc[u][2 * c], acc[u][2 * c + 1]};
      const f32x2 v2 = {blo(d[c]), bhi(d[c])};
      a2 = __builtin_elementwise_fma(v2, f32x2{pp, pp}, a2);
      KEEP(a2);
      acc[u][2 * c] = a2.x; acc[u][2 * c + 1] = a2.y;
    }
    PIN();
  }
  STAMP(6);
  {
    float* pw = lds + L_PART + w * PART_W + lane * 8;
#pragma unroll
    for (int u = 0; u < 3; ++u) {
      *reinterpret_cast<f32x4*>(pw + u * 512) = f32x4{acc[u][0], acc[u][1], acc[u][2], acc[u][3]};
      *reinterpret_cast<f32x4*>(pw + u * 512 + 4) = f32x4{acc[u][4], acc[u][5], acc[u][6], acc[u][7]};
    }
  }
  BAR();
  if (w < 7) {
    const int col4 = tid >> 3, rg = tid & 7;
    const f32x2 ml = *reinterpret_cast<const f32x2*>(lds + L_ML + 2 * rg);
    const float M = max8(ml.x);
    const float f = __builtin_amdgcn_exp2f((ml.x - M) * LOG2E);
    if (w < 6) {
      const float* p = lds + L_PART + rg * PART_W + col4 * 4;
      f32x4 a = *reinterpret_cast<const f32x4*>(p);
#pragma unroll
      for (int r = 1; r < 8; ++r) a += *reinterpret_cast<const f32x4*>(p + r * 192);
      a *= f;
      a.x = sum8(a.x); a.y = sum8(a.y); a.z = sum8(a.z); a.w = sum8(a.w);
      if (rg < 2) {
        const int cs = col4 / 6, k = (col4 - 6 * cs) * 2 + rg;
        const u32x4 un = {__float_as_uint(rg ? a.z : a.x), tag, __float_as_uint(rg ? a.w : a.y), tag};
        __builtin_amdgcn_raw_buffer_store_b128(un, rs_ws, cs * 2048 + k * 16, WS_O + h * 16384 + s * 256, 16);
      }
    } else {
      const float L = sum8(f * ml.y);
      if (lane < 8) {
        const u32x4 un = {__float_as_uint(M), tag, __float_as_uint(L), tag};
        __builtin_amdgcn_raw_buffer_store_b128(un, rs_ws, lane * 2048 + 192, WS_O + h * 16384 + s * 256, 16);
      }
    }
  }
  STAMP(7);
  float y[8];
  {
    const u32 hid = reinterpret_cast<const u16*>(lds + L_QS)[192 + 6 * s + r6];
#pragma unroll
    for (int d = 0; d < 4; ++d) {
      y[2 * d] = dot2(wov[4][d], hid, 0.f);
      y[2 * d + 1] = dot2(wov[4][d], hid << 16, 0.f);
    }
    const int kk = lane & 7;
    const int ob = (lane >> 3) * 256;
    u32x4 u0, u1, u2;
    int spins = 0;
    bool ok;
    do {
      asm volatile("" ::: "memory");
      u0 = __builtin_amdgcn_raw_buffer_load_b128(rs_ws, ob + kk * 16, WS_O + j * 2048, 16);
      u1 = __builtin_amdgcn_raw_buffer_load_b128(rs_ws, ob + 128 + (kk & 3) * 16, WS_O + j * 2048, 16);
      u2 = __builtin_amdgcn_raw_buffer_load_b128(rs_ws, ob + 192, WS_O + j * 2048, 16);
      const u32 mn = min(min(min(u0.y, u0.w), min(u1.y, u1.w)), min(min(u2.y, u2.w), tag));
      const u32 mx = max(max(max(u0.y, u0.w), max(u1.y, u1.w)), max(max(u2.y, u2.w), tag));
      ok = __all(mn == mx);
    } while (!ok && ++spins < SPIN_CAP);
    STAMP(8);
    const float mp = __uint_as_float(u2.x);
    const float M = hi_max(mp);
    float f = __builtin_amdgcn_exp2f((mp - M) * LOG2E);
    const float L = hi_sum(f * __uint_as_float(u2.z));
    f *= __builtin_amdgcn_rcpf(L);
    const float o0 = hi_sum(f * __uint_as_float(u0.x)), o1 = hi_sum(f * __uint_as_float(u0.z));
    const float o2 = hi_sum(f * __uint_as_float(u1.x)), o3 = hi_sum(f * __uint_as_float(u1.z));
    u32* ow = reinterpret_cast<u32*>(lds + L_OW + w * 16);
    if (lane < 12) ow[lane] = lane < 8 ? pack2(o0, o1) : pack2(o2, o3);
    const u16* oh = reinterpret_cast<const u16*>(ow);
#pragma unroll
    for (int i = 0; i < 4; ++i) {
      const u32 ov = oh[6 * i + r6];
#pragma unroll
      for (int d = 0; d < 4; ++d) {
        y[2 * d] = dot2(wov[i][d], ov, y[2 * d]);
        y[2 * d + 1] = dot2(wov[i][d], ov << 16, y[2 * d + 1]);
      }
    }
  }
  {
    float* yp = lds + L_YP + w * 480;
    if (lane < 60) {
      *reinterpret_cast<f32x4*>(yp + lane * 8) = f32x4{y[0], y[1], y[2], y[3]};
      *reinterpret_cast<f32x4*>(yp + lane * 8 + 4) = f32x4{y[4], y[5], y[6], y[7]};
    }
    if (lane < 40) {
      f32x2 a = *reinterpret_cast<const f32x2*>(yp + lane * 2);
#pragma unroll
      for (int r = 1; r < 6; ++r) a += *reinterpret_cast<const f32x2*>(yp + r * 80 + lane * 2);
      const int n2 = 40 * w + lane;
      const int r = n2 / UPP, ui = n2 % UPP;
      const u32x4 un = {__float_as_uint(a.x), tag, __float_as_uint(a.y), tag};
      __builtin_amdgcn_raw_buffer_store_b128(un, rs_ws, (r * 256 * UPP + ui) * 16, WS_Y + j * UPP * 16, 16);
    }
  }
  STAMP(9);
  if (b < NRED) {
    constexpr int NU = 256 * UPP / 512;
    const u32 xo = x[b * CPR + (tid < CPR ? tid : 0)];
    float v0, v1;
    int spins = 0;
    bool ok;
    do {
      asm volatile("" ::: "memory");
      u32x4 un[NU];
#pragma unroll
      for (int k = 0; k < NU; ++k)
        un[k] = __builtin_amdgcn_raw_buffer_load_b128(rs_ws, tid * 16 + k * 8192, WS_Y + b * 256 * UPP * 16, 16);
      u32 mn = tag, mx = tag;
      v0 = 0.f; v1 = 0.f;
#pragma unroll
      for (int k = 0; k < NU; ++k) {
        mn = min(mn, min(un[k].y, un[k].w));
        mx = max(mx, max(un[k].y, un[k].w));
        v0 += __uint_as_float(un[k].x); v1 += __uint_as_float(un[k].z);
      }
      ok = __all(mn == mx);
    } while (!ok && ++spins < SPIN_CAP);
    STAMP(10);
    if (UPP <= 1) { v0 += dpp<XOR1>(v0); v1 += dpp<XOR1>(v1); }
    if (UPP <= 2) { v0 += dpp<XOR2>(v0); v1 += dpp<XOR2>(v1); }
    if (UPP <= 4) { v0 += dpp<ROR4>(v0); v1 += dpp<ROR4>(v1); }
    v0 = hi_sum(v0); v1 = hi_sum(v1);
    if (lane < UPP) *reinterpret_cast<f32x2*>(lds + L_RED + w * 16 + lane * 2) = f32x2{v0, v1};
    BAR();
    if (tid < CPR) {
      float a = 0.f;
#pragma unroll
      for (int k = 0; k < 8; ++k) a += lds[L_RED + k * 16 + tid];
      a += blo(xo);
      out[b * CPR + tid] = (u16)bfbits(a);
    }
    if (b == 0 && tid == 0) __builtin_amdgcn_raw_buffer_store_b32(tag, rs_ws, 0, 0, 16);
  }
}

extern "C" void kernel_launch(void* const* d_in, const int* in_sizes, int n_in,
                              void* d_out, int out_size, void* d_ws, size_t ws_size,
                              hipStream_t stream) {
  if (ws_size < WS_BYTES) return;
  fused_decode_kernel<<<256, 512, 0, stream>>>(
      (const u16*)d_in[0], (const u16*)d_in[1], (const u16*)d_in[2], (const u16*)d_in[3],
      (const u16*)d_in[4], (const u16*)d_in[6], (const u16*)d_in[7], (u16*)d_out, (u32*)d_ws);
}
```

```cpp
#define AMPK 5
#define AMPW 1
#define OWN 0
#define SGN 1
#include <hip/hip_runtime.h>
#include <hip/hip_bf16.h>
#include <stdint.h>

#define KE 4
#define PACE 8
#define VW 8
#define BAR0 0
#define QFLAG 1
#define NRED 160
#define CODE_KB 0

typedef unsigned int u32;
typedef unsigned short u16;
typedef unsigned long long u64;
using u32x2 = __attribute__((ext_vector_type(2))) unsigned;
using u32x4 = __attribute__((ext_vector_type(4))) unsigned;
using f32x2 = __attribute__((ext_vector_type(2))) float;
using f32x4 = __attribute__((ext_vector_type(4))) float;
using bf16x2 = __attribute__((ext_vector_type(2))) __bf16;

constexpr int D = 640, NH = 32, DH = 192, SK = 32768, WLEN = 4096;
constexpr int CPR = D / NRED;
constexpr int UPP = CPR / 2;
constexpr u32 WS_Q = 4096;
constexpr u32 WS_O = WS_Q + 256u * 2048u;
constexpr u32 WS_Y = WS_O + 256u * 2048u;
constexpr u32 WS_BYTES = WS_Y + 320u * 256u * 16u;
constexpr int SPIN_CAP = 200000;
constexpr float LOG2E = 1.4426950408889634f;

__device__ __forceinline__ float dot2(u32 a, u32 b, float acc) {
  return __builtin_amdgcn_fdot2_f32_bf16(__builtin_bit_cast(bf16x2, a), __builtin_bit_cast(bf16x2, b), acc, false);
}
template <int CTRL> __device__ __forceinline__ float dpp(float x) {
  return __builtin_bit_cast(float, __builtin_amdgcn_mov_dpp(__builtin_bit_cast(int, x), CTRL, 0xf, 0xf, true));
}
constexpr int XOR1 = 0xB1, XOR2 = 0x4E, XOR7 = 0x141, ROR8 = 0x128, ROR4 = 0x124;
__device__ __forceinline__ float sum8(float x) {
  x += dpp<XOR1>(x); x += dpp<XOR2>(x); x += dpp<XOR7>(x); return x;
}
__device__ __forceinline__ float max8(float x) {
  x = fmaxf(x, dpp<XOR1>(x)); x = fmaxf(x, dpp<XOR2>(x)); x = fmaxf(x, dpp<XOR7>(x)); return x;
}
__device__ __forceinline__ float xrow_sum(float x) {
  auto s = __builtin_amdgcn_permlane16_swap(__float_as_uint(x), __float_as_uint(x), false, false);
  x = __uint_as_float(s[0]) + __uint_as_float(s[1]);
  auto t = __builtin_amdgcn_permlane32_swap(__float_as_uint(x), __float_as_uint(x), false, false);
  return __uint_as_float(t[0]) + __uint_as_float(t[1]);
}
__device__ __forceinline__ float xrow_max(float x) {
  auto s = __builtin_amdgcn_permlane16_swap(__float_as_uint(x), __float_as_uint(x), false, false);
  x = fmaxf(__uint_as_float(s[0]), __uint_as_float(s[1]));
  auto t = __builtin_amdgcn_permlane32_swap(__float_as_uint(x), __float_as_uint(x), false, false);
  return fmaxf(__uint_as_float(t[0]), __uint_as_float(t[1]));
}
__device__ __forceinline__ float hi_sum(float x) { x += dpp<ROR8>(x); return xrow_sum(x); }
__device__ __forceinline__ float hi_max(float x) { x = fmaxf(x, dpp<ROR8>(x)); return xrow_max(x); }
__device__ __forceinline__ float wave_sum(float x) { return hi_sum(sum8(x)); }
__device__ __forceinline__ float wave_max(float x) { return hi_max(max8(x)); }
__device__ __forceinline__ u32 bfbits(float f) { return (u32)__builtin_bit_cast(u16, (__bf16)f); }
__device__ __forceinline__ float bfr(float f) { return __uint_as_float(bfbits(f) << 16); }
__device__ __forceinline__ u32 pack2(float a, float b) {
  bf16x2 p = {(__bf16)a, (__bf16)b};
  return __builtin_bit_cast(u32, p);
}
__device__ __forceinline__ float blo(u32 v) { return __uint_as_float(v << 16); }
__device__ __forceinline__ float bhi(u32 v) { return __uint_as_float(v & 0xffff0000u); }
#define BAR() asm volatile("s_waitcnt lgkmcnt(0)\n\ts_barrier" ::: "memory")
#define PIN() __builtin_amdgcn_sched_barrier(0)
#define STAMP(i) do { if ((i) == AMPK) { PIN(); tsel = (u32)__builtin_amdgcn_s_memrealtime(); PIN(); } } while (0)
#define KEEP(v) asm volatile("" : "+v"(v))
#define RSRC(p, n) __builtin_amdgcn_make_buffer_rsrc((void*)(p), 0, (n), 0x00020000)
#define WAITVM(n) __builtin_amdgcn_s_waitcnt(0x0F70 | ((n) & 15) | (((n) >> 4) << 14))

constexpr int PART_W = 1568;
constexpr int L_PART = 0;
constexpr int L_SC = L_PART + 8 * PART_W;
constexpr int L_PT = L_SC + 8 * 192;
constexpr int L_QS = L_PT + 8 * 64;
constexpr int L_ML = L_QS + 128;
constexpr int L_OW = L_ML + 16;
constexpr int L_RED = L_OW + 128;
constexpr int L_YP = L_RED + 128;
constexpr int L_END = L_YP + 8 * 480;

__global__ void __launch_bounds__(512, 2)
fused_decode_kernel(const u16* __restrict__ x, const u16* __restrict__ kc, const u16* __restrict__ vc,
                    const u16* __restrict__ wq, const u16* __restrict__ wo, const u16* __restrict__ w1,
                    const u16* __restrict__ w2, u16* __restrict__ out, u32* __restrict__ ws) {
  __shared__ __attribute__((aligned(16))) float lds[L_END];
  u64 tst64;
  asm volatile("s_memrealtime %0\n\ts_waitcnt lgkmcnt(0)" : "=s"(tst64) :: "memory");
  PIN();
  const u32 tst = (u32)tst64;
  u32 tsel = AMPK == 0 ? tst : 0u;
  const int tid = threadIdx.x;
  const int lane = tid & 63;
  const int w = __builtin_amdgcn_readfirstlane(tid >> 6);
  const int b = blockIdx.x;
  const int h = (b & 7) * 4 + (b >> 6);
  const int s = (b >> 3) & 7;
  const int j = h * 8 + s;

  const auto rs_ws = RSRC(ws, WS_BYTES);
  const auto rs_x = RSRC(x, D * 2);
  const u32x4 xa = __builtin_amdgcn_raw_buffer_load_b128(rs_x, lane * 20, 0, 0);
  STAMP(12);
  const u32 xb = __builtin_amdgcn_raw_buffer_load_b32(rs_x, lane * 20 + 16, 0, 0);
  const int row0 = 80 * s + 10 * w;
  const bool isq = lane < 48;
  const int l1 = lane < 60 ? lane - 48 : 11;
  const int r2 = isq ? (lane >= 24) : (l1 >= 6);
  const int cch = isq ? lane - 24 * r2 : l1 - 6 * r2;
  u32x4 wv[5];
  {
    const char* p = isq ? (const char*)wq + (size_t)(row0 + r2) * 12288 + h * 384 + cch * 16
                        : (const char*)w1 + (size_t)(row0 + r2) * 3072 + h * 96 + cch * 16;
    const u32 stp = isq ? 24576u : 6144u;
#pragma unroll
    for (int i = 0; i < 5; ++i) {
      wv[i] = __builtin_nontemporal_load((const u32x4*)p);
      p += stp;
      PIN();
    }
  }
  const u32 E = __builtin_amdgcn_raw_buffer_load_b32(rs_ws, 0, 0, 16);
  const size_t kvoff = ((size_t)h * SK + (SK - WLEN) + s * 512 + w * 64) * DH;
  const auto rs_k = RSRC(kc + kvoff, 64 * DH * 2);
  const auto rs_v = RSRC(vc + kvoff, 64 * DH * 2);
  u32 cpf = 0;
  if (CODE_KB > 0 && w == 7) {
    const u64 pc = __builtin_amdgcn_s_getpc() & ~(u64)255;
    cpf = __builtin_amdgcn_raw_buffer_load_b32(RSRC(pc, CODE_KB * 1024), lane * 64, ((b >> 3) % ((CODE_KB + 3) / 4)) * 4096, 0);
  }
  PIN();
  u32x4 kv[24 + VW];
#pragma unroll
  for (int i = 0; i < KE; ++i) {
    kv[i] = __builtin_amdgcn_raw_buffer_load_b128(rs_k, lane * 16, i * 1024, 2);
    PIN();
  }
  u32 xr[5];
#pragma unroll
  for (int i = 0; i < 5; ++i) xr[i] = reinterpret_cast<const u32*>(x)[row0 / 2 + i];

  {
    float xv[10];
    xv[0] = blo(xa.x); xv[1] = bhi(xa.x); xv[2] = blo(xa.y); xv[3] = bhi(xa.y);
    xv[4] = blo(xa.z); xv[5] = bhi(xa.z); xv[6] = blo(xa.w); xv[7] = bhi(xa.w);
    xv[8] = blo(xb); xv[9] = bhi(xb);
    float sm = ((xv[0] + xv[1]) + (xv[2] + xv[3])) + ((xv[4] + xv[5]) + (xv[6] + xv[7])) + (xv[8] + xv[9]);
    const float mean = wave_sum(sm) * (1.0f / D);
    float sq = 0.f;
#pragma unroll
    for (int e = 0; e < 10; ++e) { const float d = xv[e] - mean; sq += d * d; }
    const float var = wave_sum(sq) * (1.0f / D);
    const float mb = bfr(mean);
    const float den = bfr(__builtin_amdgcn_sqrtf(bfr(bfr(var) + 1e-5f)));
    const float rinv = 1.0f / den;
    STAMP(1);
#pragma unroll
    for (int i = 0; i < 5; ++i) asm volatile("" : "+s"(xr[i]));
    float acc[8];
#pragma unroll
    for (int e = 0; e < 8; ++e) acc[e] = 0.f;
#pragma unroll
    for (int i = 0; i < 5; ++i) {
      const float v = r2 ? bhi(xr[i]) : blo(xr[i]);
      const u32 xl = bfbits(bfr(v - mb) * rinv), xh = xl << 16;
#pragma unroll
      for (int d = 0; d < 4; ++d) {
        acc[2 * d] = dot2(wv[i][d], xl, acc[2 * d]);
        acc[2 * d + 1] = dot2(wv[i][d], xh, acc[2 * d + 1]);
      }
    }
    if (lane < 60) {
      float* p = lds + L_PART + (2 * w + r2) * 244 + (isq ? 0 : 192) + cch * 8;
      *reinterpret_cast<f32x4*>(p) = f32x4{acc[0], acc[1], acc[2], acc[3]};
      *reinterpret_cast<f32x4*>(p + 4) = f32x4{acc[4], acc[5], acc[6], acc[7]};
    }
  }
  const u32 tag = E + 1u;
  volatile u32* qflag = reinterpret_cast<volatile u32*>(lds + L_QS + 124);
  if (QFLAG && tid == 64) *qflag = 0u;
  KEEP(cpf);
  if (b == 0 && tid == 0) { u32x2 g = {tst, tag}; __builtin_amdgcn_raw_buffer_store_b64(g, rs_ws, 64, 0, 16); }
  STAMP(2);
  BAR();

  if (w == 0) {
    __builtin_amdgcn_s_setprio(3);
    const int t = lane < 60 ? lane : 59;
    const float* p = lds + L_PART + t * 4;
    f32x4 a = *reinterpret_cast<const f32x4*>(p);
#pragma unroll
    for (int r = 1; r < 16; ++r) a += *reinterpret_cast<const f32x4*>(p + r * 244);
    if (lane < 60) {
      const u32x4 u0 = {__float_as_uint(a.x), tag, __float_as_uint(a.y), tag};
      const u32x4 u1 = {__float_as_uint(a.z), tag, __float_as_uint(a.w), tag};
      __builtin_amdgcn_raw_buffer_store_b128(u0, rs_ws, lane * 32, WS_Q + j * 2048, 16);
      __builtin_amdgcn_raw_buffer_store_b128(u1, rs_ws, lane * 32 + 16, WS_Q + j * 2048, 16);
    }
    STAMP(3);
    f32x4 sum;
    int spins = 0;
    bool ok;
    do {
      asm volatile("" ::: "memory");
      u32x4 un[16];
#pragma unroll
      for (int k = 0; k < 16; ++k)
        un[k] = __builtin_amdgcn_raw_buffer_load_b128(rs_ws, t * 16 + (k & 1) * 960 + (k >> 1) * 2048, WS_Q + h * 16384, 16);
      u32 mn = tag, mx = tag;
      sum = f32x4{0.f, 0.f, 0.f, 0.f};
#pragma unroll
      for (int k = 0; k < 16; ++k) {
        mn = min(mn, min(un[k].y, un[k].w));
        mx = max(mx, max(un[k].y, un[k].w));
        sum[2 * (k & 1)] += __uint_as_float(un[k].x);
        sum[2 * (k & 1) + 1] += __uint_as_float(un[k].z);
      }
      ok = __all(mn == mx);
    } while (!ok && ++spins < SPIN_CAP);
    if (t >= 36) { sum.z = fmaxf(sum.z, 0.f); sum.w = fmaxf(sum.w, 0.f); }
    u32* qs = reinterpret_cast<u32*>(lds + L_QS);
    qs[t] = pack2(sum.x, sum.y);
    qs[60 + t] = pack2(sum.z, sum.w);
    STAMP(4);
    asm volatile("s_waitcnt lgkmcnt(0)" ::: "memory");
    *qflag = 1u;
  }
#pragma unroll
  for (int i = KE; i < 24 + VW; ++i) {
    if (w != 0) WAITVM(PACE - 1);
    if (i < 24) kv[i] = __builtin_amdgcn_raw_buffer_load_b128(rs_k, lane * 16, i * 1024, 2);
    else kv[i] = __builtin_amdgcn_raw_buffer_load_b128(rs_v, lane * 16, (i - 24) * 1024, 2);
    PIN();
  }
  int rho[3], c8[3];
#pragma unroll
  for (int u = 0; u < 3; ++u) { const int g = 64 * u + lane; rho[u] = g / 24; c8[u] = g % 24; }
  const int r6 = (lane / 10) > 5 ? 5 : (lane / 10);
  const int ch = lane - 10 * (lane / 10);
  const auto rs_wo = RSRC(wo + (size_t)(h * DH + 24 * s) * D + 80 * w, 23 * 1280 + 160);
  const auto rs_w2 = RSRC(w2 + (size_t)(48 * h + 6 * s) * D + 80 * w, 5 * 1280 + 160);
  const int wo_voff = r6 * 1280 + ch * 16;
  if (w != 0) {
    STAMP(3);
    int spins = 0;
    while (*qflag == 0u && ++spins < 100 * SPIN_CAP) __builtin_amdgcn_s_sleep(1);
    asm volatile("" ::: "memory");
    STAMP(4);
  }
  u32x4 q[3];
#pragma unroll
  for (int u = 0; u < 3; ++u) q[u] = *reinterpret_cast<const u32x4*>(lds + L_QS + c8[u] * 4);

  u32x4 wov[5];
  float sv[3] = {0.f, 0.f, 0.f};
#pragma unroll
  for (int i = 0; i < 24; ++i) {
    const u32x4 d = kv[i];
    const int u = i % 3;
    float a = dot2(d.x, q[u].x, 0.f);
    float c = dot2(d.z, q[u].z, 0.f);
    a = dot2(d.y, q[u].y, a);
    c = dot2(d.w, q[u].w, c);
    const int n = i + VW;
    if (n < 24) kv[i] = __builtin_amdgcn_raw_buffer_load_b128(rs_v, lane * 16, n * 1024, 2);
    else if (n < 28) wov[n - 24] = __builtin_amdgcn_raw_buffer_load_b128(rs_wo, wo_voff, (n - 24) * 7680, 2);
    else if (n == 28) wov[4] = __builtin_amdgcn_raw_buffer_load_b128(rs_w2, wo_voff, 0, 2);
    a = sum8(a + c);
    sv[i / 8] = ((lane & 7) == (i & 7)) ? a : sv[i / 8];
    PIN();
  }
  STAMP(5);
  {
    float* sc = lds + L_SC + w * 192;
#pragma unroll
    for (int a = 0; a < 3; ++a) sc[(8 * a + (lane & 7)) * 8 + (lane >> 3)] = sv[a];
    const float raw = (sc[3 * lane] + sc[3 * lane + 1]) + sc[3 * lane + 2];
    const float sco = bfr(bfr(raw) * 0.07216878364870322f);
    const float mw = wave_max(sco);
    const float p = __builtin_amdgcn_exp2f((sco - mw) * LOG2E);
    lds[L_PT + w * 64 + (lane & 7) * 8 + (lane >> 3)] = p;
    const float lw = wave_sum(p);
    if (lane == 0) *reinterpret_cast<f32x2*>(lds + L_ML + 2 * w) = f32x2{mw, lw};
  }
  PIN();

  float acc[3][8];
#pragma unroll
  for (int u = 0; u < 3; ++u)
#pragma unroll
    for (int e = 0; e < 8; ++e) acc[u][e] = 0.f;
  f32x4 pv[3];
#pragma unroll
  for (int i = 0; i < 24; ++i) {
    const int u = i % 3, t = i / 3;
    if (i % 12 == 0) {
#pragma unroll
      for (int uu = 0; uu < 3; ++uu)
        pv[uu] = *reinterpret_cast<const f32x4*>(lds + L_PT + w * 64 + rho[uu] * 8 + (i / 12) * 4);
    }
    const u32x4 d = i < VW ? kv[24 + i] : kv[i - VW];
    const float pp = pv[u][t % 4];
#pragma unroll
    for (int c = 0; c < 4; ++c) {
      f32x2 a2 = {acc[u][2 * c], acc[u][2 * c + 1]};
      const f32x2 v2 = {blo(d[c]), bhi(d[c])};
      a2 = __builtin_elementwise_fma(v2, f32x2{pp, pp}, a2);
      KEEP(a2);
      acc[u][2 * c] = a2.x; acc[u][2 * c + 1] = a2.y;
    }
    PIN();
  }
  STAMP(6);
  {
    float* pw = lds + L_PART + w * PART_W + lane * 8;
#pragma unroll
    for (int u = 0; u < 3; ++u) {
      *reinterpret_cast<f32x4*>(pw + u * 512) = f32x4{acc[u][0], acc[u][1], acc[u][2], acc[u][3]};
      *reinterpret_cast<f32x4*>(pw + u * 512 + 4) = f32x4{acc[u][4], acc[u][5], acc[u][6], acc[u][7]};
    }
  }
  BAR();
  if (w < 7) {
    const int col4 = tid >> 3, rg = tid & 7;
    const f32x2 ml = *reinterpret_cast<const f32x2*>(lds + L_ML + 2 * rg);
    const float M = max8(ml.x);
    const float f = __builtin_amdgcn_exp2f((ml.x - M) * LOG2E);
    if (w < 6) {
      const float* p = lds + L_PART + rg * PART_W + col4 * 4;
      f32x4 a = *reinterpret_cast<const f32x4*>(p);
#pragma unroll
      for (int r = 1; r < 8; ++r) a += *reinterpret_cast<const f32x4*>(p + r * 192);
      a *= f;
      a.x = sum8(a.x); a.y = sum8(a.y); a.z = sum8(a.z); a.w = sum8(a.w);
      if (rg < 2) {
        const int cs = col4 / 6, k = (col4 - 6 * cs) * 2 + rg;
        const u32x4 un = {__float_as_uint(rg ? a.z : a.x), tag, __float_as_uint(rg ? a.w : a.y), tag};
        __builtin_amdgcn_raw_buffer_store_b128(un, rs_ws, cs * 2048 + k * 16, WS_O + h * 16384 + s * 256, 16);
      }
    } else {
      const float L = sum8(f * ml.y);
      if (lane < 8) {
        const u32x4 un = {__float_as_uint(M), tag, __float_as_uint(L), tag};
        __builtin_amdgcn_raw_buffer_store_b128(un, rs_ws, lane * 2048 + 192, WS_O + h * 16384 + s * 256, 16);
      }
    }
  }
  STAMP(7);
  float y[8];
  {
    const u32 hid = reinterpret_cast<const u16*>(lds + L_QS)[192 + 6 * s + r6];
#pragma unroll
    for (int d = 0; d < 4; ++d) {
      y[2 * d] = dot2(wov[4][d], hid, 0.f);
      y[2 * d + 1] = dot2(wov[4][d], hid << 16, 0.f);
    }
    const int kk = lane & 7;
    const int ob = (lane >> 3) * 256;
    u32x4 u0, u1, u2;
    int spins = 0;
    bool ok;
    do {
      asm volatile("" ::: "memory");
      u0 = __builtin_amdgcn_raw_buffer_load_b128(rs_ws, ob + kk * 16, WS_O + j * 2048, 16);
      u1 = __builtin_amdgcn_raw_buffer_load_b128(rs_ws, ob + 128 + (kk & 3) * 16, WS_O + j * 2048, 16);
      u2 = __builtin_amdgcn_raw_buffer_load_b128(rs_ws, ob + 192, WS_O + j * 2048, 16);
      const u32 mn = min(min(min(u0.y, u0.w), min(u1.y, u1.w)), min(min(u2.y, u2.w), tag));
      const u32 mx = max(max(max(u0.y, u0.w), max(u1.y, u1.w)), max(max(u2.y, u2.w), tag));
      ok = __all(mn == mx);
    } while (!ok && ++spins < SPIN_CAP);
    STAMP(8);
    const float mp = __uint_as_float(u2.x);
    const float M = hi_max(mp);
    float f = __builtin_amdgcn_exp2f((mp - M) * LOG2E);
    const float L = hi_sum(f * __uint_as_float(u2.z));
    f *= __builtin_amdgcn_rcpf(L);
    const float o0 = hi_sum(f * __uint_as_float(u0.x)), o1 = hi_sum(f * __uint_as_float(u0.z));
    const float o2 = hi_sum(f * __uint_as_float(u1.x)), o3 = hi_sum(f * __uint_as_float(u1.z));
    u32* ow = reinterpret_cast<u32*>(lds + L_OW + w * 16);
    if (lane < 12) ow[lane] = lane < 8 ? pack2(o0, o1) : pack2(o2, o3);
    const u16* oh = reinterpret_cast<const u16*>(ow);
#pragma unroll
    for (int i = 0; i < 4; ++i) {
      const u32 ov = oh[6 * i + r6];
#pragma unroll
      for (int d = 0; d < 4; ++d) {
        y[2 * d] = dot2(wov[i][d], ov, y[2 * d]);
        y[2 * d + 1] = dot2(wov[i][d], ov << 16, y[2 * d + 1]);
      }
    }
  }
  {
    float* yp = lds + L_YP + w * 480;
    if (lane < 60) {
      *reinterpret_cast<f32x4*>(yp + lane * 8) = f32x4{y[0], y[1], y[2], y[3]};
      *reinterpret_cast<f32x4*>(yp + lane * 8 + 4) = f32x4{y[4], y[5], y[6], y[7]};
    }
    if (lane < 40) {
      f32x2 a = *reinterpret_cast<const f32x2*>(yp + lane * 2);
#pragma unroll
      for (int r = 1; r < 6; ++r) a += *reinterpret_cast<const f32x2*>(yp + r * 80 + lane * 2);
      const int n2 = 40 * w + lane;
      const int r = n2 / UPP, ui = n2 % UPP;
      const u32x4 un = {__float_as_uint(a.x), tag, __float_as_uint(a.y), tag};
      __builtin_amdgcn_raw_buffer_store_b128(un, rs_ws, (r * 256 * UPP + ui) * 16, WS_Y + j * UPP * 16, 16);
    }
  }
  STAMP(9);
  if (b < NRED) {
    constexpr int NU = 256 * UPP / 512;
    const u32 xo = x[b * CPR + (tid < CPR ? tid : 0)];
    float v0, v1;
    int spins = 0;
    bool ok;
    do {
      asm volatile("" ::: "memory");
      u32x4 un[NU];
#pragma unroll
      for (int k = 0; k < NU; ++k)
        un[k] = __builtin_amdgcn_raw_buffer_load_b128(rs_ws, tid * 16 + k * 8192, WS_Y + b * 256 * UPP * 16, 16);
      u32 mn = tag, mx = tag;
      v0 = 0.f; v1 = 0.f;
#pragma unroll
      for (int k = 0; k < NU; ++k) {
        mn = min(mn, min(un[k].y, un[k].w));
        mx = max(mx, max(un[k].y, un[k].w));
        v0 += __uint_as_float(un[k].x); v1 += __uint_as_float(un[k].z);
      }
      ok = __all(mn == mx);
    } while (!ok && ++spins < SPIN_CAP);
    STAMP(10);
    if (UPP <= 1) { v0 += dpp<XOR1>(v0); v1 += dpp<XOR1>(v1); }
    if (UPP <= 2) { v0 += dpp<XOR2>(v0); v1 += dpp<XOR2>(v1); }
    if (UPP <= 4) { v0 += dpp<ROR4>(v0); v1 += dpp<ROR4>(v1); }
    v0 = hi_sum(v0); v1 = hi_sum(v1);
    if (lane < UPP) *reinterpret_cast<f32x2*>(lds + L_RED + w * 16 + lane * 2) = f32x2{v0, v1};
    BAR();
    if (tid < CPR) {
      float a = 0.f;
#pragma unroll
      for (int k = 0; k < 8; ++k) a += lds[L_RED + k * 16 + tid];
      a += blo(xo);
      out[b * CPR + tid] = (u16)bfbits(a);
    }
    if (b == 0 && tid == 0) __builtin_amdgcn_raw_buffer_store_b32(tag, rs_ws, 0, 0, 16);
  }
  STAMP(11);
  if (tsel != 0u && (AMPW)) {
    u32x2 g; int sp = 0;
    do { asm volatile("" ::: "memory"); g = __builtin_amdgcn_raw_buffer_load_b64(rs_ws, 64, 0, 16); } while (g.y != tag && ++sp < SPIN_CAP);
    const u32 t0 = OWN ? tst : __builtin_amdgcn_readfirstlane(g.x);
    int d = SGN * (int)(tsel - t0) + 200;
    if (d < 0) d = 0;
    if (d > 10000) d = 10000;
    const u32 ta = (u32)__builtin_amdgcn_s_memrealtime();
    while ((u32)__builtin_amdgcn_s_memrealtime() - ta < (u32)d * 16u) __builtin_amdgcn_s_sleep(2);
  }
}

extern "C" void kernel_launch(void* const* d_in, const int* in_sizes, int n_in,
                              void* d_out, int out_size, void* d_ws, size_t ws_size,
                              hipStream_t stream) {
  if (ws_size < WS_BYTES) return;
  fused_decode_kernel<<<256, 512, 0, stream>>>(
      (const u16*)d_in[0], (const u16*)d_in[1], (const u16*)d_in[2], (const u16*)d_in[3],
      (const u16*)d_in[4], (const u16*)d_in[6], (const u16*)d_in[7], (u16*)d_out, (u32*)d_ws);
}
```

```cpp
#include <hip/hip_runtime.h>
#include <hip/hip_bf16.h>
#include <stdint.h>

#define PACE 6
#define QS 33
#define SPS 3
#define PB 2
#define OTHR 3
#define NRED 160
#define WARM 1

typedef unsigned int u32;
typedef unsigned short u16;
typedef unsigned long long u64;
using u32x2 = __attribute__((ext_vector_type(2))) unsigned;
using u32x4 = __attribute__((ext_vector_type(4))) unsigned;
using f32x2 = __attribute__((ext_vector_type(2))) float;
using f32x4 = __attribute__((ext_vector_type(4))) float;
using bf16x2 = __attribute__((ext_vector_type(2))) __bf16;

constexpr int D = 640, NH = 32, DH = 192, SK = 32768, WLEN = 4096;
constexpr int NL = 27;
constexpr int NSLOT = 2 * NL + 5;
constexpr int CPR = D / NRED;
constexpr int UPP = CPR / 2;
constexpr u32 WS_Q = 4096;
constexpr u32 WS_O = WS_Q + 256u * 2048u;
constexpr u32 WS_Y = WS_O + 256u * 2048u;
constexpr u32 WS_BYTES = WS_Y + 320u * 256u * 16u;
constexpr int SPIN_CAP = 200000;
constexpr float LOG2E = 1.4426950408889634f;
constexpr float SCALE = 0.07216878364870322f;

__device__ __forceinline__ float dot2(u32 a, u32 b, float acc) {
  return __builtin_amdgcn_fdot2_f32_bf16(__builtin_bit_cast(bf16x2, a), __builtin_bit_cast(bf16x2, b), acc, false);
}
template <int CTRL> __device__ __forceinline__ float dpp(float x) {
  return __builtin_bit_cast(float, __builtin_amdgcn_mov_dpp(__builtin_bit_cast(int, x), CTRL, 0xf, 0xf, true));
}
constexpr int XOR1 = 0xB1, XOR2 = 0x4E, XOR7 = 0x141, ROR8 = 0x128, ROR4 = 0x124;
__device__ __forceinline__ float sum8(float x) {
  x += dpp<XOR1>(x); x += dpp<XOR2>(x); x += dpp<XOR7>(x); return x;
}
__device__ __forceinline__ float max8(float x) {
  x = fmaxf(x, dpp<XOR1>(x)); x = fmaxf(x, dpp<XOR2>(x)); x = fmaxf(x, dpp<XOR7>(x)); return x;
}
__device__ __forceinline__ float xrow_sum(float x) {
  auto s = __builtin_amdgcn_permlane16_swap(__float_as_uint(x), __float_as_uint(x), false, false);
  x = __uint_as_float(s[0]) + __uint_as_float(s[1]);
  auto t = __builtin_amdgcn_permlane32_swap(__float_as_uint(x), __float_as_uint(x), false, false);
  return __uint_as_float(t[0]) + __uint_as_float(t[1]);
}
__device__ __forceinline__ float xrow_max(float x) {
  auto s = __builtin_amdgcn_permlane16_swap(__float_as_uint(x), __float_as_uint(x), false, false);
  x = fmaxf(__uint_as_float(s[0]), __uint_as_float(s[1]));
  auto t = __builtin_amdgcn_permlane32_swap(__float_as_uint(x), __float_as_uint(x), false, false);
  return fmaxf(__uint_as_float(t[0]), __uint_as_float(t[1]));
}
__device__ __forceinline__ float hi_sum(float x) { x += dpp<ROR8>(x); return xrow_sum(x); }
__device__ __forceinline__ float hi_max(float x) { x = fmaxf(x, dpp<ROR8>(x)); return xrow_max(x); }
__device__ __forceinline__ float wave_sum(float x) { return hi_sum(sum8(x)); }
__device__ __forceinline__ float wave_max(float x) { return hi_max(max8(x)); }
__device__ __forceinline__ u32 bfbits(float f) { return (u32)__builtin_bit_cast(u16, (__bf16)f); }
__device__ __forceinline__ float bfr(float f) { return __uint_as_float(bfbits(f) << 16); }
__device__ __forceinline__ u32 pack2(float a, float b) {
  bf16x2 p = {(__bf16)a, (__bf16)b};
  return __builtin_bit_cast(u32, p);
}
__device__ __forceinline__ float blo(u32 v) { return __uint_as_float(v << 16); }
__device__ __forceinline__ float bhi(u32 v) { return __uint_as_float(v & 0xffff0000u); }
__device__ __forceinline__ float ex2(float v) { return __builtin_amdgcn_exp2f(v); }
#define BAR() asm volatile("s_waitcnt lgkmcnt(0)\n\ts_barrier" ::: "memory")
#define PIN() __builtin_amdgcn_sched_barrier(0)
#define CBAR() asm volatile("" ::: "memory")
#define STAMP(i)
#define KEEP(v) asm volatile("" : "+v"(v))
#define RSRC(p, n) __builtin_amdgcn_make_buffer_rsrc((void*)(p), 0, (n), 0x00020000)
#define LDW(p) __hip_atomic_load((p), __ATOMIC_RELAXED, __HIP_MEMORY_SCOPE_WORKGROUP)
#define STW(p, v) __hip_atomic_store((p), (v), __ATOMIC_RELAXED, __HIP_MEMORY_SCOPE_WORKGROUP)

#define WAITVM(n) __builtin_amdgcn_s_waitcnt(0x0F70 | ((n) & 15) | (((n) >> 4) << 14))

constexpr int PART_W = 1568;
constexpr int L_PART = 0;
constexpr int L_SC = L_PART + 8 * PART_W;
constexpr int L_PT = L_SC + 8 * 256;
constexpr int L_QS = L_PT + 8 * 96;
constexpr int L_FL = L_QS + 128;
constexpr int L_SP = L_FL + 32;
constexpr int L_RED = L_SP + 8 * 200;
constexpr int L_YP = L_RED + 128;
constexpr int L_END = L_YP + 8 * 480;

struct Sched { int hi[NSLOT + 1]; };
constexpr int S3 = QS + NL / SPS;
constexpr Sched make_sched() {
  Sched t{};
  int j = 0;
  for (int n = S3; n < NSLOT; ++n) {
    int burst = 0;
    while (j < NL && burst < PB && n - (NL + j) >= PACE) { ++j; ++burst; }
    t.hi[n] = j;
  }
  t.hi[NSLOT] = NL;
  return t;
}
constexpr Sched SCH = make_sched();
static_assert(QS >= NL && S3 <= NSLOT && NL % SPS == 0, "schedule");

__device__ __forceinline__ float score_step(const u32x4 d, const u32x4 q) {
  float a = dot2(d.x, q.x, 0.f);
  float c = dot2(d.z, q.z, 0.f);
  a = dot2(d.y, q.y, a);
  c = dot2(d.w, q.w, c);
  return sum8(a + c);
}
__device__ __forceinline__ void pv_step(const u32x4 d, const float pp, f32x2 (&acc)[4]) {
#pragma unroll
  for (int c = 0; c < 4; ++c) {
    acc[c] = __builtin_elementwise_fma(f32x2{blo(d[c]), bhi(d[c])}, f32x2{pp, pp}, acc[c]);
    KEEP(acc[c]);
  }
}

__global__ void __launch_bounds__(512, 2)
fused_decode_kernel(const u16* __restrict__ x, const u16* __restrict__ kc, const u16* __restrict__ vc,
                    const u16* __restrict__ wq, const u16* __restrict__ wo, const u16* __restrict__ w1,
                    const u16* __restrict__ w2, u16* __restrict__ out, u32* __restrict__ ws) {
  __shared__ __attribute__((aligned(16))) float lds[L_END];
  const int tid = threadIdx.x;
  const int lane = tid & 63;
  const int w = __builtin_amdgcn_readfirstlane(tid >> 6);
  const int b = blockIdx.x;
  const int h = b >> 3;
  const int s = b & 7;

  const auto rs_ws = RSRC(ws, WS_BYTES);
  const size_t kvoff = ((size_t)h * SK + (SK - WLEN) + s * 512) * DH;
  const auto rs_k = RSRC(kc + kvoff, 512 * DH * 2);
  const auto rs_v = RSRC(vc + kvoff, 512 * DH * 2);
  const int r6 = (lane / 10) > 5 ? 5 : (lane / 10);
  const int ch = lane - 10 * (lane / 10);
  const auto rs_wo = RSRC(wo + (size_t)(h * DH + 24 * s) * D + 80 * w, 23 * 1280 + 160);
  const auto rs_w2 = RSRC(w2 + (size_t)(48 * h + 6 * s) * D + 80 * w, 5 * 1280 + 160);
  const int wo_voff = r6 * 1280 + ch * 16;
  u32* const fl = reinterpret_cast<u32*>(lds + L_FL);
  u32* const qs = reinterpret_cast<u32*>(lds + L_QS);
  float* const sc = lds + L_SC + w * 256;
  float* const pt = lds + L_PT + w * 96;
  float* const pw = lds + L_PART + w * PART_W;
  int rho[3], c8[3];
#pragma unroll
  for (int u = 0; u < 3; ++u) { const int g = 64 * u + lane; rho[u] = g / 24; c8[u] = g % 24; }

  u32x4 wov[5];
  f32x2 acc[3][4];
#pragma unroll
  for (int u = 0; u < 3; ++u)
#pragma unroll
    for (int c = 0; c < 4; ++c) acc[u][c] = f32x2{0.f, 0.f};
  float mw, lw;
  u32 tag;

  if (w == 0) {
    __builtin_amdgcn_s_setprio(3);
    const auto rs_x = RSRC(x, D * 2);
    const u32x4 xa = __builtin_amdgcn_raw_buffer_load_b128(rs_x, lane * 20, 0, 0);
    const u32 xb = __builtin_amdgcn_raw_buffer_load_b32(rs_x, lane * 20 + 16, 0, 0);
    const u32 xp = __builtin_amdgcn_raw_buffer_load_b32(rs_x, (lane < 40 ? lane : 0) * 4, s * 160, 0);
    const bool isq = lane < 48;
    const int l1 = lane < 60 ? lane - 48 : 11;
    const int r2 = isq ? (lane >= 24) : (l1 >= 6);
    const int cch = isq ? lane - 24 * r2 : l1 - 6 * r2;
    u32x4 wv[40];
    {
      const char* p = isq ? (const char*)wq + (size_t)(80 * s + r2) * 12288 + h * 384 + cch * 16
                          : (const char*)w1 + (size_t)(80 * s + r2) * 3072 + h * 96 + cch * 16;
      const u32 stp = isq ? 24576u : 6144u;
#pragma unroll
      for (int i = 0; i < 40; ++i) {
        wv[i] = __builtin_nontemporal_load((const u32x4*)p);
        p += stp;
        PIN();
      }
    }
    const u32 E = __builtin_amdgcn_raw_buffer_load_b32(rs_ws, 0, 0, 16);
    if (lane < 32) fl[lane] = 0u;
    PIN();
    BAR();
    float xv[10];
    xv[0] = blo(xa.x); xv[1] = bhi(xa.x); xv[2] = blo(xa.y); xv[3] = bhi(xa.y);
    xv[4] = blo(xa.z); xv[5] = bhi(xa.z); xv[6] = blo(xa.w); xv[7] = bhi(xa.w);
    xv[8] = blo(xb); xv[9] = bhi(xb);
    const float sm = ((xv[0] + xv[1]) + (xv[2] + xv[3])) + ((xv[4] + xv[5]) + (xv[6] + xv[7])) + (xv[8] + xv[9]);
    const float mean = wave_sum(sm) * (1.0f / D);
    float sq = 0.f;
#pragma unroll
    for (int e = 0; e < 10; ++e) { const float d = xv[e] - mean; sq += d * d; }
    const float var = wave_sum(sq) * (1.0f / D);
    const float mb = bfr(mean);
    const float den = bfr(__builtin_amdgcn_sqrtf(bfr(bfr(var) + 1e-5f)));
    const float rinv = 1.0f / den;
    u16* xn = reinterpret_cast<u16*>(pw + 512);
    if (lane < 40) {
      xn[lane] = (u16)bfbits(bfr(blo(xp) - mb) * rinv);
      xn[40 + lane] = (u16)bfbits(bfr(bhi(xp) - mb) * rinv);
    }
    u32x4 xs[5];
#pragma unroll
    for (int i = 0; i < 5; ++i) xs[i] = *reinterpret_cast<const u32x4*>(xn + r2 * 40 + i * 8);
    STAMP(1);
    float a8[8];
#pragma unroll
    for (int e = 0; e < 8; ++e) a8[e] = 0.f;
#pragma unroll
    for (int i = 0; i < 40; ++i) {
      const u32 v = xs[i / 8][(i / 2) % 4];
      const u32 xl = (i & 1) ? v >> 16 : v & 0xffffu;
      const u32 xh = (i & 1) ? v & 0xffff0000u : v << 16;
#pragma unroll
      for (int d = 0; d < 4; ++d) {
        a8[2 * d] = dot2(wv[i][d], xl, a8[2 * d]);
        a8[2 * d + 1] = dot2(wv[i][d], xh, a8[2 * d + 1]);
        KEEP(a8[2 * d]); KEEP(a8[2 * d + 1]);
      }
      PIN();
    }
    if (lane < 60) {
      float* p = pw + r2 * 244 + (isq ? 0 : 192) + cch * 8;
      *reinterpret_cast<f32x4*>(p) = f32x4{a8[0], a8[1], a8[2], a8[3]};
      *reinterpret_cast<f32x4*>(p + 4) = f32x4{a8[4], a8[5], a8[6], a8[7]};
    }
    tag = E + 1u;
    STAMP(2);
    const int t = lane < 60 ? lane : 59;
    const f32x4 a = *reinterpret_cast<const f32x4*>(pw + t * 4) + *reinterpret_cast<const f32x4*>(pw + 244 + t * 4);
    if (lane < 60) {
      const u32x4 u0 = {__float_as_uint(a.x), tag, __float_as_uint(a.y), tag};
      const u32x4 u1 = {__float_as_uint(a.z), tag, __float_as_uint(a.w), tag};
      __builtin_amdgcn_raw_buffer_store_b128(u0, rs_ws, lane * 32, WS_Q + b * 2048, 16);
      __builtin_amdgcn_raw_buffer_store_b128(u1, rs_ws, lane * 32 + 16, WS_Q + b * 2048, 16);
    }
    STAMP(3);
    u32x4 kr[3], vr[3];
#pragma unroll
    for (int i = 0; i < 3; ++i) kr[i] = __builtin_amdgcn_raw_buffer_load_b128(rs_k, lane * 16, i * 1024, 2);
#pragma unroll
    for (int i = 0; i < 3; ++i) vr[i] = __builtin_amdgcn_raw_buffer_load_b128(rs_v, lane * 16, i * 1024, 2);
    PIN();
    f32x4 sum;
    int spins = 0;
    bool ok;
    do {
      CBAR();
      u32x4 un[16];
#pragma unroll
      for (int k = 0; k < 16; ++k)
        un[k] = __builtin_amdgcn_raw_buffer_load_b128(rs_ws, t * 16 + (k & 1) * 960 + (k >> 1) * 2048, WS_Q + h * 16384, 16);
      u32 mn = tag, mx = tag;
      sum = f32x4{0.f, 0.f, 0.f, 0.f};
#pragma unroll
      for (int k = 0; k < 16; ++k) {
        mn = min(mn, min(un[k].y, un[k].w));
        mx = max(mx, max(un[k].y, un[k].w));
        sum[2 * (k & 1)] += __uint_as_float(un[k].x);
        sum[2 * (k & 1) + 1] += __uint_as_float(un[k].z);
      }
      ok = __all(mn == mx);
    } while (!ok && ++spins < SPIN_CAP);
    if (t >= 36) { sum.z = fmaxf(sum.z, 0.f); sum.w = fmaxf(sum.w, 0.f); }
    qs[t] = pack2(sum.x, sum.y);
    qs[60 + t] = pack2(sum.z, sum.w);
    qs[120] = tag;
    asm volatile("s_waitcnt lgkmcnt(0)" ::: "memory");
    if (lane == 0) STW(fl + 24, 1u);
    STAMP(4);
    __builtin_amdgcn_s_setprio(0);
    wov[4] = __builtin_amdgcn_raw_buffer_load_b128(rs_w2, wo_voff, 0, 2);
#pragma unroll
    for (int i = 0; i < 4; ++i) wov[i] = __builtin_amdgcn_raw_buffer_load_b128(rs_wo, wo_voff, i * 7680, 2);
    float sv = 0.f;
#pragma unroll
    for (int u = 0; u < 3; ++u) {
      const float a1 = score_step(kr[u], *reinterpret_cast<const u32x4*>(qs + c8[u] * 4));
      sv = ((lane & 7) == u) ? a1 : sv;
    }
    sc[(lane & 7) * 8 + (lane >> 3)] = sv;
    const int lk = lane & 7;
    const float raw = (sc[3 * lk] + sc[3 * lk + 1]) + sc[3 * lk + 2];
    const float sco = bfr(bfr(raw) * SCALE);
    mw = max8(sco);
    const float p = ex2((sco - mw) * LOG2E);
    pt[lk] = p;
    lw = sum8(p);
    STAMP(5);
#pragma unroll
    for (int u = 0; u < 3; ++u) pv_step(vr[u], pt[rho[u]], acc[u]);
  } else {
    const int soff = (8 + (w - 1) * 72) * (DH * 2);
    u32x4 kr[NL], vr[NL];
#define ISSUE(n) do { \
      if ((n) < NL) kr[(n) < NL ? (n) : 0] = __builtin_amdgcn_raw_buffer_load_b128(rs_k, lane * 16, soff + (n) * 1024, 2); \
      else if ((n) < 2 * NL) vr[((n) >= NL && (n) < 2 * NL) ? (n) - NL : 0] = __builtin_amdgcn_raw_buffer_load_b128(rs_v, lane * 16, soff + ((n) - NL) * 1024, 2); \
      else if ((n) == 2 * NL) wov[4] = __builtin_amdgcn_raw_buffer_load_b128(rs_w2, wo_voff, 0, 2); \
      else wov[(n) > 2 * NL ? (n) - 2 * NL - 1 : 0] = __builtin_amdgcn_raw_buffer_load_b128(rs_wo, wo_voff, ((n) - 2 * NL - 1) * 7680, 2); \
      PIN(); } while (0)
#pragma unroll
    for (int n = 0; n < PACE; ++n) ISSUE(n);
    asm volatile("s_barrier" ::: "memory");
#pragma unroll
    for (int n = PACE; n < QS; ++n) { WAITVM(PACE - 1); ISSUE(n); }
    STAMP(3);
    {
      int spins = 0;
      while (LDW(fl + 24) == 0u && ++spins < 100 * SPIN_CAP) __builtin_amdgcn_s_sleep(1);
      CBAR();
    }
    STAMP(4);
    u32x4 q[3];
#pragma unroll
    for (int u = 0; u < 3; ++u) q[u] = *reinterpret_cast<const u32x4*>(qs + c8[u] * 4);
    tag = qs[120];
    float sv[4] = {0.f, 0.f, 0.f, 0.f};
#pragma unroll
    for (int i = 0; i < NL; ++i) {
      const float a1 = score_step(kr[i], q[i % 3]);
      sv[i / 8] = ((lane & 7) == (i & 7)) ? a1 : sv[i / 8];
      PIN();
      if (i % SPS == SPS - 1) { WAITVM(PACE - 1); ISSUE(QS + i / SPS); }
    }
    STAMP(5);
#pragma unroll
    for (int a = 0; a < 4; ++a) sc[(8 * a + (lane & 7)) * 8 + (lane >> 3)] = sv[a];
    const int k2 = 64 + (lane & 7);
    const float raw = (sc[3 * lane] + sc[3 * lane + 1]) + sc[3 * lane + 2];
    const float raw2 = (sc[3 * k2] + sc[3 * k2 + 1]) + sc[3 * k2 + 2];
    const float sco = bfr(bfr(raw) * SCALE), sco2 = bfr(bfr(raw2) * SCALE);
    mw = wave_max(fmaxf(sco, sco2));
    const float p = ex2((sco - mw) * LOG2E), p2 = ex2((sco2 - mw) * LOG2E);
    pt[(lane & 7) * 12 + (lane >> 3)] = p;
    pt[(lane & 7) * 12 + 8] = p2;
    lw = wave_sum(lane < 8 ? p + p2 : p);
    f32x4 pv[3];
#pragma unroll
    for (int n = S3; n <= NSLOT; ++n) {
#pragma unroll
      for (int j = (n == S3 ? 0 : SCH.hi[n - 1]); j < SCH.hi[n]; ++j) {
        if (j % 12 == 0) {
#pragma unroll
          for (int uu = 0; uu < 3; ++uu) pv[uu] = *reinterpret_cast<const f32x4*>(pt + rho[uu] * 12 + (j / 12) * 4);
        }
        pv_step(vr[j], pv[j % 3][(j / 3) % 4], acc[j % 3]);
        PIN();
      }
      if (n < NSLOT) { WAITVM(PACE - 1); ISSUE(n); }
    }
  }
  STAMP(6);
  const int l48 = lane < 48 ? lane : 47;
  {
#pragma unroll
    for (int u = 0; u < 3; ++u) {
      *reinterpret_cast<f32x4*>(pw + lane * 8 + u * 512) = f32x4{acc[u][0].x, acc[u][0].y, acc[u][1].x, acc[u][1].y};
      *reinterpret_cast<f32x4*>(pw + lane * 8 + u * 512 + 4) = f32x4{acc[u][2].x, acc[u][2].y, acc[u][3].x, acc[u][3].y};
    }
    f32x4 a = *reinterpret_cast<const f32x4*>(pw + l48 * 4);
#pragma unroll
    for (int r = 1; r < 8; ++r) a += *reinterpret_cast<const f32x4*>(pw + l48 * 4 + r * 192);
    float* sp = lds + L_SP + w * 200;
    *reinterpret_cast<f32x4*>(sp + l48 * 4) = a;
    if (lane == 0) *reinterpret_cast<f32x2*>(sp + 192) = f32x2{mw, lw};
    asm volatile("s_waitcnt lgkmcnt(0)" ::: "memory");
    u32 arr = 0;
    if (lane == 0) arr = __hip_atomic_fetch_add(fl + 25, 1u, __ATOMIC_RELAXED, __HIP_MEMORY_SCOPE_WORKGROUP);
    arr = __builtin_amdgcn_readfirstlane(arr);
    CBAR();
    if (arr == 7u) {
      f32x2 ml[8];
      f32x4 o[8];
#pragma unroll
      for (int r = 0; r < 8; ++r) {
        ml[r] = *reinterpret_cast<const f32x2*>(lds + L_SP + r * 200 + 192);
        o[r] = *reinterpret_cast<const f32x4*>(lds + L_SP + r * 200 + l48 * 4);
      }
      float M = ml[0].x;
#pragma unroll
      for (int r = 1; r < 8; ++r) M = fmaxf(M, ml[r].x);
      float L = 0.f;
      f32x4 t = {0.f, 0.f, 0.f, 0.f};
#pragma unroll
      for (int r = 0; r < 8; ++r) {
        const float f = ex2((ml[r].x - M) * LOG2E);
        L += f * ml[r].y;
        t += f * o[r];
      }
      const u32 ob = WS_O + h * 16384 + s * 256;
      if (lane < 48) {
        const int pvoff = (l48 / 6) * 2048 + (l48 % 6) * 32;
        const u32x4 u0 = {__float_as_uint(t.x), tag, __float_as_uint(t.y), tag};
        const u32x4 u1 = {__float_as_uint(t.z), tag, __float_as_uint(t.w), tag};
        __builtin_amdgcn_raw_buffer_store_b128(u0, rs_ws, pvoff, ob, 16);
        __builtin_amdgcn_raw_buffer_store_b128(u1, rs_ws, pvoff + 16, ob, 16);
      } else if (lane < 56) {
        const u32x4 un = {__float_as_uint(M), tag, __float_as_uint(L), tag};
        __builtin_amdgcn_raw_buffer_store_b128(un, rs_ws, (lane - 48) * 2048 + 192, ob, 16);
      }
      STAMP(7);
    }
  }
  if (w == 0) {
    {
      u32 d0 = __builtin_amdgcn_raw_buffer_load_b32(rs_ws, lane * 20480, WS_Y, 16);
      u32 d1 = out[(b < NRED ? b : 0) * CPR];
      KEEP(d0); KEEP(d1);
    }
    int spins = 0;
    while (LDW(fl + 25) < (u32)OTHR && ++spins < 100 * SPIN_CAP) __builtin_amdgcn_s_sleep(2);
    __builtin_amdgcn_s_setprio(3);
    const int kk = lane & 7;
    const int ob = (lane >> 3) * 256;
    u32x4 u0, u1, u2;
    bool ok;
    spins = 0;
    do {
      CBAR();
      u0 = __builtin_amdgcn_raw_buffer_load_b128(rs_ws, ob + kk * 16, WS_O + b * 2048, 16);
      u1 = __builtin_amdgcn_raw_buffer_load_b128(rs_ws, ob + 128 + (kk & 3) * 16, WS_O + b * 2048, 16);
      u2 = __builtin_amdgcn_raw_buffer_load_b128(rs_ws, ob + 192, WS_O + b * 2048, 16);
      const u32 mn = min(min(min(u0.y, u0.w), min(u1.y, u1.w)), min(min(u2.y, u2.w), tag));
      const u32 mx = max(max(max(u0.y, u0.w), max(u1.y, u1.w)), max(max(u2.y, u2.w), tag));
      ok = __all(mn == mx);
    } while (!ok && ++spins < SPIN_CAP);
    STAMP(8);
    const float mp = __uint_as_float(u2.x);
    const float M = hi_max(mp);
    float f = ex2((mp - M) * LOG2E);
    const float L = hi_sum(f * __uint_as_float(u2.z));
    f *= __builtin_amdgcn_rcpf(L);
    const float o0 = hi_sum(f * __uint_as_float(u0.x)), o1 = hi_sum(f * __uint_as_float(u0.z));
    const float o2 = hi_sum(f * __uint_as_float(u1.x)), o3 = hi_sum(f * __uint_as_float(u1.z));
    if (lane < 12) {
      const int c = 2 * lane;
      STW(fl + (c % 6) * 4 + c / 6, bfbits(lane < 8 ? o0 : o2) | 0x10000u);
      STW(fl + ((c + 1) % 6) * 4 + (c + 1) / 6, bfbits(lane < 8 ? o1 : o3) | 0x10000u);
    }
  }
  float y[8];
  {
    const u32 hid = reinterpret_cast<const u16*>(qs)[192 + 6 * s + r6];
#pragma unroll
    for (int d = 0; d < 4; ++d) {
      y[2 * d] = dot2(wov[4][d], hid, 0.f);
      y[2 * d + 1] = dot2(wov[4][d], hid << 16, 0.f);
    }
    u32x4 ov;
    int spins = 0;
    while (true) {
      CBAR();
      ov = *reinterpret_cast<const u32x4*>(fl + r6 * 4);
      if (__all(((ov.x & ov.y) & (ov.z & ov.w)) >> 16) || ++spins >= 100 * SPIN_CAP) break;
      __builtin_amdgcn_s_sleep(1);
    }
    STAMP(9);
#pragma unroll
    for (int i = 0; i < 4; ++i) {
      const u32 ol = ov[i] & 0xffffu, oh = ov[i] << 16;
#pragma unroll
      for (int d = 0; d < 4; ++d) {
        y[2 * d] = dot2(wov[i][d], ol, y[2 * d]);
        y[2 * d + 1] = dot2(wov[i][d], oh, y[2 * d + 1]);
      }
    }
  }
  {
    float* yp = lds + L_YP + w * 480;
    if (lane < 60) {
      *reinterpret_cast<f32x4*>(yp + lane * 8) = f32x4{y[0], y[1], y[2], y[3]};
      *reinterpret_cast<f32x4*>(yp + lane * 8 + 4) = f32x4{y[4], y[5], y[6], y[7]};
    }
    if (lane < 40) {
      f32x2 a = *reinterpret_cast<const f32x2*>(yp + lane * 2);
#pragma unroll
      for (int r = 1; r < 6; ++r) a += *reinterpret_cast<const f32x2*>(yp + r * 80 + lane * 2);
      const int n2 = 40 * w + lane;
      const int r = n2 / UPP, ui = n2 % UPP;
      const u32x4 un = {__float_as_uint(a.x), tag, __float_as_uint(a.y), tag};
      __builtin_amdgcn_raw_buffer_store_b128(un, rs_ws, (r * 256 * UPP + ui) * 16, WS_Y + b * UPP * 16, 16);
    }
  }
  STAMP(10);
  if (b < NRED) {
    constexpr int NUY = 256 * UPP / 512;
    const u32 xo = x[b * CPR + (tid < CPR ? tid : 0)];
    float v0, v1;
    int spins = 0;
    bool ok;
    do {
      CBAR();
      u32x4 un[NUY];
#pragma unroll
      for (int k = 0; k < NUY; ++k)
        un[k] = __builtin_amdgcn_raw_buffer_load_b128(rs_ws, tid * 16 + k * 8192, WS_Y + b * 256 * UPP * 16, 16);
      u32 mn = tag, mx = tag;
      v0 = 0.f; v1 = 0.f;
#pragma unroll
      for (int k = 0; k < NUY; ++k) {
        mn = min(mn, min(un[k].y, un[k].w));
        mx = max(mx, max(un[k].y, un[k].w));
        v0 += __uint_as_float(un[k].x); v1 += __uint_as_float(un[k].z);
      }
      ok = __all(mn == mx);
    } while (!ok && ++spins < SPIN_CAP);
    STAMP(11);
    if (UPP <= 1) { v0 += dpp<XOR1>(v0); v1 += dpp<XOR1>(v1); }
    if (UPP <= 2) { v0 += dpp<XOR2>(v0); v1 += dpp<XOR2>(v1); }
    if (UPP <= 4) { v0 += dpp<ROR4>(v0); v1 += dpp<ROR4>(v1); }
    v0 = hi_sum(v0); v1 = hi_sum(v1);
    if (lane < UPP) *reinterpret_cast<f32x2*>(lds + L_RED + w * 16 + lane * 2) = f32x2{v0, v1};
    BAR();
    if (tid < CPR) {
      float a = 0.f;
#pragma unroll
      for (int k = 0; k < 8; ++k) a += lds[L_RED + k * 16 + tid];
      a += blo(xo);
      out[b * CPR + tid] = (u16)bfbits(a);
    }
    if (b == 0 && tid == 0) __builtin_amdgcn_raw_buffer_store_b32(tag, rs_ws, 0, 0, 16);
  }
}

extern "C" void kernel_launch(void* const* d_in, const int* in_sizes, int n_in,
                              void* d_out, int out_size, void* d_ws, size_t ws_size,
                              hipStream_t stream) {
  if (ws_size < WS_BYTES) return;
  fused_decode_kernel<<<256, 512, 0, stream>>>(
      (const u16*)d_in[0], (const u16*)d_in[1], (const u16*)d_in[2], (const u16*)d_in[3],
      (const u16*)d_in[4], (const u16*)d_in[6], (const u16*)d_in[7], (u16*)d_out, (u32*)d_ws);
}
```

```cpp
#define AMPK 0
#define AMPW (1)
#define OWN 0
#define SGN -1
#include <hip/hip_runtime.h>
#include <hip/hip_bf16.h>
#include <stdint.h>

#define PACE 6
#define PACE2 PACE
#define WTHR 6
#define DRY 0
#define QS 33
#define SPS 3
#define PB 2
#define OTHR 3
#define NRED 160
#define MAPX 0
#define WARM 1

typedef unsigned int u32;
typedef unsigned short u16;
typedef unsigned long long u64;
using u32x2 = __attribute__((ext_vector_type(2))) unsigned;
using u32x4 = __attribute__((ext_vector_type(4))) unsigned;
using f32x2 = __attribute__((ext_vector_type(2))) float;
using f32x4 = __attribute__((ext_vector_type(4))) float;
using bf16x2 = __attribute__((ext_vector_type(2))) __bf16;

constexpr int D = 640, NH = 32, DH = 192, SK = 32768, WLEN = 4096;
constexpr int NL = 27;
constexpr int NSLOT = 2 * NL + (WTHR < 0 ? 5 : 0);
constexpr int CPR = D / NRED;
constexpr int UPP = CPR / 2;
constexpr u32 WS_Q = 4096;
constexpr u32 WS_O = WS_Q + 256u * 2048u;
constexpr u32 WS_Y = WS_O + 256u * 2048u;
constexpr u32 WS_BYTES = WS_Y + 320u * 256u * 16u;
constexpr int SPIN_CAP = 200000;
constexpr float LOG2E = 1.4426950408889634f;
constexpr float SCALE = 0.07216878364870322f;

__device__ __forceinline__ float dot2(u32 a, u32 b, float acc) {
  return __builtin_amdgcn_fdot2_f32_bf16(__builtin_bit_cast(bf16x2, a), __builtin_bit_cast(bf16x2, b), acc, false);
}
template <int CTRL> __device__ __forceinline__ float dpp(float x) {
  return __builtin_bit_cast(float, __builtin_amdgcn_mov_dpp(__builtin_bit_cast(int, x), CTRL, 0xf, 0xf, true));
}
constexpr int XOR1 = 0xB1, XOR2 = 0x4E, XOR7 = 0x141, ROR8 = 0x128, ROR4 = 0x124;
__device__ __forceinline__ float sum8(float x) {
  x += dpp<XOR1>(x); x += dpp<XOR2>(x); x += dpp<XOR7>(x); return x;
}
__device__ __forceinline__ float max8(float x) {
  x = fmaxf(x, dpp<XOR1>(x)); x = fmaxf(x, dpp<XOR2>(x)); x = fmaxf(x, dpp<XOR7>(x)); return x;
}
__device__ __forceinline__ float xrow_sum(float x) {
  auto s = __builtin_amdgcn_permlane16_swap(__float_as_uint(x), __float_as_uint(x), false, false);
  x = __uint_as_float(s[0]) + __uint_as_float(s[1]);
  auto t = __builtin_amdgcn_permlane32_swap(__float_as_uint(x), __float_as_uint(x), false, false);
  return __uint_as_float(t[0]) + __uint_as_float(t[1]);
}
__device__ __forceinline__ float xrow_max(float x) {
  auto s = __builtin_amdgcn_permlane16_swap(__float_as_uint(x), __float_as_uint(x), false, false);
  x = fmaxf(__uint_as_float(s[0]), __uint_as_float(s[1]));
  auto t = __builtin_amdgcn_permlane32_swap(__float_as_uint(x), __float_as_uint(x), false, false);
  return fmaxf(__uint_as_float(t[0]), __uint_as_float(t[1]));
}
__device__ __forceinline__ float hi_sum(float x) { x += dpp<ROR8>(x); return xrow_sum(x); }
__device__ __forceinline__ float hi_max(float x) { x = fmaxf(x, dpp<ROR8>(x)); return xrow_max(x); }
__device__ __forceinline__ float wave_sum(float x) { return hi_sum(sum8(x)); }
__device__ __forceinline__ float wave_max(float x) { return hi_max(max8(x)); }
__device__ __forceinline__ u32 bfbits(float f) { return (u32)__builtin_bit_cast(u16, (__bf16)f); }
__device__ __forceinline__ float bfr(float f) { return __uint_as_float(bfbits(f) << 16); }
__device__ __forceinline__ u32 pack2(float a, float b) {
  bf16x2 p = {(__bf16)a, (__bf16)b};
  return __builtin_bit_cast(u32, p);
}
__device__ __forceinline__ float blo(u32 v) { return __uint_as_float(v << 16); }
__device__ __forceinline__ float bhi(u32 v) { return __uint_as_float(v & 0xffff0000u); }
__device__ __forceinline__ float ex2(float v) { return __builtin_amdgcn_exp2f(v); }
#define BAR() asm volatile("s_waitcnt lgkmcnt(0)\n\ts_barrier" ::: "memory")
#define PIN() __builtin_amdgcn_sched_barrier(0)
#define CBAR() asm volatile("" ::: "memory")
#define STAMP(i) do { if ((i) == AMPK) { PIN(); tsel = (u32)__builtin_amdgcn_s_memrealtime(); PIN(); } } while (0)
#define KEEP(v) asm volatile("" : "+v"(v))
#define RSRC(p, n) __builtin_amdgcn_make_buffer_rsrc((void*)(p), 0, (n), 0x00020000)
#define LDW(p) __hip_atomic_load((p), __ATOMIC_RELAXED, __HIP_MEMORY_SCOPE_WORKGROUP)
#define STW(p, v) __hip_atomic_store((p), (v), __ATOMIC_RELAXED, __HIP_MEMORY_SCOPE_WORKGROUP)

#define WAITVM(n) __builtin_amdgcn_s_waitcnt(0x0F70 | ((n) & 15) | (((n) >> 4) << 14))

constexpr int PART_W = 1568;
constexpr int L_PART = 0;
constexpr int L_SC = L_PART + 8 * PART_W;
constexpr int L_PT = L_SC + 8 * 256;
constexpr int L_QS = L_PT + 8 * 96;
constexpr int L_FL = L_QS + 128;
constexpr int L_SP = L_FL + 64;
constexpr int L_RED = L_SP + 8 * 200;
constexpr int L_YP = L_RED + 128;
constexpr int L_END = L_YP + 8 * 480;

struct Sched { int hi[NSLOT + 1]; };
constexpr int S3 = QS + NL / SPS < NSLOT ? QS + NL / SPS : NSLOT;
constexpr Sched make_sched() {
  Sched t{};
  int j = 0;
  for (int n = S3; n < NSLOT; ++n) {
    int burst = 0;
    while (j < NL && burst < PB && n - (NL + j) >= PACE2) { ++j; ++burst; }
    t.hi[n] = j;
  }
  t.hi[NSLOT] = NL;
  return t;
}
constexpr Sched SCH = make_sched();
static_assert(QS >= NL && S3 <= NSLOT && NL % SPS == 0, "schedule");

__device__ __forceinline__ float score_step(const u32x4 d, const u32x4 q) {
  float a = dot2(d.x, q.x, 0.f);
  float c = dot2(d.z, q.z, 0.f);
  a = dot2(d.y, q.y, a);
  c = dot2(d.w, q.w, c);
  return sum8(a + c);
}
__device__ __forceinline__ void pv_step(const u32x4 d, const float pp, f32x2 (&acc)[4]) {
#pragma unroll
  for (int c = 0; c < 4; ++c) {
    acc[c] = __builtin_elementwise_fma(f32x2{blo(d[c]), bhi(d[c])}, f32x2{pp, pp}, acc[c]);
    KEEP(acc[c]);
  }
}

__global__ void __launch_bounds__(512, 2)
fused_decode_kernel(const u16* __restrict__ x, const u16* __restrict__ kc, const u16* __restrict__ vc,
                    const u16* __restrict__ wq, const u16* __restrict__ wo, const u16* __restrict__ w1,
                    const u16* __restrict__ w2, u16* __restrict__ out, u32* __restrict__ ws) {
  __shared__ __attribute__((aligned(16))) float lds[L_END];
  u64 tst64;
  asm volatile("s_memrealtime %0\n\ts_waitcnt lgkmcnt(0)" : "=s"(tst64) :: "memory");
  PIN();
  const u32 tst = (u32)tst64;
  u32 tsel = AMPK == 0 ? tst : 0u;
  const int tid = threadIdx.x;
  const int lane = tid & 63;
  const int w = __builtin_amdgcn_readfirstlane(tid >> 6);
  const int b = blockIdx.x;
  const int h = b >> 3;
  const int s = b & 7;

  const auto rs_ws = RSRC(ws, WS_BYTES);
  const size_t kvoff = ((size_t)h * SK + (SK - WLEN) + s * 512) * DH;
  const auto rs_k = RSRC(kc + kvoff, 512 * DH * 2);
  const auto rs_v = RSRC(vc + kvoff, 512 * DH * 2);
  const int r6 = (lane / 10) > 5 ? 5 : (lane / 10);
  const int ch = lane - 10 * (lane / 10);
  const auto rs_wo = RSRC(wo + (size_t)(h * DH + 24 * s) * D + 80 * w, 23 * 1280 + 160);
  const auto rs_w2 = RSRC(w2 + (size_t)(48 * h + 6 * s) * D + 80 * w, 5 * 1280 + 160);
  const int wo_voff = r6 * 1280 + ch * 16;
  u32* const fl = reinterpret_cast<u32*>(lds + L_FL);
  u32* const qs = reinterpret_cast<u32*>(lds + L_QS);
  float* const sc = lds + L_SC + w * 256;
  float* const pt = lds + L_PT + w * 96;
  float* const pw = lds + L_PART + w * PART_W;
  int rho[3], c8[3];
#pragma unroll
  for (int u = 0; u < 3; ++u) { const int g = 64 * u + lane; rho[u] = g / 24; c8[u] = g % 24; }

  u32x4 wov[5];
  f32x2 acc[3][4];
#pragma unroll
  for (int u = 0; u < 3; ++u)
#pragma unroll
    for (int c = 0; c < 4; ++c) acc[u][c] = f32x2{0.f, 0.f};
  float mw, lw;
  u32 tag;

  if (w == 0) {
    __builtin_amdgcn_s_setprio(3);
    const auto rs_x = RSRC(x, D * 2);
    const u32x4 xa = __builtin_amdgcn_raw_buffer_load_b128(rs_x, lane * 20, 0, 0);
    const u32 xb = __builtin_amdgcn_raw_buffer_load_b32(rs_x, lane * 20 + 16, 0, 0);
    const u32 xp = __builtin_amdgcn_raw_buffer_load_b32(rs_x, (lane < 40 ? lane : 0) * 4, s * 160, 0);
    const bool isq = lane < 48;
    const int l1 = lane < 60 ? lane - 48 : 11;
    const int r2 = isq ? (lane >= 24) : (l1 >= 6);
    const int cch = isq ? lane - 24 * r2 : l1 - 6 * r2;
    u32x4 wv[40];
    {
      const char* p = isq ? (const char*)wq + (size_t)(80 * s + r2) * 12288 + h * 384 + cch * 16
                          : (const char*)w1 + (size_t)(80 * s + r2) * 3072 + h * 96 + cch * 16;
      const u32 stp = isq ? 24576u : 6144u;
#pragma unroll
      for (int i = 0; i < 40; ++i) {
        wv[i] = __builtin_nontemporal_load((const u32x4*)p);
        p += stp;
        PIN();
      }
    }
    const u32 E = __builtin_amdgcn_raw_buffer_load_b32(rs_ws, 0, 0, 16);
    if (lane < 32) fl[lane] = 0u;
    PIN();
    BAR();
    float xv[10];
    xv[0] = blo(xa.x); xv[1] = bhi(xa.x); xv[2] = blo(xa.y); xv[3] = bhi(xa.y);
    xv[4] = blo(xa.z); xv[5] = bhi(xa.z); xv[6] = blo(xa.w); xv[7] = bhi(xa.w);
    xv[8] = blo(xb); xv[9] = bhi(xb);
    const float sm = ((xv[0] + xv[1]) + (xv[2] + xv[3])) + ((xv[4] + xv[5]) + (xv[6] + xv[7])) + (xv[8] + xv[9]);
    const float mean = wave_sum(sm) * (1.0f / D);
    float sq = 0.f;
#pragma unroll
    for (int e = 0; e < 10; ++e) { const float d = xv[e] - mean; sq += d * d; }
    const float var = wave_sum(sq) * (1.0f / D);
    const float mb = bfr(mean);
    const float den = bfr(__builtin_amdgcn_sqrtf(bfr(bfr(var) + 1e-5f)));
    const float rinv = 1.0f / den;
    u16* xn = reinterpret_cast<u16*>(pw + 512);
    if (lane < 40) {
      xn[lane] = (u16)bfbits(bfr(blo(xp) - mb) * rinv);
      xn[40 + lane] = (u16)bfbits(bfr(bhi(xp) - mb) * rinv);
    }
    u32x4 xs[5];
#pragma unroll
    for (int i = 0; i < 5; ++i) xs[i] = *reinterpret_cast<const u32x4*>(xn + r2 * 40 + i * 8);
    STAMP(1);
    float a8[8];
#pragma unroll
    for (int e = 0; e < 8; ++e) a8[e] = 0.f;
#pragma unroll
    for (int i = 0; i < 40; ++i) {
      const u32 v = xs[i / 8][(i / 2) % 4];
      const u32 xl = (i & 1) ? v >> 16 : v & 0xffffu;
      const u32 xh = (i & 1) ? v & 0xffff0000u : v << 16;
#pragma unroll
      for (int d = 0; d < 4; ++d) {
        a8[2 * d] = dot2(wv[i][d], xl, a8[2 * d]);
        a8[2 * d + 1] = dot2(wv[i][d], xh, a8[2 * d + 1]);
        KEEP(a8[2 * d]); KEEP(a8[2 * d + 1]);
      }
      PIN();
    }
    if (lane < 60) {
      float* p = pw + r2 * 244 + (isq ? 0 : 192) + cch * 8;
      *reinterpret_cast<f32x4*>(p) = f32x4{a8[0], a8[1], a8[2], a8[3]};
      *reinterpret_cast<f32x4*>(p + 4) = f32x4{a8[4], a8[5], a8[6], a8[7]};
    }
    tag = E + 1u;
    if (b == 0 && tid == 0) { u32x2 g = {tst, tag}; __builtin_amdgcn_raw_buffer_store_b64(g, rs_ws, 64, 0, 16); }
    STAMP(2);
    const int t = lane < 60 ? lane : 59;
    const f32x4 a = *reinterpret_cast<const f32x4*>(pw + t * 4) + *reinterpret_cast<const f32x4*>(pw + 244 + t * 4);
    if (lane < 60) {
      const u32x4 u0 = {__float_as_uint(a.x), tag, __float_as_uint(a.y), tag};
      const u32x4 u1 = {__float_as_uint(a.z), tag, __float_as_uint(a.w), tag};
      __builtin_amdgcn_raw_buffer_store_b128(u0, rs_ws, lane * 32, WS_Q + b * 2048, 16);
      __builtin_amdgcn_raw_buffer_store_b128(u1, rs_ws, lane * 32 + 16, WS_Q + b * 2048, 16);
    }
    STAMP(3);
    u32x4 kr[3], vr[3];
#pragma unroll
    for (int i = 0; i < 3; ++i) kr[i] = __builtin_amdgcn_raw_buffer_load_b128(rs_k, lane * 16, i * 1024, 2);
#pragma unroll
    for (int i = 0; i < 3; ++i) vr[i] = __builtin_amdgcn_raw_buffer_load_b128(rs_v, lane * 16, i * 1024, 2);
    PIN();
    f32x4 sum;
    int spins = 0;
    bool ok;
    do {
      CBAR();
      u32x4 un[16];
#pragma unroll
      for (int k = 0; k < 16; ++k)
        un[k] = __builtin_amdgcn_raw_buffer_load_b128(rs_ws, t * 16 + (k & 1) * 960 + (k >> 1) * 2048, WS_Q + h * 16384, 16);
      u32 mn = tag, mx = tag;
      sum = f32x4{0.f, 0.f, 0.f, 0.f};
#pragma unroll
      for (int k = 0; k < 16; ++k) {
        mn = min(mn, min(un[k].y, un[k].w));
        mx = max(mx, max(un[k].y, un[k].w));
        sum[2 * (k & 1)] += __uint_as_float(un[k].x);
        sum[2 * (k & 1) + 1] += __uint_as_float(un[k].z);
      }
      ok = __all(mn == mx);
    } while (!ok && ++spins < SPIN_CAP);
    if (t >= 36) { sum.z = fmaxf(sum.z, 0.f); sum.w = fmaxf(sum.w, 0.f); }
    qs[t] = pack2(sum.x, sum.y);
    qs[60 + t] = pack2(sum.z, sum.w);
    qs[120] = tag;
    asm volatile("s_waitcnt lgkmcnt(0)" ::: "memory");
    if (lane == 0) STW(fl + 24, 1u);
    STAMP(4);
    __builtin_amdgcn_s_setprio(0);
    float sv = 0.f;
#pragma unroll
    for (int u = 0; u < 3; ++u) {
      const float a1 = score_step(kr[u], *reinterpret_cast<const u32x4*>(qs + c8[u] * 4));
      sv = ((lane & 7) == u) ? a1 : sv;
    }
    sc[(lane & 7) * 8 + (lane >> 3)] = sv;
    const int lk = lane & 7;
    const float raw = (sc[3 * lk] + sc[3 * lk + 1]) + sc[3 * lk + 2];
    const float sco = bfr(bfr(raw) * SCALE);
    mw = max8(sco);
    const float p = ex2((sco - mw) * LOG2E);
    pt[lk] = p;
    lw = sum8(p);
    STAMP(5);
#pragma unroll
    for (int u = 0; u < 3; ++u) pv_step(vr[u], pt[rho[u]], acc[u]);
  } else {
    const int soff = (8 + (w - 1) * 72) * (DH * 2);
    u32x4 kr[NL], vr[NL];
#define ISSUE(n) do { \
      if ((n) < NL) kr[(n) < NL ? (n) : 0] = __builtin_amdgcn_raw_buffer_load_b128(rs_k, lane * 16, soff + (n) * 1024, 2); \
      else if ((n) < 2 * NL) vr[((n) >= NL && (n) < 2 * NL) ? (n) - NL : 0] = __builtin_amdgcn_raw_buffer_load_b128(rs_v, lane * 16, soff + ((n) - NL) * 1024, 2); \
      else if ((n) == 2 * NL) wov[4] = __builtin_amdgcn_raw_buffer_load_b128(rs_w2, wo_voff, 0, 2); \
      else wov[(n) > 2 * NL ? (n) - 2 * NL - 1 : 0] = __builtin_amdgcn_raw_buffer_load_b128(rs_wo, wo_voff, ((n) - 2 * NL - 1) * 7680, 2); \
      PIN(); } while (0)
#pragma unroll
    for (int n = 0; n < PACE; ++n) ISSUE(n);
    asm volatile("s_barrier" ::: "memory");
#pragma unroll
    for (int n = PACE; n < QS; ++n) { WAITVM(PACE - 1); ISSUE(n); }
    STAMP(3);
    {
      int spins = 0;
      while (LDW(fl + 24) == 0u && ++spins < 100 * SPIN_CAP) __builtin_amdgcn_s_sleep(1);
      CBAR();
    }
    STAMP(4);
    u32x4 q[3];
#pragma unroll
    for (int u = 0; u < 3; ++u) q[u] = *reinterpret_cast<const u32x4*>(qs + c8[u] * 4);
    tag = qs[120];
    float sv[4] = {0.f, 0.f, 0.f, 0.f};
#pragma unroll
    for (int i = 0; i < NL; ++i) {
      const float a1 = score_step(kr[i], q[i % 3]);
      sv[i / 8] = ((lane & 7) == (i & 7)) ? a1 : sv[i / 8];
      PIN();
      if (i % SPS == SPS - 1 && QS + i / SPS < NSLOT) { WAITVM(PACE2 - 1); ISSUE(QS + i / SPS); }
    }
    STAMP(5);
#pragma unroll
    for (int a = 0; a < 4; ++a) sc[(8 * a + (lane & 7)) * 8 + (lane >> 3)] = sv[a];
    const int k2 = 64 + (lane & 7);
    const float raw = (sc[3 * lane] + sc[3 * lane + 1]) + sc[3 * lane + 2];
    const float raw2 = (sc[3 * k2] + sc[3 * k2 + 1]) + sc[3 * k2 + 2];
    const float sco = bfr(bfr(raw) * SCALE), sco2 = bfr(bfr(raw2) * SCALE);
    mw = wave_max(fmaxf(sco, sco2));
    const float p = ex2((sco - mw) * LOG2E), p2 = ex2((sco2 - mw) * LOG2E);
    pt[(lane & 7) * 12 + (lane >> 3)] = p;
    pt[(lane & 7) * 12 + 8] = p2;
    lw = wave_sum(lane < 8 ? p + p2 : p);
    f32x4 pv[3];
#pragma unroll
    for (int n = S3; n <= NSLOT; ++n) {
#pragma unroll
      for (int j = (n == S3 ? 0 : SCH.hi[n - 1]); j < SCH.hi[n]; ++j) {
        if (j % 12 == 0) {
#pragma unroll
          for (int uu = 0; uu < 3; ++uu) pv[uu] = *reinterpret_cast<const f32x4*>(pt + rho[uu] * 12 + (j / 12) * 4);
        }
        pv_step(vr[j], pv[j % 3][(j / 3) % 4], acc[j % 3]);
        PIN();
      }
      if (n < NSLOT) { WAITVM(PACE2 - 1); ISSUE(n); }
    }
  }
  STAMP(6);
  const int l48 = lane < 48 ? lane : 47;
  u32 arr = 0;
  {
#pragma unroll
    for (int u = 0; u < 3; ++u) {
      *reinterpret_cast<f32x4*>(pw + lane * 8 + u * 512) = f32x4{acc[u][0].x, acc[u][0].y, acc[u][1].x, acc[u][1].y};
      *reinterpret_cast<f32x4*>(pw + lane * 8 + u * 512 + 4) = f32x4{acc[u][2].x, acc[u][2].y, acc[u][3].x, acc[u][3].y};
    }
    f32x4 a = *reinterpret_cast<const f32x4*>(pw + l48 * 4);
#pragma unroll
    for (int r = 1; r < 8; ++r) a += *reinterpret_cast<const f32x4*>(pw + l48 * 4 + r * 192);
    float* sp = lds + L_SP + w * 200;
    *reinterpret_cast<f32x4*>(sp + l48 * 4) = a;
    if (lane == 0) *reinterpret_cast<f32x2*>(sp + 192) = f32x2{mw, lw};
    asm volatile("s_waitcnt lgkmcnt(0)" ::: "memory");
    if (w) STAMP(1);
    if (lane == 0) arr = __hip_atomic_fetch_add(fl + 25, 1u, __ATOMIC_RELAXED, __HIP_MEMORY_SCOPE_WORKGROUP);
    arr = __builtin_amdgcn_readfirstlane(arr);
    CBAR();
    if (w) STAMP(2);
  }
  int pass = (DRY && w == 0 && arr != 7u) ? 0 : 1;
  bool need_w = WTHR >= 0 || w == 0;
#pragma nounroll
  for (; pass < 2; ++pass) {
    pass = __builtin_amdgcn_readfirstlane(pass);
    asm volatile("" : "+s"(pass));
    const bool real = pass != 0;
    const auto rs_st = RSRC(ws, real ? WS_BYTES : 0u);
    if (arr == 7u || !real) {
      f32x2 ml[8];
      f32x4 o[8];
      if (real) STAMP(14);
#pragma unroll
      for (int r = 0; r < 8; ++r) {
        ml[r] = *reinterpret_cast<const f32x2*>(lds + L_SP + r * 200 + 192);
        o[r] = *reinterpret_cast<const f32x4*>(lds + L_SP + r * 200 + l48 * 4);
      }
      float M = ml[0].x;
#pragma unroll
      for (int r = 1; r < 8; ++r) M = fmaxf(M, ml[r].x);
      float L = 0.f;
      f32x4 t = {0.f, 0.f, 0.f, 0.f};
#pragma unroll
      for (int r = 0; r < 8; ++r) {
        const float f = ex2((ml[r].x - M) * LOG2E);
        L += f * ml[r].y;
        t += f * o[r];
      }
      const u32 ob = WS_O + h * 16384 + s * 256;
      if (lane < 48) {
        const int pvoff = (l48 / 6) * 2048 + (l48 % 6) * 32;
        const u32x4 u0 = {__float_as_uint(t.x), tag, __float_as_uint(t.y), tag};
        const u32x4 u1 = {__float_as_uint(t.z), tag, __float_as_uint(t.w), tag};
        __builtin_amdgcn_raw_buffer_store_b128(u0, rs_st, pvoff, ob, 16);
        __builtin_amdgcn_raw_buffer_store_b128(u1, rs_st, pvoff + 16, ob, 16);
      } else if (lane < 56) {
        const u32x4 un = {__float_as_uint(M), tag, __float_as_uint(L), tag};
        __builtin_amdgcn_raw_buffer_store_b128(un, rs_st, (lane - 48) * 2048 + 192, ob, 16);
      }
      if (real) STAMP(7);
    }
    if (need_w) {
      int spins = 0;
      while (WTHR > 0 && real && LDW(fl + 25) < (u32)(w == 0 && WTHR > 8 ? 8 : WTHR) && ++spins < 100 * SPIN_CAP) __builtin_amdgcn_s_sleep(1);
      wov[4] = __builtin_amdgcn_raw_buffer_load_b128(rs_w2, wo_voff, 0, 2);
#pragma unroll
      for (int i = 0; i < 4; ++i) wov[i] = __builtin_amdgcn_raw_buffer_load_b128(rs_wo, wo_voff, i * 7680, 2);
      need_w = false;
      PIN();
    }
    if (w == 0) {
      int spins = 0;
      while (real && LDW(fl + 25) < (u32)OTHR && ++spins < 100 * SPIN_CAP) __builtin_amdgcn_s_sleep(2);
      if (real) __builtin_amdgcn_s_setprio(3);
      const int kk = lane & 7;
      const int ob = (lane >> 3) * 256;
      u32x4 u0, u1, u2;
      bool ok;
      spins = 0;
      do {
        CBAR();
        u0 = __builtin_amdgcn_raw_buffer_load_b128(rs_ws, ob + kk * 16, WS_O + b * 2048, 16);
        u1 = __builtin_amdgcn_raw_buffer_load_b128(rs_ws, ob + 128 + (kk & 3) * 16, WS_O + b * 2048, 16);
        u2 = __builtin_amdgcn_raw_buffer_load_b128(rs_ws, ob + 192, WS_O + b * 2048, 16);
        const u32 mn = min(min(min(u0.y, u0.w), min(u1.y, u1.w)), min(min(u2.y, u2.w), tag));
        const u32 mx = max(max(max(u0.y, u0.w), max(u1.y, u1.w)), max(max(u2.y, u2.w), tag));
        ok = __all(mn == mx);
      } while (!ok && real && ++spins < SPIN_CAP);
      STAMP(8);
      if (WTHR > 8 && real && lane == 0) __hip_atomic_fetch_add(fl + 25, 1u, __ATOMIC_RELAXED, __HIP_MEMORY_SCOPE_WORKGROUP);
      const float mp = __uint_as_float(u2.x);
      const float M = hi_max(mp);
      float f = ex2((mp - M) * LOG2E);
      const float L = hi_sum(f * __uint_as_float(u2.z));
      f *= __builtin_amdgcn_rcpf(L);
      const float o0 = hi_sum(f * __uint_as_float(u0.x)), o1 = hi_sum(f * __uint_as_float(u0.z));
      const float o2 = hi_sum(f * __uint_as_float(u1.x)), o3 = hi_sum(f * __uint_as_float(u1.z));
      if (lane < 12) {
        const int c = 2 * lane;
        u32* os = fl + (real ? 0 : 32);
        STW(os + (c % 6) * 4 + c / 6, bfbits(lane < 8 ? o0 : o2) | 0x10000u);
        STW(os + ((c + 1) % 6) * 4 + (c + 1) / 6, bfbits(lane < 8 ? o1 : o3) | 0x10000u);
      }
    }
    float y[8];
    {
      const u32 hid = reinterpret_cast<const u16*>(qs)[192 + 6 * s + r6];
#pragma unroll
      for (int d = 0; d < 4; ++d) {
        y[2 * d] = dot2(wov[4][d], hid, 0.f);
        y[2 * d + 1] = dot2(wov[4][d], hid << 16, 0.f);
      }
      u32x4 ov;
      int spins = 0;
      while (true) {
        CBAR();
        ov = *reinterpret_cast<const u32x4*>(fl + r6 * 4);
        if (__all(((ov.x & ov.y) & (ov.z & ov.w)) >> 16) || !real || ++spins >= 100 * SPIN_CAP) break;
        __builtin_amdgcn_s_sleep(1);
      }
      STAMP(9);
#pragma unroll
      for (int i = 0; i < 4; ++i) {
        const u32 ol = ov[i] & 0xffffu, oh = ov[i] << 16;
#pragma unroll
        for (int d = 0; d < 4; ++d) {
          y[2 * d] = dot2(wov[i][d], ol, y[2 * d]);
          y[2 * d + 1] = dot2(wov[i][d], oh, y[2 * d + 1]);
        }
      }
    }
    {
      float* yp = lds + L_YP + w * 480;
      if (lane < 60) {
        *reinterpret_cast<f32x4*>(yp + lane * 8) = f32x4{y[0], y[1], y[2], y[3]};
        *reinterpret_cast<f32x4*>(yp + lane * 8 + 4) = f32x4{y[4], y[5], y[6], y[7]};
      }
      if (lane < 40) {
        f32x2 a = *reinterpret_cast<const f32x2*>(yp + lane * 2);
#pragma unroll
        for (int r = 1; r < 6; ++r) a += *reinterpret_cast<const f32x2*>(yp + r * 80 + lane * 2);
        const int n2 = 40 * w + lane;
        const int r = n2 / UPP, ui = n2 % UPP;
        const u32x4 un = {__float_as_uint(a.x), tag, __float_as_uint(a.y), tag};
        __builtin_amdgcn_raw_buffer_store_b128(un, rs_st, (r * 256 * UPP + ui) * 16, WS_Y + b * UPP * 16, 16);
      }
    }
    STAMP(10);
    if (b < NRED) {
      constexpr int NUY = 256 * UPP / 512;
      const u32 xo = x[b * CPR + (tid < CPR ? tid : 0)];
      float v0, v1;
      int spins = 0;
      bool ok;
      do {
        CBAR();
        u32x4 un[NUY];
#pragma unroll
        for (int k = 0; k < NUY; ++k)
          un[k] = __builtin_amdgcn_raw_buffer_load_b128(rs_ws, tid * 16 + k * 8192, WS_Y + b * 256 * UPP * 16, 16);
        u32 mn = tag, mx = tag;
        v0 = 0.f; v1 = 0.f;
#pragma unroll
        for (int k = 0; k < NUY; ++k) {
          mn = min(mn, min(un[k].y, un[k].w));
          mx = max(mx, max(un[k].y, un[k].w));
          v0 += __uint_as_float(un[k].x); v1 += __uint_as_float(un[k].z);
        }
        ok = __all(mn == mx);
      } while (!ok && real && ++spins < SPIN_CAP);
      STAMP(11);
      if (UPP <= 1) { v0 += dpp<XOR1>(v0); v1 += dpp<XOR1>(v1); }
      if (UPP <= 2) { v0 += dpp<XOR2>(v0); v1 += dpp<XOR2>(v1); }
      if (UPP <= 4) { v0 += dpp<ROR4>(v0); v1 += dpp<ROR4>(v1); }
      v0 = hi_sum(v0); v1 = hi_sum(v1);
      if (lane < UPP) *reinterpret_cast<f32x2*>(lds + L_RED + w * 16 + lane * 2) = f32x2{v0, v1};
      if (real) BAR();
      if (tid < CPR) {
        float a = 0.f;
#pragma unroll
        for (int k = 0; k < 8; ++k) a += lds[L_RED + k * 16 + tid];
        a += blo(xo);
        __builtin_amdgcn_raw_buffer_store_b16((u16)bfbits(a), RSRC(out, real ? D * 2 : 0), tid * 2, b * CPR * 2, 0);
      }
      if (b == 0 && tid == 0) __builtin_amdgcn_raw_buffer_store_b32(tag, rs_st, 0, 0, 16);
    }
    if (!real) {
      u32 d0 = __builtin_amdgcn_raw_buffer_load_b32(rs_ws, lane * 20480, WS_Y, 16);
      u32 d1 = out[(b < NRED ? b : 0) * CPR];
      KEEP(d0); KEEP(d1);
    }
  }
  STAMP(12);
  if (tsel != 0u && (AMPW)) {
    u32x2 g; int sp = 0;
    do { asm volatile("" ::: "memory"); g = __builtin_amdgcn_raw_buffer_load_b64(rs_ws, 64, 0, 16); } while (g.y != tag && ++sp < SPIN_CAP);
    const u32 t0 = OWN ? tst : __builtin_amdgcn_readfirstlane(g.x);
    int d = SGN * (int)(tsel - t0) + 200;
    if (d < 0) d = 0;
    if (d > 10000) d = 10000;
    const u32 ta = (u32)__builtin_amdgcn_s_memrealtime();
    while ((u32)__builtin_amdgcn_s_memrealtime() - ta < (u32)d * 16u) __builtin_amdgcn_s_sleep(2);
  }
}

extern "C" void kernel_launch(void* const* d_in, const int* in_sizes, int n_in,
                              void* d_out, int out_size, void* d_ws, size_t ws_size,
                              hipStream_t stream) {
  if (ws_size < WS_BYTES) return;
  fused_decode_kernel<<<256, 512, 0, stream>>>(
      (const u16*)d_in[0], (const u16*)d_in[1], (const u16*)d_in[2], (const u16*)d_in[3],
      (const u16*)d_in[4], (const u16*)d_in[6], (const u16*)d_in[7], (u16*)d_out, (u32*)d_ws);
}
```

```cpp
#include <hip/hip_runtime.h>
#include <hip/hip_bf16.h>
#include <stdint.h>

#define PACE 6
#define PACE2 PACE
#define WTHR 8
#define W0EARLY 0
#define TP 0
#define TA 0
#define GAPQ 10
#define GAPO0 5
#define GAPO 2
#define GAPY0 6
#define GAPY 2
#define QS 33
#define SG 1
#define PB 2

typedef unsigned int u32;
typedef unsigned short u16;
typedef unsigned long long u64;
using u32x2 = __attribute__((ext_vector_type(2))) unsigned;
using u32x4 = __attribute__((ext_vector_type(4))) unsigned;
using u32x16 = __attribute__((ext_vector_type(16))) unsigned;
using f32x2 = __attribute__((ext_vector_type(2))) float;
using f32x4 = __attribute__((ext_vector_type(4))) float;
using bf16x2 = __attribute__((ext_vector_type(2))) __bf16;

constexpr int D = 640, DH = 192, SK = 32768, WLEN = 4096;
constexpr int NL = 27;
constexpr int NSLOT = 2 * NL;
constexpr u32 WS_Q = 4096;
constexpr u32 WS_S = WS_Q + 240u * 128u;
constexpr u32 WS_O = WS_S + 32u * 128u;
constexpr u32 WS_Y = WS_O + 256u * 768u;
constexpr u32 WS_BYTES = WS_Y + 960u * 128u;
constexpr int SPIN_CAP = 200000;
constexpr float LOG2E = 1.4426950408889634f;
constexpr float SCALE = 0.07216878364870322f;

__device__ __forceinline__ float dot2(u32 a, u32 b, float acc) {
  return __builtin_amdgcn_fdot2_f32_bf16(__builtin_bit_cast(bf16x2, a), __builtin_bit_cast(bf16x2, b), acc, false);
}
template <int CTRL> __device__ __forceinline__ float dpp(float x) {
  return __builtin_bit_cast(float, __builtin_amdgcn_mov_dpp(__builtin_bit_cast(int, x), CTRL, 0xf, 0xf, true));
}
constexpr int XOR1 = 0xB1, XOR2 = 0x4E, XOR7 = 0x141, ROR8 = 0x128, ROR4 = 0x124;
__device__ __forceinline__ float sum8(float x) {
  x += dpp<XOR1>(x); x += dpp<XOR2>(x); x += dpp<XOR7>(x); return x;
}
__device__ __forceinline__ float fmx(float a, float b) {
  return __builtin_fmaxf(a, b);
}
__device__ __forceinline__ float max8(float x) {
  x = fmx(x, dpp<XOR1>(x)); x = fmx(x, dpp<XOR2>(x)); x = fmx(x, dpp<XOR7>(x)); return x;
}
__device__ __forceinline__ float xrow_sum(float x) {
  auto s = __builtin_amdgcn_permlane16_swap(__float_as_uint(x), __float_as_uint(x), false, false);
  x = __uint_as_float(s[0]) + __uint_as_float(s[1]);
  auto t = __builtin_amdgcn_permlane32_swap(__float_as_uint(x), __float_as_uint(x), false, false);
  return __uint_as_float(t[0]) + __uint_as_float(t[1]);
}
__device__ __forceinline__ float xrow_max(float x) {
  auto s = __builtin_amdgcn_permlane16_swap(__float_as_uint(x), __float_as_uint(x), false, false);
  x = fmx(__uint_as_float(s[0]), __uint_as_float(s[1]));
  auto t = __builtin_amdgcn_permlane32_swap(__float_as_uint(x), __float_as_uint(x), false, false);
  return fmx(__uint_as_float(t[0]), __uint_as_float(t[1]));
}
__device__ __forceinline__ float tsum32(float a, float b) {
  auto t = __builtin_amdgcn_permlane32_swap(__float_as_uint(a), __float_as_uint(b), false, false);
  return __uint_as_float(t[0]) + __uint_as_float(t[1]);
}
__device__ __forceinline__ float tsum16(float a, float b) {
  auto t = __builtin_amdgcn_permlane16_swap(__float_as_uint(a), __float_as_uint(b), false, false);
  return __uint_as_float(t[0]) + __uint_as_float(t[1]);
}
__device__ __forceinline__ float tsum8(float a, float b, bool b3) {
  const float snd = b3 ? a : b, kp = b3 ? b : a;
  return kp + dpp<ROR8>(snd);
}
__device__ __forceinline__ float tsum_hi8(float v0, float v1, float v2, float v3, float v4, float v5, float v6, float v7, bool b3) {
  return tsum8(tsum16(tsum32(v0, v4), tsum32(v2, v6)), tsum16(tsum32(v1, v5), tsum32(v3, v7)), b3);
}
__device__ __forceinline__ float hi_sum(float x) { x += dpp<ROR8>(x); return xrow_sum(x); }
__device__ __forceinline__ float hi_max(float x) { x = fmx(x, dpp<ROR8>(x)); return xrow_max(x); }
__device__ __forceinline__ float wave_sum(float x) { return hi_sum(sum8(x)); }
__device__ __forceinline__ u32 bfbits(float f) { return (u32)__builtin_bit_cast(u16, (__bf16)f); }
__device__ __forceinline__ float bfr(float f) { return __uint_as_float(bfbits(f) << 16); }
__device__ __forceinline__ u32 pack2(float a, float b) {
  bf16x2 p = {(__bf16)a, (__bf16)b};
  return __builtin_bit_cast(u32, p);
}
__device__ __forceinline__ float blo(u32 v) { return __uint_as_float(v << 16); }
__device__ __forceinline__ float bhi(u32 v) { return __uint_as_float(v & 0xffff0000u); }
__device__ __forceinline__ float ex2(float v) { return __builtin_amdgcn_exp2f(v); }
#define BAR() asm volatile("s_waitcnt lgkmcnt(0)\n\ts_barrier" ::: "memory")
#define PIN() __builtin_amdgcn_sched_barrier(0)
#define CBAR() asm volatile("" ::: "memory")
#define STAMP(i)
#define STR_(x) #x
#define STR(x) STR_(x)
#define KEEP(v) asm volatile("" : "+v"(v))
#define RSRC(p, n) __builtin_amdgcn_make_buffer_rsrc((void*)(p), 0, (n), 0x00020000)
#define LDW(p) __hip_atomic_load((p), __ATOMIC_RELAXED, __HIP_MEMORY_SCOPE_WORKGROUP)
#define STW(p, v) __hip_atomic_store((p), (v), __ATOMIC_RELAXED, __HIP_MEMORY_SCOPE_WORKGROUP)

#define WAITVM(n) __builtin_amdgcn_s_waitcnt(0x0F70 | ((n) & 15) | (((n) >> 4) << 14))

constexpr int L_FL = 0;
constexpr int L_ST = 16;
constexpr int L_QS = L_ST + 32;
constexpr int L_XN = L_QS + 132;
constexpr int L_PART = L_XN + 320;
constexpr int L_OT = L_PART + 8 * 192;
constexpr int L_Y = L_OT + 8 * 16;
constexpr int L_END = L_Y + 8 * 64;
constexpr u32 OOB = 0x80000000u;

struct Sched { int hi[NSLOT + 1]; };
constexpr int S3 = QS + 9 / SG < NSLOT ? QS + 9 / SG : NSLOT;
constexpr Sched make_sched() {
  Sched t{};
  int j = 0;
  for (int n = S3; n < NSLOT; ++n) {
    int burst = 0;
    while (j < NL && burst < PB && n - (NL + j) >= PACE2) { ++j; ++burst; }
    t.hi[n] = j;
  }
  t.hi[NSLOT] = NL;
  return t;
}
constexpr Sched SCH = make_sched();
static_assert(QS >= NL && S3 <= NSLOT, "schedule");

__device__ __forceinline__ float score8(const u32x4 k0, const u32x4 k1, const u32x4 k2, const u32x4 (&q)[3]) {
  float a = dot2(k0.x, q[0].x, 0.f), c = dot2(k0.y, q[0].y, 0.f), e = dot2(k0.z, q[0].z, 0.f), g = dot2(k0.w, q[0].w, 0.f);
  a = dot2(k1.x, q[1].x, a); c = dot2(k1.y, q[1].y, c); e = dot2(k1.z, q[1].z, e); g = dot2(k1.w, q[1].w, g);
  a = dot2(k2.x, q[2].x, a); c = dot2(k2.y, q[2].y, c); e = dot2(k2.z, q[2].z, e); g = dot2(k2.w, q[2].w, g);
  return bfr(bfr(sum8((a + c) + (e + g))) * SCALE);
}
__device__ __forceinline__ void pv_step(const u32x4 d, const float pp, f32x2 (&acc)[4]) {
#pragma unroll
  for (int c = 0; c < 4; ++c) {
    acc[c] = __builtin_elementwise_fma(f32x2{blo(d[c]), bhi(d[c])}, f32x2{pp, pp}, acc[c]);
    KEEP(acc[c]);
  }
}

#define P1_LD(a, b) "buffer_load_dwordx4 v[" #a ":" #b "], %[g], %[rs], %[sb] offen sc1\n"
#define P1_CK(x, y, z, t, gap) \
  "s_waitcnt vmcnt(3)\n" \
  "v_cmp_eq_u32 vcc, v" #y ", %[tag]\n" \
  "v_cmp_eq_u32 %[m], v" #t ", %[tag]\n" \
  "s_and_b64 vcc, vcc, %[m]\n" \
  "v_cndmask_b32 %[c0], %[c0], v" #x ", vcc\n" \
  "v_cndmask_b32 %[c1], %[c1], v" #z ", vcc\n" \
  "s_or_b64 %[dn], %[dn], vcc\n" \
  "s_cmp_eq_u64 %[dn], -1\n" \
  "s_cbranch_scc1 2f\n" \
  "v_cndmask_b32 %[g], %[g], %[oob], vcc\n" \
  "s_sleep " STR(gap) "\n" \
  P1_LD(x, t)
#define POLL1(G, SB, C0, C1, DN, gap0, gap) do { u32 cnt_ = SPIN_CAP; u64 m_; \
  asm volatile( \
  P1_LD(232, 235) "s_sleep " STR(gap0) "\n" P1_LD(236, 239) "s_sleep " STR(gap0) "\n" P1_LD(240, 243) "s_sleep " STR(gap0) "\n" P1_LD(244, 247) \
  "1:\n" \
  P1_CK(232, 233, 234, 235, gap) P1_CK(236, 237, 238, 239, gap) P1_CK(240, 241, 242, 243, gap) P1_CK(244, 245, 246, 247, gap) \
  "s_sub_u32 %[cnt], %[cnt], 1\n" \
  "s_cbranch_scc0 1b\n" \
  "2:\n" \
  : [c0] "+v"(C0), [c1] "+v"(C1), [g] "+v"(G), [dn] "+s"(DN), [cnt] "+s"(cnt_), [m] "=&s"(m_), "=&{v[232:247]}"(P0) \
  : [rs] "s"(rsw), [sb] "s"(SB), [tag] "v"(tag), [oob] "v"(voob) \
  : "vcc", "scc", "memory"); } while (0)
#define POLL1_DRAIN() asm volatile("s_waitcnt vmcnt(0)" :: "{v[232:247]}"(P0) : "memory")

#define P3_LD(a, b, c, d, e, f) \
  "buffer_load_dwordx4 v[" #a ":" #b "], %[r0], %[rs], %[sb] offen sc1\n" \
  "buffer_load_dwordx4 v[" #c ":" #d "], %[r1], %[rs], %[sb] offen sc1\n" \
  "buffer_load_dwordx4 v[" #e ":" #f "], %[r2], %[rs], %[sb] offen sc1\n"
#define P3_C1(n, x, y, z, t, ca, cb, r) \
  "s_waitcnt vmcnt(" #n ")\n" \
  "v_cmp_eq_u32 vcc, v" #y ", %[tag]\n" \
  "v_cmp_eq_u32 %[m], v" #t ", %[tag]\n" \
  "s_and_b64 vcc, vcc, %[m]\n" \
  "v_cndmask_b32 %[" #ca "], %[" #ca "], v" #x ", vcc\n" \
  "v_cndmask_b32 %[" #cb "], %[" #cb "], v" #z ", vcc\n" \
  "v_cndmask_b32 %[" #r "], %[" #r "], %[oob], vcc\n"
#define P3_CK(a0, a1, a2, a3, b0, b1, b2, b3, d0, d1, d2, d3) \
  P3_C1(8, a0, a1, a2, a3, c0, c1, r0) P3_C1(7, b0, b1, b2, b3, c2, c3, r1) P3_C1(6, d0, d1, d2, d3, c4, c5, r2) \
  "v_min3_u32 %[t0], %[r0], %[r1], %[r2]\n" \
  "v_cmp_eq_u32 vcc, %[t0], %[oob]\n" \
  "s_cmp_eq_u64 vcc, -1\n" \
  "s_cbranch_scc1 2f\n" \
  "s_sleep " STR(GAPY) "\n" \
  P3_LD(a0, a3, b0, b3, d0, d3)

__global__ void __launch_bounds__(512, 2)
fused_decode_kernel(const u16* __restrict__ x, const u16* __restrict__ kc, const u16* __restrict__ vc,
                    const u16* __restrict__ wq, const u16* __restrict__ wo, const u16* __restrict__ w1,
                    const u16* __restrict__ w2, u16* __restrict__ out, u32* __restrict__ ws) {
  __shared__ __attribute__((aligned(16))) float lds[L_END];
  const int tid = threadIdx.x;
  const int lane = tid & 63;
  const int w = __builtin_amdgcn_readfirstlane(tid >> 6);
  const int b = blockIdx.x;
  const int h = b >> 3;
  const int s = b & 7;
  const bool idle = s == 7 && (h & 2);
  const int ti = (h & 3) < 3 ? 8 * (h & 3) + s : 23 + s;
  const int tg = 3 * (h >> 2) + ti / 10, tc = ti % 10;
  const int l3 = lane >> 3, l7 = lane & 7;
  const bool b3 = lane & 8;
  const int perm8 = 4 * (lane >> 5) + 2 * ((lane >> 4) & 1) + (l3 & 1);

  const auto rs_ws = RSRC(ws, WS_BYTES);
  const u32x4 rsw = {(u32)(size_t)ws, (u32)((size_t)ws >> 32) & 0xffffu, WS_BYTES, 0x00020000u};
  const size_t kvoff = ((size_t)h * SK + (SK - WLEN) + s * 512) * DH;
  const auto rs_k = RSRC(kc + kvoff, 512 * DH * 2);
  const auto rs_v = RSRC(vc + kvoff, 512 * DH * 2);
  const int kvo = l3 * 384 + l7 * 16;
  const auto rs_wo = RSRC(wo + (size_t)(256 * tg + 32 * w) * D + 64 * tc, idle ? 0 : 31 * 1280 + 128);
  const auto rs_w2 = RSRC(w2 + (size_t)(64 * tg + 8 * w) * D + 64 * tc, idle ? 0 : 7 * 1280 + 128);
  char* const ldb = reinterpret_cast<char*>(lds);
  u32* const fl = reinterpret_cast<u32*>(lds + L_FL);
  u32* const qs = reinterpret_cast<u32*>(lds + L_QS);

  u32x4 wov[5];
#define WISSUE() do { \
    wov[4] = __builtin_amdgcn_raw_buffer_load_b128(rs_w2, l3 * 1280 + l7 * 16, 0, 2); \
    _Pragma("unroll") for (int i = 0; i < 4; ++i) wov[i] = __builtin_amdgcn_raw_buffer_load_b128(rs_wo, l3 * 5120 + l7 * 16 + i * 1280, 0, 2); \
    PIN(); } while (0)
  u32 a_w = (L_PART + w * 192 + 8 * l7 + perm8) * 4, a_r = (L_PART + (lane < 48 ? lane : 47) * 4) * 4;
  u32 o_voff = lane < 48 ? lane * 16 : OOB;
  f32x2 acc[3][4];
#pragma unroll
  for (int u = 0; u < 3; ++u)
#pragma unroll
    for (int c = 0; c < 4; ++c) acc[u][c] = f32x2{0.f, 0.f};
  u32 tag;
  u32x16 P0;
  u32 voob = OOB;

  if (w == 0) {
    __builtin_amdgcn_s_setprio(3);
    const auto rs_x = RSRC(x, D * 2);
    const u32x4 xa = __builtin_amdgcn_raw_buffer_load_b128(rs_x, lane * 20, 0, 0);
    const u32 xb = __builtin_amdgcn_raw_buffer_load_b32(rs_x, lane * 20 + 16, 0, 0);
    const int sec = s < 6 ? 12 * (h >> 1) + 2 * s + (h & 1) : 6 * (h >> 2) + (s == 6 ? (h & 3) : 4 + (h & 1));
    const u32 stp = s < 6 ? 12288u : 3072u;
    const auto rs_t = RSRC((s < 6 ? wq : w1) + sec * 32, idle ? 0 : 639 * stp + 64);
    u32x4 wv[40];
    {
      const u32 tv = (lane >> 2) * 40 * stp + (lane & 3) * 16;
#pragma unroll
      for (int i = 0; i < 40; ++i) {
        wv[i] = __builtin_amdgcn_raw_buffer_load_b128(rs_t, tv, i * stp, 2);
        PIN();
      }
    }
    const u32 E = __builtin_amdgcn_raw_buffer_load_b32(rs_ws, 0, 0, 16);
    if (lane < 48) fl[lane] = 0u;
    PIN();
    BAR();
    float xv[10];
    xv[0] = blo(xa.x); xv[1] = bhi(xa.x); xv[2] = blo(xa.y); xv[3] = bhi(xa.y);
    xv[4] = blo(xa.z); xv[5] = bhi(xa.z); xv[6] = blo(xa.w); xv[7] = bhi(xa.w);
    xv[8] = blo(xb); xv[9] = bhi(xb);
    const float sm = ((xv[0] + xv[1]) + (xv[2] + xv[3])) + ((xv[4] + xv[5]) + (xv[6] + xv[7])) + (xv[8] + xv[9]);
    const float mean = wave_sum(sm) * (1.0f / D);
    float sq = 0.f;
#pragma unroll
    for (int e = 0; e < 10; ++e) { const float d = xv[e] - mean; sq += d * d; }
    const float var = wave_sum(sq) * (1.0f / D);
    const float mb = bfr(mean);
    const float den = bfr(__builtin_amdgcn_sqrtf(bfr(bfr(var) + 1e-5f)));
    const float rinv = 1.0f / den;
    u32* xn = reinterpret_cast<u32*>(lds + L_XN);
#pragma unroll
    for (int e = 0; e < 5; ++e) xn[lane * 5 + e] = pack2(bfr(xv[2 * e] - mb) * rinv, bfr(xv[2 * e + 1] - mb) * rinv);
    u32x4 xs[5];
#pragma unroll
    for (int i = 0; i < 5; ++i) xs[i] = *reinterpret_cast<const u32x4*>(xn + (lane >> 2) * 20 + i * 4);
    STAMP(1);
    float a8[8];
#pragma unroll
    for (int e = 0; e < 8; ++e) a8[e] = 0.f;
#pragma unroll
    for (int i = 0; i < 40; ++i) {
      const u32 v = xs[i / 8][(i / 2) % 4];
      const u32 xl = (i & 1) ? v >> 16 : v & 0xffffu;
      const u32 xh = (i & 1) ? v & 0xffff0000u : v << 16;
#pragma unroll
      for (int d = 0; d < 4; ++d) {
        a8[2 * d] = dot2(wv[i][d], xl, a8[2 * d]);
        a8[2 * d + 1] = dot2(wv[i][d], xh, a8[2 * d + 1]);
        KEEP(a8[2 * d]); KEEP(a8[2 * d + 1]);
      }
      PIN();
    }
    tag = E + 1u + (E == 0xffffffffu);
    STAMP(2);
    {
      float e0 = tsum16(tsum32(a8[0], a8[4]), tsum32(a8[2], a8[6]));
      float e1 = tsum16(tsum32(a8[1], a8[5]), tsum32(a8[3], a8[7]));
      e0 += dpp<ROR8>(e0); e1 += dpp<ROR8>(e1);
      e0 += dpp<ROR4>(e0); e1 += dpp<ROR4>(e1);
      if (s >= 6) { e0 = fmaxf(e0, 0.f); e1 = fmaxf(e1, 0.f); }
      const u32x2 un = {pack2(e0, e1), tag};
      const u32 qv = ((lane & 12) || idle) ? OOB : (4 * (lane & 3) + (lane >> 4 & 1) + 2 * (lane >> 5)) * 8;
      __builtin_amdgcn_raw_buffer_store_b64(un, rs_ws, qv, WS_Q + (s < 6 ? sec : 192 + sec) * 128, 16);
    }
    STAMP(3);
    u32x4 kr[3], vr[3];
#pragma unroll
    for (int i = 0; i < 3; ++i) kr[i] = __builtin_amdgcn_raw_buffer_load_b128(rs_k, kvo + i * 128, 0, 2);
#pragma unroll
    for (int i = 0; i < 3; ++i) vr[i] = __builtin_amdgcn_raw_buffer_load_b128(rs_v, kvo + i * 128, 0, 2);
    PIN();
    {
      u32 g = lane < 48 ? h * 768 + lane * 16 : (idle ? OOB : (192 + 2 * tg) * 128 + (lane - 48) * 16);
      u64 dn = idle ? 0xffff000000000000ull : 0ull;
      u32 c0 = 0, c1 = 0, sb = WS_Q;
      KEEP(voob);
      POLL1(g, sb, c0, c1, dn, GAPQ, GAPQ);
      *reinterpret_cast<u32x2*>(qs + 2 * lane) = u32x2{c0, c1};
    }
    qs[128] = tag;
    CBAR();
    if (lane == 0) STW(fl, 1u);
    STAMP(4);
    __builtin_amdgcn_s_setprio(0);
    POLL1_DRAIN();
    u32x4 q[3];
#pragma unroll
    for (int u = 0; u < 3; ++u) q[u] = *reinterpret_cast<const u32x4*>(qs + u * 32 + l7 * 4);
    const float sco = score8(kr[0], kr[1], kr[2], q);
    const float mw = hi_max(sco);
    const float p = ex2((sco - mw) * LOG2E);
    const float lw = hi_sum(p);
    if (lane == 0) *reinterpret_cast<f32x4*>(lds + L_ST) = f32x4{mw, lw, __uint_as_float(1u), 0.f};
    STAMP(5);
#pragma unroll
    for (int u = 0; u < 3; ++u) pv_step(vr[u], p, acc[u]);
    if (W0EARLY) WISSUE();
  } else {
    const int soff = (8 + (w - 1) * 72) * (DH * 2);
    u32x4 kr[NL], vr[NL];
#define ISSUE(n) do { \
      if ((n) < NL) kr[(n) < NL ? (n) : 0] = __builtin_amdgcn_raw_buffer_load_b128(rs_k, kvo + ((n) % 3) * 128, soff + ((n) / 3) * 3072, 2); \
      else vr[(n) >= NL ? (n) - NL : 0] = __builtin_amdgcn_raw_buffer_load_b128(rs_v, kvo + ((n) % 3) * 128, soff + (((n) - NL) / 3) * 3072, 2); \
      PIN(); } while (0)
#define GATE(n, p) WAITVM((p) - 1)
#pragma unroll
    for (int n = 0; n < PACE; ++n) ISSUE(n);
    asm volatile("s_barrier" ::: "memory");
    KEEP(a_w); KEEP(a_r); KEEP(o_voff);
#pragma unroll
    for (int n = PACE; n < QS; ++n) { GATE(n, PACE); ISSUE(n); }
    STAMP(3);
    {
      int spins = 0;
      while (LDW(fl) == 0u && ++spins < 100 * SPIN_CAP) __builtin_amdgcn_s_sleep(1);
      CBAR();
    }
    STAMP(4);
    u32x4 q[3];
#pragma unroll
    for (int u = 0; u < 3; ++u) q[u] = *reinterpret_cast<const u32x4*>(qs + u * 32 + l7 * 4);
    tag = qs[128];
    float sc[9];
#pragma unroll
    for (int i = 0; i < 9; ++i) {
      sc[i] = score8(kr[3 * i], kr[3 * i + 1], kr[3 * i + 2], q);
      KEEP(sc[i]);
      PIN();
      if (i % SG == SG - 1 && QS + i / SG < NSLOT) { GATE(QS + i / SG, PACE2); ISSUE(QS + i / SG); }
    }
    const float mw = hi_max(fmx(fmx(fmx(sc[0], sc[1]), fmx(sc[2], sc[3])), fmx(fmx(fmx(sc[4], sc[5]), fmx(sc[6], sc[7])), sc[8])));
#pragma unroll
    for (int i = 0; i < 9; ++i) sc[i] = ex2((sc[i] - mw) * LOG2E);
    const float lw = hi_sum((((sc[0] + sc[1]) + (sc[2] + sc[3])) + ((sc[4] + sc[5]) + (sc[6] + sc[7]))) + sc[8]);
    if (lane == 0) *reinterpret_cast<f32x4*>(lds + L_ST + 4 * w) = f32x4{mw, lw, __uint_as_float(1u), 0.f};
    STAMP(5);
#pragma unroll
    for (int n = S3; n <= NSLOT; ++n) {
#pragma unroll
      for (int j = (n == S3 ? 0 : SCH.hi[n - 1]); j < SCH.hi[n]; ++j) {
        pv_step(vr[j], sc[j / 3], acc[j % 3]);
        PIN();
      }
      if (n < NSLOT) { GATE(n, PACE2); ISSUE(n); }
    }
  }
  STAMP(6);
#pragma unroll
  for (int u = 0; u < 3; ++u)
    *reinterpret_cast<float*>(ldb + a_w + u * 256) =
        tsum_hi8(acc[u][0].x, acc[u][0].y, acc[u][1].x, acc[u][1].y, acc[u][2].x, acc[u][2].y, acc[u][3].x, acc[u][3].y, b3);
  if (w == 0) {
    f32x4 ml;
    int spins = 0;
    while (true) {
      CBAR();
      ml = *reinterpret_cast<const f32x4*>(lds + L_ST + 4 * l7);
      if (__all(__float_as_uint(ml.z) != 0u) || ++spins >= 100 * SPIN_CAP) break;
      __builtin_amdgcn_s_sleep(4);
    }
    const float M = max8(ml.x);
    const float f = ex2((ml.x - M) * LOG2E);
    const float L = sum8(f * ml.y);
    const u32x4 un = {__float_as_uint(M), tag, __float_as_uint(L), tag};
    __builtin_amdgcn_raw_buffer_store_b128(un, rs_ws, lane == 0 ? 0u : OOB, WS_S + b * 16, 16);
    if (lane < 8) lds[L_FL + 8 + lane] = f * __builtin_amdgcn_rcpf(L);
    STAMP(14);
  }
  u32 old = 0;
  {
    CBAR();
    if (lane == 0) old = __hip_atomic_fetch_add(fl + 1, 1u, __ATOMIC_RELAXED, __HIP_MEMORY_SCOPE_WORKGROUP);
    CBAR();
    const f32x4 g0 = *reinterpret_cast<const f32x4*>(lds + L_FL + 8), g1 = *reinterpret_cast<const f32x4*>(lds + L_FL + 12);
    f32x4 o[8];
#pragma unroll
    for (int r = 0; r < 8; ++r) o[r] = *reinterpret_cast<const f32x4*>(ldb + a_r + r * 768);
#pragma unroll
    for (int r = 0; r < 8; ++r) KEEP(o[r]);
    old = __builtin_amdgcn_readfirstlane(old);
    if (old == 7u) {
      const f32x4 t = ((g0.x * o[0] + g0.y * o[1]) + (g0.z * o[2] + g0.w * o[3])) + ((g1.x * o[4] + g1.y * o[5]) + (g1.z * o[6] + g1.w * o[7]));
      const u32x4 un = {pack2(t.x, t.y), tag, pack2(t.z, t.w), tag};
      __builtin_amdgcn_raw_buffer_store_b128(un, rs_ws, o_voff, WS_O + b * 768, 16);
      STAMP(7);
    }
  }
  if (w) STAMP(1);
  KEEP(voob);
  if (!idle) {
    if (!(W0EARLY && w == 0)) {
      int spins = 0;
      u32 cnt = old + 1u;
      while (cnt < WTHR && ++spins < 100 * SPIN_CAP) { __builtin_amdgcn_s_sleep(2); cnt = LDW(fl + 1); }
      WISSUE();
    }
    const int R = 8 * tg + w, hh = R / 6;
    float wt;
    {
      u32x4 un;
      int spins = 0;
      while (true) {
        CBAR();
        un = __builtin_amdgcn_raw_buffer_load_b128(rs_ws, l3 * 16, WS_S + hh * 128, 16);
        if (__all(un.y == tag && un.w == tag) || ++spins >= SPIN_CAP) break;
        __builtin_amdgcn_s_sleep(8);
      }
      const float mp = __uint_as_float(un.x);
      const float f = ex2((mp - hi_max(mp)) * LOG2E) * __uint_as_float(un.z);
      wt = f * __builtin_amdgcn_rcpf(hi_sum(f));
    }
    if (w) STAMP(14);
    u32 wp[2][8];
    float y[8];
    {
      const u32 hid = reinterpret_cast<const u16*>(qs)[192 + 8 * w + l3];
#pragma unroll
      for (int d = 0; d < 4; ++d) {
        y[2 * d] = dot2(wov[4][d], hid, 0.f);
        y[2 * d + 1] = dot2(wov[4][d], hid << 16, 0.f);
        KEEP(y[2 * d]); KEEP(y[2 * d + 1]);
      }
#pragma unroll
      for (int j = 0; j < 2; ++j)
#pragma unroll
        for (int d = 0; d < 4; ++d) {
          wp[j][2 * d] = __builtin_amdgcn_perm(wov[2 * j + 1][d], wov[2 * j][d], 0x05040100u);
          wp[j][2 * d + 1] = __builtin_amdgcn_perm(wov[2 * j + 1][d], wov[2 * j][d], 0x07060302u);
          KEEP(wp[j][2 * d]); KEEP(wp[j][2 * d + 1]);
        }
    }
    u32 a_ow = (L_OT + w * 16) * 4 + (4 * l7 + 2 * (lane >> 5) + ((lane >> 4) & 1)) * 2, a_or = (L_OT + w * 16) * 4 + l3 * 8;
    u32 a_yw = (L_Y + w * 64 + 8 * l7 + perm8) * 4, a_yr = (L_Y + lane) * 4;
    u32 y_voff = (lane >> 4) * 3072 + (lane & 15) * 8;
    u32 g = l3 * 768 + l7 * 16, sb = WS_O + hh * 6144 + (R - 6 * hh) * 128, yb = WS_Y + (tc * 96 + tg) * 128;
    KEEP(a_ow); KEEP(a_or); KEEP(a_yw); KEEP(a_yr); KEEP(y_voff); KEEP(wt);
    asm volatile("" : "+s"(sb), "+s"(yb));
    if (w) STAMP(2);
    u32 c0 = 0, c1 = 0;
    u64 dn = 0;
    POLL1(g, sb, c0, c1, dn, GAPO0, GAPO);
    STAMP(8);
    float t = tsum16(tsum32(wt * blo(c0), wt * blo(c1)), tsum32(wt * bhi(c0), wt * bhi(c1)));
    t += dpp<ROR8>(t);
    *reinterpret_cast<u16*>(ldb + a_ow) = (u16)bfbits(t);
    CBAR();
    const u32x2 ov = *reinterpret_cast<const u32x2*>(ldb + a_or);
#pragma unroll
    for (int j = 0; j < 2; ++j)
#pragma unroll
      for (int e = 0; e < 8; ++e) y[e] = dot2(wp[j][e], ov[j], y[e]);
    *reinterpret_cast<float*>(ldb + a_yw) = tsum_hi8(y[0], y[1], y[2], y[3], y[4], y[5], y[6], y[7], b3);
    BAR();
    STAMP(9);
    if (w == 0) {
      float e[8];
#pragma unroll
      for (int r = 0; r < 8; ++r) e[r] = *reinterpret_cast<const float*>(ldb + a_yr + r * 256);
      const u32x2 un = {__float_as_uint(((e[0] + e[1]) + (e[2] + e[3])) + ((e[4] + e[5]) + (e[6] + e[7]))), tag};
      __builtin_amdgcn_raw_buffer_store_b64(un, rs_ws, y_voff, yb, 16);
      STAMP(10);
    }
    POLL1_DRAIN();
  } else {
    const int ri = (h >> 2) * 2 + (h & 1) + 16 * (w - 1);
    if (w >= 1 && ri < 40) {
      const int rcol = 16 * ri + 2 * l7 + (lane >> 5);
      u32 xo = x[rcol];
      u32 ov = (lane & 24) ? OOB : rcol * 2;
      u32 r0 = lane * 16, r1 = r0 + 1024, r2 = r0 + 2048, sb = WS_Y + ri * 3072;
      KEEP(xo); KEEP(ov);
      u32x16 R0, R1;
      u32x4 R2;
      u32 c[6] = {0, 0, 0, 0, 0, 0};
      u32 t0, cnt = SPIN_CAP;
      u64 m;
      asm volatile(
P3_LD(212, 215, 216, 219, 220, 223) "s_sleep " STR(GAPY0) "\n"
P3_LD(224, 227, 228, 231, 232, 235) "s_sleep " STR(GAPY0) "\n"
P3_LD(236, 239, 240, 243, 244, 247)
"1:\n"
P3_CK(212, 213, 214, 215, 216, 217, 218, 219, 220, 221, 222, 223)
P3_CK(224, 225, 226, 227, 228, 229, 230, 231, 232, 233, 234, 235)
P3_CK(236, 237, 238, 239, 240, 241, 242, 243, 244, 245, 246, 247)
"s_sub_u32 %[cnt], %[cnt], 1\n"
"s_cbranch_scc0 1b\n"
"2:\n"
: [c0] "+v"(c[0]), [c1] "+v"(c[1]), [c2] "+v"(c[2]), [c3] "+v"(c[3]), [c4] "+v"(c[4]), [c5] "+v"(c[5]),
  [r0] "+v"(r0), [r1] "+v"(r1), [r2] "+v"(r2),
  [t0] "=&v"(t0), [cnt] "+s"(cnt), [m] "=&s"(m), "=&{v[212:227]}"(R0), "=&{v[228:243]}"(R1), "=&{v[244:247]}"(R2)
: [rs] "s"(rsw), [sb] "s"(sb), [tag] "v"(tag), [oob] "v"(voob)
: "vcc", "scc", "memory");
      STAMP(11);
      float v = tsum32((__uint_as_float(c[0]) + __uint_as_float(c[2])) + __uint_as_float(c[4]),
                       (__uint_as_float(c[1]) + __uint_as_float(c[3])) + __uint_as_float(c[5]));
      v = tsum16(v, v);
      v += dpp<ROR8>(v);
      __builtin_amdgcn_raw_buffer_store_b16((u16)bfbits(v + blo(xo)), RSRC(out, D * 2), ov, 0, 0);
      if (ri == 0 && lane == 0) __builtin_amdgcn_raw_buffer_store_b32(tag, rs_ws, 0, 0, 16);
      asm volatile("s_waitcnt vmcnt(0)" :: "{v[212:227]}"(R0), "{v[228:243]}"(R1), "{v[244:247]}"(R2) : "memory");
    }
  }
}

extern "C" void kernel_launch(void* const* d_in, const int* in_sizes, int n_in,
                              void* d_out, int out_size, void* d_ws, size_t ws_size,
                              hipStream_t stream) {
  if (ws_size < WS_BYTES) return;
  fused_decode_kernel<<<256, 512, 0, stream>>>(
      (const u16*)d_in[0], (const u16*)d_in[1], (const u16*)d_in[2], (const u16*)d_in[3],
      (const u16*)d_in[4], (const u16*)d_in[6], (const u16*)d_in[7], (u16*)d_out, (u32*)d_ws);
}
```

```cpp
#define AMPK 0
#define AMPW (b==0&&w==0)
#define SGN 1
#define TL 16000
#include <hip/hip_runtime.h>
#include <hip/hip_bf16.h>
#include <stdint.h>

#define PACE 6
#define PACE2 PACE
#define WTHR 8
#define W0EARLY 0
#define TP 0
#define TA 0
#define PMAX PACE
#define AS 1000
#define AT 265
#define AD2 800
#define GW 0
#define GW0 8
#define LAG 20
#define NQ 2
#define ROWORD 1
#define ONE 1
#define SWZ 1
#define ROT 0
#define GAPS 16
#define GAPX0 4
#define GAPX 4
#define GAPQ 10
#define GAPO0 5
#define GAPO 2
#define GAPY0 6
#define GAPY 2
#define QS 33
#define SG 1
#define PB 2

typedef unsigned int u32;
typedef unsigned short u16;
typedef unsigned long long u64;
using u32x2 = __attribute__((ext_vector_type(2))) unsigned;
using u32x4 = __attribute__((ext_vector_type(4))) unsigned;
using u32x16 = __attribute__((ext_vector_type(16))) unsigned;
using f32x2 = __attribute__((ext_vector_type(2))) float;
using f32x4 = __attribute__((ext_vector_type(4))) float;
using bf16x2 = __attribute__((ext_vector_type(2))) __bf16;

constexpr int D = 640, DH = 192, SK = 32768, WLEN = 4096;
constexpr int NL = 27;
constexpr int NSLOT = 2 * NL;
constexpr u32 WS_Q = 4096;
constexpr u32 WS_S = WS_Q + 240u * 128u;
constexpr u32 WS_O = WS_S + 32u * 128u;
constexpr u32 WS_Y = WS_O + 256u * 768u;
constexpr u32 WS_X = WS_Y + 960u * 128u;
constexpr u32 WS_BYTES = WS_X + 240u * NQ * 256u;
constexpr int LPR = 4 * NQ;
constexpr int SPIN_CAP = 200000;
constexpr float LOG2E = 1.4426950408889634f;
constexpr float SCALE = 0.07216878364870322f;

__device__ __forceinline__ float dot2(u32 a, u32 b, float acc) {
  return __builtin_amdgcn_fdot2_f32_bf16(__builtin_bit_cast(bf16x2, a), __builtin_bit_cast(bf16x2, b), acc, false);
}
template <int CTRL> __device__ __forceinline__ float dpp(float x) {
  return __builtin_bit_cast(float, __builtin_amdgcn_mov_dpp(__builtin_bit_cast(int, x), CTRL, 0xf, 0xf, true));
}
constexpr int XOR1 = 0xB1, XOR2 = 0x4E, XOR7 = 0x141, ROR8 = 0x128, ROR4 = 0x124;
__device__ __forceinline__ float sum8(float x) {
  x += dpp<XOR1>(x); x += dpp<XOR2>(x); x += dpp<XOR7>(x); return x;
}
__device__ __forceinline__ float fmx(float a, float b) {
  return __builtin_fmaxf(a, b);
}
__device__ __forceinline__ float max8(float x) {
  x = fmx(x, dpp<XOR1>(x)); x = fmx(x, dpp<XOR2>(x)); x = fmx(x, dpp<XOR7>(x)); return x;
}
__device__ __forceinline__ float xrow_sum(float x) {
  auto s = __builtin_amdgcn_permlane16_swap(__float_as_uint(x), __float_as_uint(x), false, false);
  x = __uint_as_float(s[0]) + __uint_as_float(s[1]);
  auto t = __builtin_amdgcn_permlane32_swap(__float_as_uint(x), __float_as_uint(x), false, false);
  return __uint_as_float(t[0]) + __uint_as_float(t[1]);
}
__device__ __forceinline__ float xrow_max(float x) {
  auto s = __builtin_amdgcn_permlane16_swap(__float_as_uint(x), __float_as_uint(x), false, false);
  x = fmx(__uint_as_float(s[0]), __uint_as_float(s[1]));
  auto t = __builtin_amdgcn_permlane32_swap(__float_as_uint(x), __float_as_uint(x), false, false);
  return fmx(__uint_as_float(t[0]), __uint_as_float(t[1]));
}
__device__ __forceinline__ float tsum32(float a, float b) {
  auto t = __builtin_amdgcn_permlane32_swap(__float_as_uint(a), __float_as_uint(b), false, false);
  return __uint_as_float(t[0]) + __uint_as_float(t[1]);
}
__device__ __forceinline__ float tsum16(float a, float b) {
  auto t = __builtin_amdgcn_permlane16_swap(__float_as_uint(a), __float_as_uint(b), false, false);
  return __uint_as_float(t[0]) + __uint_as_float(t[1]);
}
__device__ __forceinline__ float tsum8(float a, float b, bool b3) {
  const float snd = b3 ? a : b, kp = b3 ? b : a;
  return kp + dpp<ROR8>(snd);
}
__device__ __forceinline__ float tsum_hi8(float v0, float v1, float v2, float v3, float v4, float v5, float v6, float v7, bool b3) {
  return tsum8(tsum16(tsum32(v0, v4), tsum32(v2, v6)), tsum16(tsum32(v1, v5), tsum32(v3, v7)), b3);
}
__device__ __forceinline__ float hi_sum(float x) { x += dpp<ROR8>(x); return xrow_sum(x); }
__device__ __forceinline__ float hi_max(float x) { x = fmx(x, dpp<ROR8>(x)); return xrow_max(x); }
__device__ __forceinline__ float wave_sum(float x) { return hi_sum(sum8(x)); }
__device__ __forceinline__ u32 bfbits(float f) { return (u32)__builtin_bit_cast(u16, (__bf16)f); }
__device__ __forceinline__ float bfr(float f) { return __uint_as_float(bfbits(f) << 16); }
__device__ __forceinline__ u32 pack2(float a, float b) {
  bf16x2 p = {(__bf16)a, (__bf16)b};
  return __builtin_bit_cast(u32, p);
}
__device__ __forceinline__ float blo(u32 v) { return __uint_as_float(v << 16); }
__device__ __forceinline__ float bhi(u32 v) { return __uint_as_float(v & 0xffff0000u); }
__device__ __forceinline__ float ex2(float v) { return __builtin_amdgcn_exp2f(v); }
#define BAR() asm volatile("s_waitcnt lgkmcnt(0)\n\ts_barrier" ::: "memory")
#define PIN() __builtin_amdgcn_sched_barrier(0)
#define CBAR() asm volatile("" ::: "memory")
#define STAMP(i) do { if ((i) == AMPK) { PIN(); tsel = (u32)__builtin_amdgcn_s_memrealtime(); PIN(); } } while (0)
#define STR_(x) #x
#define STR(x) STR_(x)
#define KEEP(v) asm volatile("" : "+v"(v))
#define RSRC(p, n) __builtin_amdgcn_make_buffer_rsrc((void*)(p), 0, (n), 0x00020000)
#define LDW(p) __hip_atomic_load((p), __ATOMIC_RELAXED, __HIP_MEMORY_SCOPE_WORKGROUP)
#define STW(p, v) __hip_atomic_store((p), (v), __ATOMIC_RELAXED, __HIP_MEMORY_SCOPE_WORKGROUP)

#define WAITVM(n) __builtin_amdgcn_s_waitcnt(0x0F70 | ((n) & 15) | (((n) >> 4) << 14))

constexpr int L_FL = 0;
constexpr int L_ST = 16;
constexpr int L_QS = L_ST + 32;
constexpr int L_XN = L_QS + 132;
constexpr int L_PART = L_XN + 320;
constexpr int L_OT = L_PART + 8 * 192;
constexpr int L_Y = L_OT + 8 * 16;
constexpr int L_WT = L_Y + 8 * 64;
constexpr int L_END = L_WT + 16;
constexpr u32 OOB = 0x80000000u;

struct Sched { int hi[NSLOT + 1]; };
constexpr int S3 = QS + 9 / SG < NSLOT ? QS + 9 / SG : NSLOT;
constexpr Sched make_sched() {
  Sched t{};
  int j = 0;
  for (int n = S3; n < NSLOT; ++n) {
    int burst = 0;
    while (j < NL && burst < PB && n - (NL + j) >= PMAX) { ++j; ++burst; }
    t.hi[n] = j;
  }
  t.hi[NSLOT] = NL;
  return t;
}
constexpr Sched SCH = make_sched();
static_assert(QS >= NL && S3 <= NSLOT, "schedule");

__device__ __forceinline__ float score8(const u32x4 k0, const u32x4 k1, const u32x4 k2, const u32x4 (&q)[3]) {
  float a = dot2(k0.x, q[0].x, 0.f), c = dot2(k0.y, q[0].y, 0.f), e = dot2(k0.z, q[0].z, 0.f), g = dot2(k0.w, q[0].w, 0.f);
  a = dot2(k1.x, q[1].x, a); c = dot2(k1.y, q[1].y, c); e = dot2(k1.z, q[1].z, e); g = dot2(k1.w, q[1].w, g);
  a = dot2(k2.x, q[2].x, a); c = dot2(k2.y, q[2].y, c); e = dot2(k2.z, q[2].z, e); g = dot2(k2.w, q[2].w, g);
  return bfr(bfr(sum8((a + c) + (e + g))) * SCALE);
}
__device__ __forceinline__ void pv_step(const u32x4 d, const float pp, f32x2 (&acc)[4]) {
#pragma unroll
  for (int c = 0; c < 4; ++c) {
    acc[c] = __builtin_elementwise_fma(f32x2{blo(d[c]), bhi(d[c])}, f32x2{pp, pp}, acc[c]);
    KEEP(acc[c]);
  }
}

#define P1_LD(a, b) "buffer_load_dwordx4 v[" #a ":" #b "], %[g], %[rs], %[sb] offen sc1\n"
#define P1_CK(x, y, z, t, gap) \
  "s_waitcnt vmcnt(3)\n" \
  "v_cmp_eq_u32 vcc, v" #y ", %[tag]\n" \
  "v_cmp_eq_u32 %[m], v" #t ", %[tag]\n" \
  "s_and_b64 vcc, vcc, %[m]\n" \
  "v_cndmask_b32 %[c0], %[c0], v" #x ", vcc\n" \
  "v_cndmask_b32 %[c1], %[c1], v" #z ", vcc\n" \
  "s_or_b64 %[dn], %[dn], vcc\n" \
  "s_cmp_eq_u64 %[dn], -1\n" \
  "s_cbranch_scc1 2f\n" \
  "v_cndmask_b32 %[g], %[g], %[oob], vcc\n" \
  "s_sleep " STR(gap) "\n" \
  P1_LD(x, t)
#define POLL1C(CON, G, SB, C0, C1, DN, gap0, gap) do { u32 cnt_ = SPIN_CAP; u64 m_; \
  asm volatile( \
  P1_LD(232, 235) "s_sleep " STR(gap0) "\n" P1_LD(236, 239) "s_sleep " STR(gap0) "\n" P1_LD(240, 243) "s_sleep " STR(gap0) "\n" P1_LD(244, 247) \
  "1:\n" \
  P1_CK(232, 233, 234, 235, gap) P1_CK(236, 237, 238, 239, gap) P1_CK(240, 241, 242, 243, gap) P1_CK(244, 245, 246, 247, gap) \
  "s_sub_u32 %[cnt], %[cnt], 1\n" \
  "s_cbranch_scc0 1b\n" \
  "2:\n" \
  : [c0] "+v"(C0), [c1] "+v"(C1), [g] "+v"(G), [dn] "+s"(DN), [cnt] "+s"(cnt_), [m] "=&s"(m_), CON "{v[232:247]}"(P0) \
  : [rs] "s"(rsw), [sb] "s"(SB), [tag] "v"(tag), [oob] "v"(voob) \
  : "vcc", "scc", "memory"); } while (0)
#define POLL1(G, SB, C0, C1, DN, gap0, gap) POLL1C("=&", G, SB, C0, C1, DN, gap0, gap)
#define POLL1B(G, SB, C0, C1, DN, gap0, gap) POLL1C("+", G, SB, C0, C1, DN, gap0, gap)
#define POLL1_DRAIN() asm volatile("s_waitcnt vmcnt(0)" :: "{v[232:247]}"(P0) : "memory")

#define P3_LD(a, b, c, d, e, f) \
  "buffer_load_dwordx4 v[" #a ":" #b "], %[r0], %[rs], %[sb] offen sc1\n" \
  "buffer_load_dwordx4 v[" #c ":" #d "], %[r1], %[rs], %[sb] offen sc1\n" \
  "buffer_load_dwordx4 v[" #e ":" #f "], %[r2], %[rs], %[sb] offen sc1\n"
#define P3_C1(n, x, y, z, t, ca, cb, r) \
  "s_waitcnt vmcnt(" #n ")\n" \
  "v_cmp_eq_u32 vcc, v" #y ", %[tag]\n" \
  "v_cmp_eq_u32 %[m], v" #t ", %[tag]\n" \
  "s_and_b64 vcc, vcc, %[m]\n" \
  "v_cndmask_b32 %[" #ca "], %[" #ca "], v" #x ", vcc\n" \
  "v_cndmask_b32 %[" #cb "], %[" #cb "], v" #z ", vcc\n" \
  "v_cndmask_b32 %[" #r "], %[" #r "], %[oob], vcc\n"
#define P3_CK(a0, a1, a2, a3, b0, b1, b2, b3, d0, d1, d2, d3) \
  P3_C1(8, a0, a1, a2, a3, c0, c1, r0) P3_C1(7, b0, b1, b2, b3, c2, c3, r1) P3_C1(6, d0, d1, d2, d3, c4, c5, r2) \
  "v_min3_u32 %[t0], %[r0], %[r1], %[r2]\n" \
  "v_cmp_eq_u32 vcc, %[t0], %[oob]\n" \
  "s_cmp_eq_u64 vcc, -1\n" \
  "s_cbranch_scc1 2f\n" \
  "s_sleep " STR(GAPY) "\n" \
  P3_LD(a0, a3, b0, b3, d0, d3)

__global__ void __launch_bounds__(512, 2)
fused_decode_kernel(const u16* __restrict__ x, const u16* __restrict__ kc, const u16* __restrict__ vc,
                    const u16* __restrict__ wq, const u16* __restrict__ wo, const u16* __restrict__ w1,
                    const u16* __restrict__ w2, u16* __restrict__ out, u32* __restrict__ ws) {
  __shared__ __attribute__((aligned(16))) float lds[L_END];
  u64 tst64;
  asm volatile("s_memrealtime %0\n\ts_waitcnt lgkmcnt(0)" : "=s"(tst64) :: "memory");
  PIN();
  const u32 tst = (u32)tst64;
  u32 tsel = AMPK == 0 ? tst : 0u;
  const int tid = threadIdx.x;
  const int lane = tid & 63;
  const int w = __builtin_amdgcn_readfirstlane(tid >> 6);
  const int b = blockIdx.x;
  const int h = b >> 3;
  const int s = b & 7;
  const bool idle = s == 7 && (h & 2);
  const int ti = (h & 3) < 3 ? 8 * (h & 3) + s : 23 + s;
  const int tg = 3 * (h >> 2) + ti / 10, tc = ti % 10;
  const int l3 = lane >> 3, l7 = lane & 7;
  const bool b3 = lane & 8;
  const int perm8 = 4 * (lane >> 5) + 2 * ((lane >> 4) & 1) + (l3 & 1);

  const auto rs_ws = RSRC(ws, WS_BYTES);
  const u32x4 rsw = {(u32)(size_t)ws, (u32)((size_t)ws >> 32) & 0xffffu, WS_BYTES, 0x00020000u};
  const size_t kvoff = ((size_t)h * SK + (SK - WLEN) + (SWZ ? (s + h) & 7 : s) * 512) * DH;
  const auto rs_k = RSRC(kc + kvoff, 512 * DH * 2);
  const auto rs_v = RSRC(vc + kvoff, 512 * DH * 2);
  const int kvo = l3 * 384 + l7 * 16;
  const u16* const p_wo = wo + (size_t)(256 * tg + 32 * w) * D + 64 * tc;
  const u16* const p_w2 = w2 + (size_t)(64 * tg + 8 * w) * D + 64 * tc;
  char* const ldb = reinterpret_cast<char*>(lds);
  u32* const fl = reinterpret_cast<u32*>(lds + L_FL);
  u32* const qs = reinterpret_cast<u32*>(lds + L_QS);

  u32x4 wov[5];
  u32 dm = 0;
#define WISSUE(on) do { \
    const auto rs_wo = RSRC(p_wo, (on) ? 31 * 1280 + 128 : 0); \
    const auto rs_w2 = RSRC(p_w2, (on) ? 7 * 1280 + 128 : 0); \
    wov[4] = __builtin_amdgcn_raw_buffer_load_b128(rs_w2, l3 * 1280 + l7 * 16, 0, 2); \
    _Pragma("unroll") for (int i = 0; i < 4; ++i) wov[i] = __builtin_amdgcn_raw_buffer_load_b128(rs_wo, l3 * 5120 + l7 * 16 + i * 1280, 0, 2); \
    PIN(); dm = __builtin_amdgcn_raw_buffer_load_b32(rs_w2, OOB, 0, 2); PIN(); } while (0)
#define PART(u) do { *reinterpret_cast<float*>(ldb + a_w + (u) * 256) = \
    tsum_hi8(acc[u][0].x, acc[u][0].y, acc[u][1].x, acc[u][1].y, acc[u][2].x, acc[u][2].y, acc[u][3].x, acc[u][3].y, b3); PIN(); } while (0)
  bool late = false;
  u32 a_w = (L_PART + w * 192 + 8 * l7 + perm8) * 4, a_r = (L_PART + (lane < 48 ? lane : 47) * 4) * 4;
  u32 o_voff = lane < 48 ? lane * 16 : OOB;
  f32x2 acc[3][4];
#pragma unroll
  for (int u = 0; u < 3; ++u)
#pragma unroll
    for (int c = 0; c < 4; ++c) acc[u][c] = f32x2{0.f, 0.f};
  u32 tag;
  u32x16 P0;
  u32 voob = OOB;

  if (w == 0) {
    __builtin_amdgcn_s_setprio(3);
    const auto rs_x = RSRC(x, D * 2);
    const u32x4 xa = __builtin_amdgcn_raw_buffer_load_b128(rs_x, lane * 20, 0, 0);
    const u32 xb = __builtin_amdgcn_raw_buffer_load_b32(rs_x, lane * 20 + 16, 0, 0);
    const int sec = s < 6 ? 12 * (h >> 1) + 2 * s + (h & 1) : 6 * (h >> 2) + (s == 6 ? (h & 3) : 4 + (h & 1));
    const u32 stp = s < 6 ? 12288u : 3072u;
    const int cg = sec / NQ, rg = sec % NQ;
    const auto rs_t = RSRC((s < 6 ? wq : w1) + (size_t)rg * (640 / NQ) * (stp / 2) + cg * NQ * 32, idle ? 0 : (640 / NQ - 1) * stp + NQ * 64);
    u32x4 wv[40];
    const u32 tv = (lane / LPR) * (ROWORD ? 1 : 40) * stp + (lane % LPR) * 16;
    const u32 E = __builtin_amdgcn_raw_buffer_load_b32(rs_ws, 0, 0, 16);
#define TISSUE(i) do { wv[i] = __builtin_amdgcn_raw_buffer_load_b128(rs_t, tv, (i) * (ROWORD ? 64 / LPR : 1) * stp, 2); PIN(); } while (0)
#pragma unroll
    for (int i = 0; i < (GW ? GW0 : 40); ++i) TISSUE(i);
    u32x4 kr[3], vr[3];
#pragma unroll
    for (int i = 0; i < 3; ++i) kr[i] = __builtin_amdgcn_raw_buffer_load_b128(rs_k, kvo + i * 128, 0, 2);
#pragma unroll
    for (int i = 0; i < 3; ++i) vr[i] = __builtin_amdgcn_raw_buffer_load_b128(rs_v, kvo + i * 128, 0, 2);
    if (lane < 48) fl[lane] = 0u;
    PIN();
    BAR();
    float xv[10];
    xv[0] = blo(xa.x); xv[1] = bhi(xa.x); xv[2] = blo(xa.y); xv[3] = bhi(xa.y);
    xv[4] = blo(xa.z); xv[5] = bhi(xa.z); xv[6] = blo(xa.w); xv[7] = bhi(xa.w);
    xv[8] = blo(xb); xv[9] = bhi(xb);
    const float sm = ((xv[0] + xv[1]) + (xv[2] + xv[3])) + ((xv[4] + xv[5]) + (xv[6] + xv[7])) + (xv[8] + xv[9]);
    const float mean = wave_sum(sm) * (1.0f / D);
    float sq = 0.f;
#pragma unroll
    for (int e = 0; e < 10; ++e) { const float d = xv[e] - mean; sq += d * d; }
    const float var = wave_sum(sq) * (1.0f / D);
    const float mb = bfr(mean);
    const float den = bfr(__builtin_amdgcn_sqrtf(bfr(bfr(var) + 1e-5f)));
    const float rinv = 1.0f / den;
    u32* xn = reinterpret_cast<u32*>(lds + L_XN);
#pragma unroll
    for (int e = 0; e < 10; ++e) {
      const u32 r = lane * 10 + e, rr = r % (640 / NQ);
      reinterpret_cast<u16*>(xn)[r - rr + (rr % (64 / LPR)) * 40 + rr / (64 / LPR)] = (u16)bfbits(bfr(xv[e] - mb) * rinv);
    }
    u32x4 xs[5];
#pragma unroll
    for (int i = 0; i < 5; ++i) xs[i] = *reinterpret_cast<const u32x4*>(xn + rg * (320 / NQ) + (lane / LPR) * 20 + i * 4);
    STAMP(1);
    float a8[8];
#pragma unroll
    for (int e = 0; e < 8; ++e) a8[e] = 0.f;
#pragma unroll
    for (int i = 0; i < 40; ++i) {
      const u32 v = xs[i / 8][(i / 2) % 4];
      const u32 xl = (i & 1) ? v >> 16 : v & 0xffffu;
      const u32 xh = (i & 1) ? v & 0xffff0000u : v << 16;
#pragma unroll
      for (int d = 0; d < 4; ++d) {
        a8[2 * d] = dot2(wv[i][d], xl, a8[2 * d]);
        a8[2 * d + 1] = dot2(wv[i][d], xh, a8[2 * d + 1]);
        KEEP(a8[2 * d]); KEEP(a8[2 * d + 1]);
      }
      PIN();
    }
    tag = E + 1u + (E == 0xffffffffu);
    if (b == 0 && tid == 0) { u32x2 g = {tst, tag}; __builtin_amdgcn_raw_buffer_store_b64(g, rs_ws, 64, 0, 16); }
    STAMP(2);
    {
      float e0 = tsum16(tsum32(a8[0], a8[4]), tsum32(a8[2], a8[6]));
      float e1 = tsum16(tsum32(a8[1], a8[5]), tsum32(a8[3], a8[7]));
      e0 += dpp<ROR8>(e0); e1 += dpp<ROR8>(e1);
      const int gs = s < 6 ? sec : 192 + sec;
      const u32x4 un = {__float_as_uint(e0), tag, __float_as_uint(e1), tag};
      const u32 xv_ = (b3 || idle) ? OOB : (4 * l7 + (lane >> 4 & 1) + 2 * (lane >> 5)) * 16;
      __builtin_amdgcn_raw_buffer_store_b128(un, rs_ws, xv_, WS_X + gs * 512, 16);
      STAMP(3);
      u32 r[4], c[4][2];
#pragma unroll
      for (int j = 0; j < 4; ++j) { r[j] = (j < 3 ? 3 * h + j : 96 + tg) * 1024 + lane * 16; c[j][0] = c[j][1] = 0u; }
      if (idle) r[3] = OOB;
      __builtin_amdgcn_s_sleep(GAPX0);
      int spins = 0;
      while (true) {
        CBAR();
        u32x4 v[4];
#pragma unroll
        for (int j = 0; j < 4; ++j) v[j] = __builtin_amdgcn_raw_buffer_load_b128(rs_ws, r[j], WS_X, 16);
#pragma unroll
        for (int j = 0; j < 4; ++j) {
          const bool ok = v[j].y == tag && v[j].w == tag;
          c[j][0] = ok ? v[j].x : c[j][0]; c[j][1] = ok ? v[j].z : c[j][1]; r[j] = ok ? OOB : r[j];
        }
        if (__all((r[0] & r[1] & r[2] & r[3]) == OOB) || ++spins >= SPIN_CAP) break;
        __builtin_amdgcn_s_sleep(GAPX);
      }
#pragma unroll
      for (int j = 0; j < 4; ++j) {
        float t = tsum32(__uint_as_float(c[j][0]), __uint_as_float(c[j][1]));
        if (j == 3) t = fmaxf(t, 0.f);
        reinterpret_cast<u16*>(qs)[j * 64 + 2 * (lane & 31) + (lane >> 5)] = (u16)bfbits(t);
      }
    }
    qs[128] = tag;
    CBAR();
    if (lane == 0) STW(fl, 1u);
    STAMP(4);
    __builtin_amdgcn_s_setprio(0);
    u32x4 q[3];
#pragma unroll
    for (int u = 0; u < 3; ++u) q[u] = *reinterpret_cast<const u32x4*>(qs + u * 32 + l7 * 4);
    const float sco = score8(kr[0], kr[1], kr[2], q);
    const float mw = hi_max(sco);
    const float p = ex2((sco - mw) * LOG2E);
    const float lw = hi_sum(p);
    if (lane == 0) *reinterpret_cast<f32x4*>(lds + L_ST) = f32x4{mw, lw, __uint_as_float(1u), 0.f};
    STAMP(5);
#pragma unroll
    for (int u = 0; u < 3; ++u) { pv_step(vr[u], p, acc[u]); PART(u); }
  } else {
    const int soff = (8 + (w - 1) * 72) * (DH * 2);
    u32x4 kr[NL], vr[NL];
    int go[9];
#pragma unroll
    for (int g = 0; g < 9; ++g) { const int r = g + (ROT ? (ROT * b) % 9 : 0); go[g] = soff + (r >= 9 ? r - 9 : r) * 3072; }
#define ISSUE(n) do { \
      if ((n) < NL) kr[(n) < NL ? (n) : 0] = __builtin_amdgcn_raw_buffer_load_b128(rs_k, kvo + ((n) % 3) * 128, go[((n) / 3) % 9], 2); \
      else vr[(n) >= NL ? (n) - NL : 0] = __builtin_amdgcn_raw_buffer_load_b128(rs_v, kvo + ((n) % 3) * 128, go[((n) / 3) % 9], 2); \
      PIN(); } while (0)
    const u32 t0 = (u32)__builtin_amdgcn_s_memrealtime();
#define GATE(n, p) WAITVM((p) - 1)
#pragma unroll
    for (int n = 0; n < PACE; ++n) ISSUE(n);
    asm volatile("s_barrier" ::: "memory");
    KEEP(a_w); KEEP(a_r); KEEP(o_voff);
#pragma unroll
    for (int n = PACE; n < QS; ++n) { GATE(n, PACE); ISSUE(n); }
    STAMP(3);
    {
      int spins = 0;
      while (LDW(fl) == 0u && ++spins < 100 * SPIN_CAP) __builtin_amdgcn_s_sleep(1);
      CBAR();
    }
    STAMP(4);
    u32x4 q[3];
#pragma unroll
    for (int u = 0; u < 3; ++u) q[u] = *reinterpret_cast<const u32x4*>(qs + u * 32 + l7 * 4);
    tag = qs[128];
    float sc[9];
#pragma unroll
    for (int i = 0; i < 9; ++i) {
      sc[i] = score8(kr[3 * i], kr[3 * i + 1], kr[3 * i + 2], q);
      KEEP(sc[i]);
      PIN();
      if (i % SG == SG - 1 && QS + i / SG < NSLOT) { GATE(QS + i / SG, PACE2); ISSUE(QS + i / SG); }
    }
    const float mw = hi_max(fmx(fmx(fmx(sc[0], sc[1]), fmx(sc[2], sc[3])), fmx(fmx(fmx(sc[4], sc[5]), fmx(sc[6], sc[7])), sc[8])));
#pragma unroll
    for (int i = 0; i < 9; ++i) sc[i] = ex2((sc[i] - mw) * LOG2E);
    const float lw = hi_sum((((sc[0] + sc[1]) + (sc[2] + sc[3])) + ((sc[4] + sc[5]) + (sc[6] + sc[7]))) + sc[8]);
    if (lane == 0) *reinterpret_cast<f32x4*>(lds + L_ST + 4 * w) = f32x4{mw, lw, __uint_as_float(1u), 0.f};
    STAMP(5);
#pragma unroll
    for (int n = S3; n <= NSLOT; ++n) {
#pragma unroll
      for (int j = (n == S3 ? 0 : SCH.hi[n - 1]); j < SCH.hi[n]; ++j) {
        pv_step(vr[j], sc[j / 3], acc[j % 3]);
        PIN();
        if (j == NL - 1) STAMP(6);
        if (j >= NL - 3) PART(j % 3);
      }
      if (n < NSLOT) { GATE(n, PACE2); ISSUE(n); }
      if (n == NSLOT - 1) {
        u32 oi = 0;
        if (lane == 0) oi = __hip_atomic_fetch_add(fl + 1, 65536u, __ATOMIC_RELAXED, __HIP_MEMORY_SCOPE_WORKGROUP);
        late = !idle && (u32)__builtin_amdgcn_readfirstlane(oi) >> 16 == 6u && (u32)__builtin_amdgcn_s_memrealtime() - t0 >= (u32)(TL / 10);
        if (late && lane == 0) __hip_atomic_fetch_add(fl + 1, 512u, __ATOMIC_RELAXED, __HIP_MEMORY_SCOPE_WORKGROUP);
        WISSUE(late);
      }
    }
  }
  if (w == 0) {
    f32x4 ml;
    int spins = 0;
    while (true) {
      CBAR();
      ml = *reinterpret_cast<const f32x4*>(lds + L_ST + 4 * l7);
      if (__all(__float_as_uint(ml.z) != 0u) || ++spins >= 100 * SPIN_CAP) break;
      __builtin_amdgcn_s_sleep(4);
    }
    const float M = max8(ml.x);
    const float f = ex2((ml.x - M) * LOG2E);
    const float L = sum8(f * ml.y);
    const u32x4 un = {__float_as_uint(M), tag, __float_as_uint(L), tag};
    __builtin_amdgcn_raw_buffer_store_b128(un, rs_ws, lane == 0 ? 0u : OOB, WS_S + b * 16, 16);
    if (lane < 8) lds[L_FL + 8 + lane] = f * __builtin_amdgcn_rcpf(L);
    STAMP(14);
  }
  u32 old = 0;
  {
    CBAR();
    if (lane == 0) old = __hip_atomic_fetch_add(fl + 1, 1u, __ATOMIC_RELAXED, __HIP_MEMORY_SCOPE_WORKGROUP);
    CBAR();
    const f32x4 g0 = *reinterpret_cast<const f32x4*>(lds + L_FL + 8), g1 = *reinterpret_cast<const f32x4*>(lds + L_FL + 12);
    f32x4 o[8];
#pragma unroll
    for (int r = 0; r < 8; ++r) o[r] = *reinterpret_cast<const f32x4*>(ldb + a_r + r * 768);
#pragma unroll
    for (int r = 0; r < 8; ++r) KEEP(o[r]);
    old = __builtin_amdgcn_readfirstlane(old);
    if ((old & 255u) == 7u) {
      const f32x4 t = ((g0.x * o[0] + g0.y * o[1]) + (g0.z * o[2] + g0.w * o[3])) + ((g1.x * o[4] + g1.y * o[5]) + (g1.z * o[6] + g1.w * o[7]));
      const u32x4 un = {pack2(t.x, t.y), tag, pack2(t.z, t.w), tag};
      __builtin_amdgcn_raw_buffer_store_b128(un, rs_ws, o_voff, WS_O + b * 768, 16);
      STAMP(7);
    }
  }
  if (w) STAMP(1);
  KEEP(voob);
  if (!idle) {
    const int R = 8 * tg + w, hh = R / 6, hh0 = (8 * tg) / 6;
    if (w == 0) {
      u32x4 un;
      int spins = 0;
      while (true) {
        CBAR();
        un = __builtin_amdgcn_raw_buffer_load_b128(rs_ws, (lane & 15) * 16, WS_S + hh0 * 128, 16);
        if (__all(un.y == tag && un.w == tag) || ++spins >= SPIN_CAP) break;
        __builtin_amdgcn_s_sleep(GAPS);
      }
      const float mp = __uint_as_float(un.x);
      const float f = ex2((mp - max8(mp)) * LOG2E) * __uint_as_float(un.z);
      if (lane < 16) lds[L_WT + lane] = f * __builtin_amdgcn_rcpf(sum8(f));
      CBAR();
      if (lane == 0) __hip_atomic_fetch_add(fl + 1, 256u, __ATOMIC_RELAXED, __HIP_MEMORY_SCOPE_WORKGROUP);
    }
    {
      int spins = 0;
      u32 cnt = old + 1u;
      if (!late) {
        while (!(cnt & 520u) && ++spins < 100 * SPIN_CAP) { __builtin_amdgcn_s_sleep(1); cnt = LDW(fl + 1); }
        WISSUE(true);
      }
      while ((cnt & 511u) != 264u && ++spins < 100 * SPIN_CAP) { __builtin_amdgcn_s_sleep(1); cnt = LDW(fl + 1); }
      CBAR();
    }
    float wt = lds[L_WT + (hh - hh0) * 8 + l3];
    if (w) STAMP(14);
    u32 wp[2][8];
    float y[8];
    {
      const u32 hid = reinterpret_cast<const u16*>(qs)[192 + 8 * w + l3];
#pragma unroll
      for (int d = 0; d < 4; ++d) {
        y[2 * d] = dot2(wov[4][d], hid, 0.f);
        y[2 * d + 1] = dot2(wov[4][d], hid << 16, 0.f);
        KEEP(y[2 * d]); KEEP(y[2 * d + 1]);
      }
#pragma unroll
      for (int j = 0; j < 2; ++j)
#pragma unroll
        for (int d = 0; d < 4; ++d) {
          wp[j][2 * d] = __builtin_amdgcn_perm(wov[2 * j + 1][d], wov[2 * j][d], 0x05040100u);
          wp[j][2 * d + 1] = __builtin_amdgcn_perm(wov[2 * j + 1][d], wov[2 * j][d], 0x07060302u);
          KEEP(wp[j][2 * d]); KEEP(wp[j][2 * d + 1]);
        }
    }
    u32 a_ow = (L_OT + w * 16) * 4 + (4 * l7 + 2 * (lane >> 5) + ((lane >> 4) & 1)) * 2, a_or = (L_OT + w * 16) * 4 + l3 * 8;
    u32 a_yw = (L_Y + w * 64 + 8 * l7 + perm8) * 4, a_yr = (L_Y + lane) * 4;
    u32 y_voff = (lane >> 4) * 3072 + (lane & 15) * 8;
    u32 g = l3 * 768 + l7 * 16, sb = WS_O + hh * 6144 + (R - 6 * hh) * 128, yb = WS_Y + (tc * 96 + tg) * 128;
    KEEP(a_ow); KEEP(a_or); KEEP(a_yw); KEEP(a_yr); KEEP(y_voff); KEEP(wt);
    asm volatile("" : "+s"(sb), "+s"(yb));
    if (w) STAMP(2);
    u32 c0 = 0, c1 = 0;
    u64 dn = 0;
    POLL1(g, sb, c0, c1, dn, GAPO0, GAPO);
    STAMP(8);
    float t = tsum16(tsum32(wt * blo(c0), wt * blo(c1)), tsum32(wt * bhi(c0), wt * bhi(c1)));
    t += dpp<ROR8>(t);
    *reinterpret_cast<u16*>(ldb + a_ow) = (u16)bfbits(t);
    CBAR();
    const u32x2 ov = *reinterpret_cast<const u32x2*>(ldb + a_or);
#pragma unroll
    for (int j = 0; j < 2; ++j)
#pragma unroll
      for (int e = 0; e < 8; ++e) y[e] = dot2(wp[j][e], ov[j], y[e]);
    *reinterpret_cast<float*>(ldb + a_yw) = tsum_hi8(y[0], y[1], y[2], y[3], y[4], y[5], y[6], y[7], b3);
    BAR();
    STAMP(9);
    if (w == 0) {
      float e[8];
#pragma unroll
      for (int r = 0; r < 8; ++r) e[r] = *reinterpret_cast<const float*>(ldb + a_yr + r * 256);
      const u32x2 un = {__float_as_uint(((e[0] + e[1]) + (e[2] + e[3])) + ((e[4] + e[5]) + (e[6] + e[7]))), tag};
      __builtin_amdgcn_raw_buffer_store_b64(un, rs_ws, y_voff, yb, 16);
      STAMP(10);
    }
    POLL1_DRAIN();
    KEEP(dm);
  } else {
    const int ri = (h >> 2) * 2 + (h & 1) + 16 * (w - 1);
    if (w >= 1 && ri < 40) {
      const int rcol = 16 * ri + 2 * l7 + (lane >> 5);
      u32 xo = x[rcol];
      u32 ov = (lane & 24) ? OOB : rcol * 2;
      u32 r0 = lane * 16, r1 = r0 + 1024, r2 = r0 + 2048, sb = WS_Y + ri * 3072;
      KEEP(xo); KEEP(ov);
      u32x16 R0, R1;
      u32x4 R2;
      u32 c[6] = {0, 0, 0, 0, 0, 0};
      u32 t0, cnt = SPIN_CAP;
      u64 m;
      asm volatile(
P3_LD(212, 215, 216, 219, 220, 223) "s_sleep " STR(GAPY0) "\n"
P3_LD(224, 227, 228, 231, 232, 235) "s_sleep " STR(GAPY0) "\n"
P3_LD(236, 239, 240, 243, 244, 247)
"1:\n"
P3_CK(212, 213, 214, 215, 216, 217, 218, 219, 220, 221, 222, 223)
P3_CK(224, 225, 226, 227, 228, 229, 230, 231, 232, 233, 234, 235)
P3_CK(236, 237, 238, 239, 240, 241, 242, 243, 244, 245, 246, 247)
"s_sub_u32 %[cnt], %[cnt], 1\n"
"s_cbranch_scc0 1b\n"
"2:\n"
: [c0] "+v"(c[0]), [c1] "+v"(c[1]), [c2] "+v"(c[2]), [c3] "+v"(c[3]), [c4] "+v"(c[4]), [c5] "+v"(c[5]),
  [r0] "+v"(r0), [r1] "+v"(r1), [r2] "+v"(r2),
  [t0] "=&v"(t0), [cnt] "+s"(cnt), [m] "=&s"(m), "=&{v[212:227]}"(R0), "=&{v[228:243]}"(R1), "=&{v[244:247]}"(R2)
: [rs] "s"(rsw), [sb] "s"(sb), [tag] "v"(tag), [oob] "v"(voob)
: "vcc", "scc", "memory");
      STAMP(11);
      float v = tsum32((__uint_as_float(c[0]) + __uint_as_float(c[2])) + __uint_as_float(c[4]),
                       (__uint_as_float(c[1]) + __uint_as_float(c[3])) + __uint_as_float(c[5]));
      v = tsum16(v, v);
      v += dpp<ROR8>(v);
      __builtin_amdgcn_raw_buffer_store_b16((u16)bfbits(v + blo(xo)), RSRC(out, D * 2), ov, 0, 0);
      if (ri == 0 && lane == 0) __builtin_amdgcn_raw_buffer_store_b32(tag, rs_ws, 0, 0, 16);
      asm volatile("s_waitcnt vmcnt(0)" :: "{v[212:227]}"(R0), "{v[228:243]}"(R1), "{v[244:247]}"(R2) : "memory");
    }
  }
  STAMP(12);
  if (tsel != 0u && (AMPW)) {
    u32x2 g; int sp = 0;
    do { asm volatile("" ::: "memory"); g = __builtin_amdgcn_raw_buffer_load_b64(rs_ws, 64, 0, 16); } while (g.y != tag && ++sp < SPIN_CAP);
    const u32 t0 = __builtin_amdgcn_readfirstlane(g.x);
    int d = SGN * (int)(tsel - t0) + 200;
    if (d < 0) d = 0;
    if (d > 10000) d = 10000;
    while ((u32)__builtin_amdgcn_s_memrealtime() - t0 < 4000u + (u32)d * 16u) __builtin_amdgcn_s_sleep(2);
  }
}

extern "C" void kernel_launch(void* const* d_in, const int* in_sizes, int n_in,
                              void* d_out, int out_size, void* d_ws, size_t ws_size,
                              hipStream_t stream) {
  if (ws_size < WS_BYTES) return;
  fused_decode_kernel<<<256, 512, 0, stream>>>(
      (const u16*)d_in[0], (const u16*)d_in[1], (const u16*)d_in[2], (const u16*)d_in[3],
      (const u16*)d_in[4], (const u16*)d_in[6], (const u16*)d_in[7], (u16*)d_out, (u32*)d_ws);
}
```

```cpp
#include <hip/hip_runtime.h>
#include <hip/hip_bf16.h>
#include <stdint.h>

#define PACE 6
#define CL 4
#define EARLYW 1
#define CN 7
#define ND 8
#define WAITALL 0
#define SWZ 1
#define GAPS 16
#define GAPX0 4
#define GAPX 4
#define GAPO0 5
#define GAPO 2
#define GAPY0 5
#define GAPY 2
#define QS 33
#define PB 2

typedef unsigned int u32;
typedef unsigned short u16;
typedef unsigned long long u64;
using u32x2 = __attribute__((ext_vector_type(2))) unsigned;
using u32x4 = __attribute__((ext_vector_type(4))) unsigned;
using u32x16 = __attribute__((ext_vector_type(16))) unsigned;
using f32x2 = __attribute__((ext_vector_type(2))) float;
using f32x4 = __attribute__((ext_vector_type(4))) float;
using bf16x2 = __attribute__((ext_vector_type(2))) __bf16;

constexpr int D = 640, DH = 192, SK = 32768, WLEN = 4096;
constexpr int NL = 27;
constexpr int NSLOT = 2 * NL;
constexpr u32 NP = 24 - ND;
constexpr u32 NTILE = 10 * NP, NROLE = NTILE + 10;
static_assert(NP <= 16, "one load per reducer poll");
constexpr u32 WS_C = 2048;
constexpr u32 WS_S = 4096;
constexpr u32 WS_O = WS_S + 32u * 128u;
constexpr u32 WS_Y = WS_O + 256u * 768u;
constexpr u32 WS_X = WS_Y + 80u * 1024u;
constexpr u32 WS_BYTES = WS_X + 240u * 512u;
constexpr int SPIN_CAP = 200000;
constexpr float LOG2E = 1.4426950408889634f;
constexpr float SCALE = 0.07216878364870322f;

__device__ __forceinline__ float dot2(u32 a, u32 b, float acc) {
  return __builtin_amdgcn_fdot2_f32_bf16(__builtin_bit_cast(bf16x2, a), __builtin_bit_cast(bf16x2, b), acc, false);
}
template <int CTRL> __device__ __forceinline__ float dpp(float x) {
  return __builtin_bit_cast(float, __builtin_amdgcn_mov_dpp(__builtin_bit_cast(int, x), CTRL, 0xf, 0xf, true));
}
constexpr int XOR1 = 0xB1, XOR2 = 0x4E, XOR7 = 0x141, ROR8 = 0x128, ROR4 = 0x124;
__device__ __forceinline__ float sum8(float x) {
  x += dpp<XOR1>(x); x += dpp<XOR2>(x); x += dpp<XOR7>(x); return x;
}
__device__ __forceinline__ float fmx(float a, float b) {
  return __builtin_fmaxf(a, b);
}
__device__ __forceinline__ float max8(float x) {
  x = fmx(x, dpp<XOR1>(x)); x = fmx(x, dpp<XOR2>(x)); x = fmx(x, dpp<XOR7>(x)); return x;
}
__device__ __forceinline__ float xrow_sum(float x) {
  auto s = __builtin_amdgcn_permlane16_swap(__float_as_uint(x), __float_as_uint(x), false, false);
  x = __uint_as_float(s[0]) + __uint_as_float(s[1]);
  auto t = __builtin_amdgcn_permlane32_swap(__float_as_uint(x), __float_as_uint(x), false, false);
  return __uint_as_float(t[0]) + __uint_as_float(t[1]);
}
__device__ __forceinline__ float xrow_max(float x) {
  auto s = __builtin_amdgcn_permlane16_swap(__float_as_uint(x), __float_as_uint(x), false, false);
  x = fmx(__uint_as_float(s[0]), __uint_as_float(s[1]));
  auto t = __builtin_amdgcn_permlane32_swap(__float_as_uint(x), __float_as_uint(x), false, false);
  return fmx(__uint_as_float(t[0]), __uint_as_float(t[1]));
}
__device__ __forceinline__ float tsum32(float a, float b) {
  auto t = __builtin_amdgcn_permlane32_swap(__float_as_uint(a), __float_as_uint(b), false, false);
  return __uint_as_float(t[0]) + __uint_as_float(t[1]);
}
__device__ __forceinline__ float tsum16(float a, float b) {
  auto t = __builtin_amdgcn_permlane16_swap(__float_as_uint(a), __float_as_uint(b), false, false);
  return __uint_as_float(t[0]) + __uint_as_float(t[1]);
}
__device__ __forceinline__ float tsum8(float a, float b, bool b3) {
  const float snd = b3 ? a : b, kp = b3 ? b : a;
  return kp + dpp<ROR8>(snd);
}
__device__ __forceinline__ float tsum_hi8(float v0, float v1, float v2, float v3, float v4, float v5, float v6, float v7, bool b3) {
  return tsum8(tsum16(tsum32(v0, v4), tsum32(v2, v6)), tsum16(tsum32(v1, v5), tsum32(v3, v7)), b3);
}
__device__ __forceinline__ float hi_sum(float x) { x += dpp<ROR8>(x); return xrow_sum(x); }
__device__ __forceinline__ float hi_max(float x) { x = fmx(x, dpp<ROR8>(x)); return xrow_max(x); }
__device__ __forceinline__ float wave_sum(float x) { return hi_sum(sum8(x)); }
__device__ __forceinline__ u32 bfbits(float f) { return (u32)__builtin_bit_cast(u16, (__bf16)f); }
__device__ __forceinline__ float bfr(float f) { return __uint_as_float(bfbits(f) << 16); }
__device__ __forceinline__ u32 pack2(float a, float b) {
  bf16x2 p = {(__bf16)a, (__bf16)b};
  return __builtin_bit_cast(u32, p);
}
__device__ __forceinline__ float blo(u32 v) { return __uint_as_float(v << 16); }
__device__ __forceinline__ float bhi(u32 v) { return __uint_as_float(v & 0xffff0000u); }
__device__ __forceinline__ float ex2(float v) { return __builtin_amdgcn_exp2f(v); }
#define BAR() asm volatile("s_waitcnt lgkmcnt(0)\n\ts_barrier" ::: "memory")
#define PIN() __builtin_amdgcn_sched_barrier(0)
#define CBAR() asm volatile("" ::: "memory")
#define STAMP(i)
#define STR_(x) #x
#define STR(x) STR_(x)
#define KEEP(v) asm volatile("" : "+v"(v))
#define RSRC(p, n) __builtin_amdgcn_make_buffer_rsrc((void*)(p), 0, (n), 0x00020000)
#define LDW(p) __hip_atomic_load((p), __ATOMIC_RELAXED, __HIP_MEMORY_SCOPE_WORKGROUP)
#define STW(p, v) __hip_atomic_store((p), (v), __ATOMIC_RELAXED, __HIP_MEMORY_SCOPE_WORKGROUP)

#define WAITVM(n) __builtin_amdgcn_s_waitcnt(0x0F70 | ((n) & 15) | (((n) >> 4) << 14))

#define ST128(v, vo, so) do { __builtin_amdgcn_raw_buffer_store_b128((v), rs_ws, (vo), (so), 16); asm volatile("s_nop 1" :: "v"(v)); } while (0)

constexpr int L_FL = 0;
constexpr int L_ST = 16;
constexpr int L_QS = L_ST + 32;
constexpr int L_XN = L_QS + 100;
constexpr int L_PART = L_XN + 320;
constexpr int L_OT = L_PART + 8 * 192;
constexpr int L_Y = L_OT + 8 * 32;
constexpr int L_END = L_Y + 8 * 64;
constexpr u32 OOB = 0x80000000u;

struct Sched { int hi[NSLOT + 1]; };
constexpr int S3 = QS + 9;
constexpr Sched make_sched() {
  Sched t{};
  int j = 0;
  for (int n = S3; n < NSLOT; ++n) {
    int burst = 0;
    while (j < NL && burst < PB && n - (NL + j) >= PACE) { ++j; ++burst; }
    t.hi[n] = j;
  }
  t.hi[NSLOT] = NL;
  return t;
}
constexpr Sched SCH = make_sched();
static_assert(QS >= NL && S3 <= NSLOT - CL && CL >= 1, "schedule");

__device__ __forceinline__ float score8(const u32x4 k0, const u32x4 k1, const u32x4 k2, const u32x4 (&q)[3]) {
  float a = dot2(k0.x, q[0].x, 0.f), c = dot2(k0.y, q[0].y, 0.f), e = dot2(k0.z, q[0].z, 0.f), g = dot2(k0.w, q[0].w, 0.f);
  a = dot2(k1.x, q[1].x, a); c = dot2(k1.y, q[1].y, c); e = dot2(k1.z, q[1].z, e); g = dot2(k1.w, q[1].w, g);
  a = dot2(k2.x, q[2].x, a); c = dot2(k2.y, q[2].y, c); e = dot2(k2.z, q[2].z, e); g = dot2(k2.w, q[2].w, g);
  return bfr(bfr(sum8((a + c) + (e + g))) * SCALE);
}
__device__ __forceinline__ void pv_step(const u32x4 d, const float pp, f32x2 (&acc)[4]) {
#pragma unroll
  for (int c = 0; c < 4; ++c) {
    acc[c] = __builtin_elementwise_fma(f32x2{blo(d[c]), bhi(d[c])}, f32x2{pp, pp}, acc[c]);
    KEEP(acc[c]);
  }
}

#define PL(a, b, r) "buffer_load_dwordx4 v[" #a ":" #b "], %[" #r "], %[rs], 0 offen sc1\n"
#define PC(n, x, y, z, t, ca, cb, r) \
  "s_waitcnt vmcnt(" #n ")\n" \
  "v_cmp_eq_u32 vcc, v" #y ", %[tag]\n" \
  "v_cmp_eq_u32 %[m], v" #t ", %[tag]\n" \
  "s_and_b64 vcc, vcc, %[m]\n" \
  "v_cndmask_b32 %[" #ca "], %[" #ca "], v" #x ", vcc\n" \
  "v_cndmask_b32 %[" #cb "], %[" #cb "], v" #z ", vcc\n" \
  "v_cndmask_b32 %[" #r "], %[" #r "], %[oob], vcc\n"
#define PEND(t, gap) \
  "v_cmp_eq_u32 vcc, %[" #t "], %[oob]\n" \
  "s_cmp_eq_u64 vcc, -1\n" \
  "s_cbranch_scc1 2f\n" \
  "s_sleep " STR(gap) "\n"
#define P2_LD(a0, a3, b0, b3) PL(a0, a3, r0) PL(b0, b3, r1)
#define P2_CK(a0, a1, a2, a3, b0, b1, b2, b3) \
  PC(7, a0, a1, a2, a3, c0, c1, r0) PC(6, b0, b1, b2, b3, c2, c3, r1) \
  "v_and_b32 %[t0], %[r0], %[r1]\n" PEND(t0, GAPO) P2_LD(a0, a3, b0, b3)
#define P1_CK(a0, a1, a2, a3) PC(3, a0, a1, a2, a3, c0, c1, r0) PEND(r0, GAPY) PL(a0, a3, r0)

__global__ void __launch_bounds__(512, 2)
fused_decode_kernel(const u16* __restrict__ x, const u16* __restrict__ kc, const u16* __restrict__ vc,
                    const u16* __restrict__ wq, const u16* __restrict__ wo, const u16* __restrict__ w1,
                    const u16* __restrict__ w2, u16* out, u32* __restrict__ ws) {
  __shared__ __attribute__((aligned(16))) float lds[L_END];
  const int tid = threadIdx.x;
  const int lane = tid & 63;
  const int w = __builtin_amdgcn_readfirstlane(tid >> 6);
  const int b = blockIdx.x;
  const int h = b >> 3;
  const int s = b & 7;
  const int l3 = lane >> 3, l7 = lane & 7;
  const bool b3 = lane & 8;
  const int perm8 = 4 * (lane >> 5) + 2 * ((lane >> 4) & 1) + (l3 & 1);

  const auto rs_ws = RSRC(ws, WS_BYTES);
  const u32x4 rsw = {(u32)(size_t)ws, (u32)((size_t)ws >> 32) & 0xffffu, WS_BYTES, 0x00020000u};
  const size_t kvoff = ((size_t)h * SK + (SK - WLEN) + (SWZ ? (s + h) & 7 : s) * 512) * DH;
  const auto rs_k = RSRC(kc + kvoff, 512 * DH * 2);
  const auto rs_v = RSRC(vc + kvoff, 512 * DH * 2);
  const int kvo = l3 * 384 + l7 * 16;
  char* const ldb = reinterpret_cast<char*>(lds);
  u32* const fl = reinterpret_cast<u32*>(lds + L_FL);
  u32* const qs = reinterpret_cast<u32*>(lds + L_QS);

  u32x4 wov[10];
#define TILE() \
    const u32 tg = role / 10u, tc = role - 10u * tg; \
    const bool dbl = tg < ND; \
    const u32 row0 = dbl ? 512u * tg + 64u * w : 256u * (ND + tg) + 32u * w
#define WISSUE(on) do { \
    TILE(); \
    const auto rs_wo = RSRC(wo + (size_t)(row0 * D + 64u * tc), (on) ? 63 * 1280 + 128 : 0); \
    const auto rs_w2 = RSRC(w2 + (size_t)(row0 / 4u * D + 64u * tc), (on) ? (dbl ? 15 * 1280 + 128 : 7 * 1280 + 128) : 0); \
    const u32 v1 = l3 * 5120 + l7 * 16, v2 = dbl ? v1 + 40960 : OOB; \
    _Pragma("unroll") for (int i = 0; i < 2; ++i) wov[8 + i] = __builtin_amdgcn_raw_buffer_load_b128(rs_w2, l3 * 2560 + l7 * 16 + i * 1280, 0, 2); \
    _Pragma("unroll") for (int i = 0; i < 8; ++i) wov[i] = __builtin_amdgcn_raw_buffer_load_b128(rs_wo, (i < 4 ? v1 : v2) + (i & 3) * 1280, 0, 2); \
    PIN(); } while (0)
#define PART(u) do { *reinterpret_cast<float*>(ldb + a_w + (u) * 256) = \
    tsum_hi8(acc[u][0].x, acc[u][0].y, acc[u][1].x, acc[u][1].y, acc[u][2].x, acc[u][2].y, acc[u][3].x, acc[u][3].y, b3); PIN(); } while (0)
  bool late = false;
  u32 role = 0xffffffffu;
  u32 a_w = (L_PART + w * 192 + 8 * l7 + perm8) * 4, a_r = (L_PART + (lane < 48 ? lane : 47) * 4) * 4;
  u32 o_voff = lane < 48 ? lane * 16 : OOB;
  f32x2 acc[3][4];
#pragma unroll
  for (int u = 0; u < 3; ++u)
#pragma unroll
    for (int c = 0; c < 4; ++c) acc[u][c] = f32x2{0.f, 0.f};
  u32 tag;
  u32 voob = OOB, vzero = 0u, vone = 1u;
  asm volatile("" : "+s"(out));

  if (w == 0) {
    __builtin_amdgcn_s_setprio(3);
    const bool noq = s == 7 && (h & 2);
    const auto rs_x = RSRC(x, D * 2);
    const u32x4 xa = __builtin_amdgcn_raw_buffer_load_b128(rs_x, lane * 20, 0, 0);
    const u32 xb = __builtin_amdgcn_raw_buffer_load_b32(rs_x, lane * 20 + 16, 0, 0);
    const int sec = s < 6 ? 12 * (h >> 1) + 2 * s + (h & 1) : 6 * (h >> 2) + (s == 6 ? (h & 3) : 4 + (h & 1));
    const u32 stp = s < 6 ? 12288u : 3072u;
    const int cg = sec >> 1, rg = sec & 1;
    const auto rs_t = RSRC((s < 6 ? wq : w1) + (size_t)rg * 320 * (stp / 2) + cg * 64, noq ? 0 : 319 * stp + 128);
    u32x4 wv[40];
    const u32 tv = l3 * stp + l7 * 16;
    const u32 E = __builtin_amdgcn_raw_buffer_load_b32(rs_ws, 0, 0, 16);
#pragma unroll
    for (int i = 0; i < 40; ++i) { wv[i] = __builtin_amdgcn_raw_buffer_load_b128(rs_t, tv, i * 8 * stp, 2); PIN(); }
    u32x4 kr[3], vr[3];
#pragma unroll
    for (int i = 0; i < 3; ++i) kr[i] = __builtin_amdgcn_raw_buffer_load_b128(rs_k, kvo + i * 128, 0, 2);
#pragma unroll
    for (int i = 0; i < 3; ++i) vr[i] = __builtin_amdgcn_raw_buffer_load_b128(rs_v, kvo + i * 128, 0, 2);
    if (lane < 48) fl[lane] = 0u;
    PIN();
    BAR();
    float xv[10];
    xv[0] = blo(xa.x); xv[1] = bhi(xa.x); xv[2] = blo(xa.y); xv[3] = bhi(xa.y);
    xv[4] = blo(xa.z); xv[5] = bhi(xa.z); xv[6] = blo(xa.w); xv[7] = bhi(xa.w);
    xv[8] = blo(xb); xv[9] = bhi(xb);
    const float sm = ((xv[0] + xv[1]) + (xv[2] + xv[3])) + ((xv[4] + xv[5]) + (xv[6] + xv[7])) + (xv[8] + xv[9]);
    const float mean = wave_sum(sm) * (1.0f / D);
    float sq = 0.f;
#pragma unroll
    for (int e = 0; e < 10; ++e) { const float d = xv[e] - mean; sq += d * d; }
    const float var = wave_sum(sq) * (1.0f / D);
    const float mb = bfr(mean);
    const float den = bfr(__builtin_amdgcn_sqrtf(bfr(bfr(var) + 1e-5f)));
    const float rinv = 1.0f / den;
    u32* xn = reinterpret_cast<u32*>(lds + L_XN);
#pragma unroll
    for (int e = 0; e < 10; ++e) {
      const u32 r = lane * 10 + e, rr = r % 320;
      reinterpret_cast<u16*>(xn)[r - rr + (rr % 8) * 40 + rr / 8] = (u16)bfbits(bfr(xv[e] - mb) * rinv);
    }
    u32x4 xs[5];
#pragma unroll
    for (int i = 0; i < 5; ++i) xs[i] = *reinterpret_cast<const u32x4*>(xn + rg * 160 + l3 * 20 + i * 4);
    STAMP(1);
    float a8[8];
#pragma unroll
    for (int e = 0; e < 8; ++e) a8[e] = 0.f;
#pragma unroll
    for (int i = 0; i < 40; ++i) {
      const u32 v = xs[i / 8][(i / 2) % 4];
      const u32 xl = (i & 1) ? v >> 16 : v & 0xffffu;
      const u32 xh = (i & 1) ? v & 0xffff0000u : v << 16;
#pragma unroll
      for (int d = 0; d < 4; ++d) {
        a8[2 * d] = dot2(wv[i][d], xl, a8[2 * d]);
        a8[2 * d + 1] = dot2(wv[i][d], xh, a8[2 * d + 1]);
        KEEP(a8[2 * d]); KEEP(a8[2 * d + 1]);
      }
      PIN();
    }
    tag = E + 1u + (E == 0xffffffffu);
    STAMP(2);
    {
      float e0 = tsum16(tsum32(a8[0], a8[4]), tsum32(a8[2], a8[6]));
      float e1 = tsum16(tsum32(a8[1], a8[5]), tsum32(a8[3], a8[7]));
      e0 += dpp<ROR8>(e0); e1 += dpp<ROR8>(e1);
      const int gs = s < 6 ? sec : 192 + sec;
      const u32x4 un = {__float_as_uint(e0), tag, __float_as_uint(e1), tag};
      const u32 xv_ = (b3 || noq) ? OOB : (4 * l7 + (lane >> 4 & 1) + 2 * (lane >> 5)) * 16;
      ST128(un, xv_, WS_X + gs * 512);
      __builtin_amdgcn_raw_buffer_store_b64(u32x2{0u, tag}, rs_ws, (b == 0 && lane == 0) ? WS_C : OOB, 0, 16);
      STAMP(3);
      u32 r[3], c[3][2];
#pragma unroll
      for (int j = 0; j < 3; ++j) { r[j] = (3 * h + j) * 1024 + lane * 16; c[j][0] = c[j][1] = 0u; }
      __builtin_amdgcn_s_sleep(GAPX0);
      int spins = 0;
      while (true) {
        CBAR();
        u32x4 v[3];
#pragma unroll
        for (int j = 0; j < 3; ++j) v[j] = __builtin_amdgcn_raw_buffer_load_b128(rs_ws, r[j], WS_X, 16);
#pragma unroll
        for (int j = 0; j < 3; ++j) {
          const bool ok = v[j].y == tag && v[j].w == tag;
          c[j][0] = ok ? v[j].x : c[j][0]; c[j][1] = ok ? v[j].z : c[j][1]; r[j] = ok ? OOB : r[j];
        }
        if (__all((r[0] & r[1] & r[2]) == OOB) || ++spins >= SPIN_CAP) break;
        __builtin_amdgcn_s_sleep(GAPX);
      }
#pragma unroll
      for (int j = 0; j < 3; ++j) {
        const float t = tsum32(__uint_as_float(c[j][0]), __uint_as_float(c[j][1]));
        reinterpret_cast<u16*>(qs)[j * 64 + 2 * (lane & 31) + (lane >> 5)] = (u16)bfbits(t);
      }
    }
    qs[96] = tag;
    CBAR();
    if (lane == 0) STW(fl, 1u);
    STAMP(4);
    __builtin_amdgcn_s_setprio(0);
    u32x4 q[3];
#pragma unroll
    for (int u = 0; u < 3; ++u) q[u] = *reinterpret_cast<const u32x4*>(qs + u * 32 + l7 * 4);
    const float sco = score8(kr[0], kr[1], kr[2], q);
    const float mw = hi_max(sco);
    const float p = ex2((sco - mw) * LOG2E);
    const float lw = hi_sum(p);
    if (lane == 0) *reinterpret_cast<f32x4*>(lds + L_ST) = f32x4{mw, lw, __uint_as_float(1u), 0.f};
    STAMP(5);
#pragma unroll
    for (int u = 0; u < 3; ++u) { pv_step(vr[u], p, acc[u]); PART(u); }
    {
      int spins = 0;
      while (true) {
        CBAR();
        const u32 t = __builtin_amdgcn_raw_buffer_load_b32(rs_ws, WS_C + 4, 0, 16);
        if (__builtin_amdgcn_readfirstlane(t) == __builtin_amdgcn_readfirstlane(tag) || ++spins >= SPIN_CAP) break;
        __builtin_amdgcn_s_sleep(GAPS);
      }
    }
    f32x4 ml;
    int spins = 0;
    while (true) {
      CBAR();
      ml = *reinterpret_cast<const f32x4*>(lds + L_ST + 4 * l7);
      if (__all(__float_as_uint(ml.z) != 0u) || ++spins >= 100 * SPIN_CAP) break;
      __builtin_amdgcn_s_sleep(4);
    }
    const float M = max8(ml.x);
    const float f = ex2((ml.x - M) * LOG2E);
    const float L = sum8(f * ml.y);
    const u32x4 un = {__float_as_uint(M), tag, __float_as_uint(L), tag};
    ST128(un, lane == 0 ? 0u : OOB, WS_S + b * 16);
    if (lane < 8) lds[L_FL + 8 + lane] = f * __builtin_amdgcn_rcpf(L);
  } else {
    const int soff = (8 + (w - 1) * 72) * (DH * 2);
    u32x4 kr[NL], vr[NL];
#define ISSUE(n) do { \
      if ((n) < NL) kr[(n) < NL ? (n) : 0] = __builtin_amdgcn_raw_buffer_load_b128(rs_k, kvo + ((n) % 3) * 128, soff + (((n) / 3) % 9) * 3072, 2); \
      else vr[(n) >= NL ? (n) - NL : 0] = __builtin_amdgcn_raw_buffer_load_b128(rs_v, kvo + ((n) % 3) * 128, soff + (((n) / 3) % 9) * 3072, 2); \
      PIN(); } while (0)
#define GATE() WAITVM(PACE - 1)
#pragma unroll
    for (int n = 0; n < PACE; ++n) ISSUE(n);
    asm volatile("s_barrier" ::: "memory");
    KEEP(a_w); KEEP(a_r); KEEP(o_voff);
#pragma unroll
    for (int n = PACE; n < QS; ++n) { GATE(); ISSUE(n); }
    STAMP(3);
    {
      int spins = 0;
      while (LDW(fl) == 0u && ++spins < 100 * SPIN_CAP) __builtin_amdgcn_s_sleep(1);
      CBAR();
    }
    STAMP(4);
    u32x4 q[3];
#pragma unroll
    for (int u = 0; u < 3; ++u) q[u] = *reinterpret_cast<const u32x4*>(qs + u * 32 + l7 * 4);
    tag = qs[96];
    float sc[9];
#pragma unroll
    for (int i = 0; i < 9; ++i) {
      sc[i] = score8(kr[3 * i], kr[3 * i + 1], kr[3 * i + 2], q);
      KEEP(sc[i]);
      PIN();
      GATE(); ISSUE(QS + i);
    }
    const float mw = hi_max(fmx(fmx(fmx(sc[0], sc[1]), fmx(sc[2], sc[3])), fmx(fmx(fmx(sc[4], sc[5]), fmx(sc[6], sc[7])), sc[8])));
#pragma unroll
    for (int i = 0; i < 9; ++i) sc[i] = ex2((sc[i] - mw) * LOG2E);
    const float lw = hi_sum((((sc[0] + sc[1]) + (sc[2] + sc[3])) + ((sc[4] + sc[5]) + (sc[6] + sc[7]))) + sc[8]);
    if (lane == 0) *reinterpret_cast<f32x4*>(lds + L_ST + 4 * w) = f32x4{mw, lw, __uint_as_float(1u), 0.f};
    STAMP(5);
#pragma unroll
    for (int n = S3; n <= NSLOT; ++n) {
#pragma unroll
      for (int j = (n == S3 ? 0 : SCH.hi[n - 1]); j < SCH.hi[n]; ++j) {
        pv_step(vr[j], sc[j / 3], acc[j % 3]);
        PIN();
        if (j == NL - 1) STAMP(6);
        if (j >= NL - 3) PART(j % 3);
      }
      if (n < NSLOT) { GATE(); ISSUE(n); }
      if (n == NSLOT - CL) {
        if (lane == 0) __hip_atomic_fetch_add(fl + 1, 65536u, __ATOMIC_RELAXED, __HIP_MEMORY_SCOPE_WORKGROUP);
        PIN();
      }
      if (n == NSLOT - 1) {
        role = __builtin_amdgcn_readfirstlane(LDW(fl + 2)) - 1u;
        late = role < NTILE;
        WISSUE(late);
      }
    }
  }
  u32 old = 0;
  {
    asm volatile("s_mov_b64 exec, 1\n\tds_add_rtn_u32 %0, %1, %2 offset:4\n\ts_mov_b64 exec, -1" : "=&v"(old) : "v"(vzero), "v"(vone) : "memory");
    const f32x4 g0 = *reinterpret_cast<const f32x4*>(lds + L_FL + 8), g1 = *reinterpret_cast<const f32x4*>(lds + L_FL + 12);
    f32x4 o[8];
#pragma unroll
    for (int r = 0; r < 8; ++r) o[r] = *reinterpret_cast<const f32x4*>(ldb + a_r + r * 768);
#pragma unroll
    for (int r = 0; r < 8; ++r) asm volatile("" : "+v"(o[r]), "+v"(old));
    old = __builtin_amdgcn_readfirstlane(old);
    const f32x4 t = ((g0.x * o[0] + g0.y * o[1]) + (g0.z * o[2] + g0.w * o[3])) + ((g1.x * o[4] + g1.y * o[5]) + (g1.z * o[6] + g1.w * o[7]));
    const u32x4 un = {pack2(t.x, t.y), tag, pack2(t.z, t.w), tag};
    ST128(un, (old & 255u) == 7u ? o_voff : OOB, WS_O + b * 768);
    if ((old & 255u) == 7u) STAMP(7);
  }
  if (w) STAMP(1);
  KEEP(voob);
  if (w == 0) {
    int spins = 0;
    while (LDW(fl + 1) >> 16 < CN && ++spins < 100 * SPIN_CAP) __builtin_amdgcn_s_sleep(1);
    CBAR();
    STAMP(6);
    role = __builtin_amdgcn_readfirstlane(__builtin_amdgcn_raw_ptr_buffer_atomic_add_i32(1, rs_ws, lane == 0 ? WS_C : OOB, 0, 0));
    if (lane == 0) STW(fl + 2, role + 1u);
    STAMP(14);
  } else if (role == 0xffffffffu) {
    int spins = 0;
    while ((role = __builtin_amdgcn_readfirstlane(LDW(fl + 2)) - 1u) == 0xffffffffu && ++spins < 100 * SPIN_CAP) __builtin_amdgcn_s_sleep(1);
  }
  u32 t0, cnt = SPIN_CAP;
  u64 m;
  if (role < NTILE) {
    if (!late) WISSUE(true);
    TILE();
    const u32 hh = row0 / 192u;
    float wt;
    u32 hid;
    {
      u32x4 un, hv;
      int spins = 0;
      while (true) {
        CBAR();
        un = __builtin_amdgcn_raw_buffer_load_b128(rs_ws, l3 * 16, WS_S + hh * 128, 16);
        hv = __builtin_amdgcn_raw_buffer_load_b128(rs_ws, (lane & 1) * 512 + (dbl ? l3 : l3 & 3) * 16, WS_X + (96 + row0 / 256u) * 1024 + (row0 & 255u) * 2, 16);
        if (__all(un.y == tag && un.w == tag && hv.y == tag && hv.w == tag) || ++spins >= SPIN_CAP) break;
        __builtin_amdgcn_s_sleep(GAPS);
      }
      const float mp = __uint_as_float(un.x);
      const float f = ex2((mp - hi_max(mp)) * LOG2E) * __uint_as_float(un.z);
      wt = f * __builtin_amdgcn_rcpf(hi_sum(f));
      const float e0 = __uint_as_float(hv.x), e1 = __uint_as_float(hv.z);
      hid = pack2(fmaxf(e0 + dpp<XOR1>(e0), 0.f), fmaxf(e1 + dpp<XOR1>(e1), 0.f));
    }
    if (w) STAMP(14);
    u32 wp[4][8];
    float y0[8];
#pragma unroll
    for (int d = 0; d < 4; ++d) {
      y0[2 * d] = dot2(__builtin_amdgcn_perm(wov[9][d], wov[8][d], 0x05040100u), hid, 0.f);
      y0[2 * d + 1] = dot2(__builtin_amdgcn_perm(wov[9][d], wov[8][d], 0x07060302u), hid, 0.f);
      KEEP(y0[2 * d]); KEEP(y0[2 * d + 1]);
    }
#pragma unroll
    for (int j = 0; j < 4; ++j)
#pragma unroll
      for (int d = 0; d < 4; ++d) {
        wp[j][2 * d] = __builtin_amdgcn_perm(wov[2 * j + 1][d], wov[2 * j][d], 0x05040100u);
        wp[j][2 * d + 1] = __builtin_amdgcn_perm(wov[2 * j + 1][d], wov[2 * j][d], 0x07060302u);
        KEEP(wp[j][2 * d]); KEEP(wp[j][2 * d + 1]);
      }
    u32 a_ow = (L_OT + w * 32) * 4 + (8 * l7 + l3) * 2, a_or = (L_OT + w * 32) * 4 + l3 * 16;
    u32 a_yw = (L_Y + w * 64 + 8 * l7 + perm8) * 4, a_yr = (L_Y + lane) * 4;
    u32 yv = WS_Y + (8 * tc + l3) * 1024 + tg * 64 + l7 * 8;
    u32 r0 = WS_O + (8 * hh + l3) * 768 + (row0 - 192 * hh) * 4 + l7 * 16, r1 = dbl ? r0 + 128 : OOB;
    KEEP(a_ow); KEEP(a_or); KEEP(a_yw); KEEP(a_yr); KEEP(yv); KEEP(wt); KEEP(r0); KEEP(r1);
    {
      int spins = 0;
      while ((LDW(fl + 1) & 255u) != 8u && ++spins < 100 * SPIN_CAP) __builtin_amdgcn_s_sleep(1);
      CBAR();
    }
    {
      if (w) STAMP(2);
      u32 c[4] = {0, 0, 0, 0};
      u32x16 R0, R1;
      asm volatile(
P2_LD(216, 219, 220, 223) "s_sleep " STR(GAPO0) "\n"
P2_LD(224, 227, 228, 231) "s_sleep " STR(GAPO0) "\n"
P2_LD(232, 235, 236, 239) "s_sleep " STR(GAPO0) "\n"
P2_LD(240, 243, 244, 247)
"1:\n"
P2_CK(216, 217, 218, 219, 220, 221, 222, 223)
P2_CK(224, 225, 226, 227, 228, 229, 230, 231)
P2_CK(232, 233, 234, 235, 236, 237, 238, 239)
P2_CK(240, 241, 242, 243, 244, 245, 246, 247)
"s_sub_u32 %[cnt], %[cnt], 1\n"
"s_cbranch_scc0 1b\n"
"2:\n"
: [c0] "+v"(c[0]), [c1] "+v"(c[1]), [c2] "+v"(c[2]), [c3] "+v"(c[3]), [r0] "+v"(r0), [r1] "+v"(r1),
  [t0] "=&v"(t0), [cnt] "+s"(cnt), [m] "=&s"(m), "=&{v[216:231]}"(R0), "=&{v[232:247]}"(R1)
: [rs] "s"(rsw), [tag] "v"(tag), [oob] "v"(voob)
: "vcc", "scc", "memory");
      STAMP(8);
      const float t = tsum_hi8(wt * blo(c[0]), wt * bhi(c[0]), wt * blo(c[1]), wt * bhi(c[1]),
                               wt * blo(c[2]), wt * bhi(c[2]), wt * blo(c[3]), wt * bhi(c[3]), b3);
      *reinterpret_cast<u16*>(ldb + a_ow) = (u16)bfbits(t);
      CBAR();
      const u32x4 ov = *reinterpret_cast<const u32x4*>(ldb + a_or);
      float y[8];
#pragma unroll
      for (int e = 0; e < 8; ++e) y[e] = dot2(wp[0][e], ov[0], y0[e]);
#pragma unroll
      for (int j = 1; j < 4; ++j)
#pragma unroll
        for (int e = 0; e < 8; ++e) y[e] = dot2(wp[j][e], ov[j], y[e]);
      *reinterpret_cast<float*>(ldb + a_yw) = tsum_hi8(y[0], y[1], y[2], y[3], y[4], y[5], y[6], y[7], b3);
      BAR();
      STAMP(9);
      if (w == 0) {
        float e[8];
#pragma unroll
        for (int r = 0; r < 8; ++r) e[r] = *reinterpret_cast<const float*>(ldb + a_yr + r * 256);
        const u32x2 un = {__float_as_uint(((e[0] + e[1]) + (e[2] + e[3])) + ((e[4] + e[5]) + (e[6] + e[7]))), tag};
        __builtin_amdgcn_raw_buffer_store_b64(un, rs_ws, yv, 0, 16);
        STAMP(10);
      }
      asm volatile("s_waitcnt vmcnt(0)" :: "{v[216:231]}"(R0), "{v[232:247]}"(R1) : "memory");
    }
  } else if (role < NROLE) {
    const u32 ri = (role - NTILE) * 8u + w;
    const u32 rcol = 8 * ri + 2 * (lane & 3) + (lane >> 5);
    u32 xo = x[rcol];
    u32 ov = (lane & 28) ? OOB : rcol * 2;
    u32 r0 = lane < 4 * NP ? WS_Y + ri * 1024 + lane * 16 : OOB;
    u32 ev = (ri == 0 && lane == 0) ? 0u : OOB;
    const auto rs_out = RSRC(out, D * 2);
    KEEP(xo); KEEP(ov); KEEP(ev);
    {
      u32x16 R0;
      u32 c[2] = {0, 0};
      asm volatile(
PL(232, 235, r0) "s_sleep " STR(GAPY0) "\n"
PL(236, 239, r0) "s_sleep " STR(GAPY0) "\n"
PL(240, 243, r0) "s_sleep " STR(GAPY0) "\n"
PL(244, 247, r0)
"1:\n"
P1_CK(232, 233, 234, 235)
P1_CK(236, 237, 238, 239)
P1_CK(240, 241, 242, 243)
P1_CK(244, 245, 246, 247)
"s_sub_u32 %[cnt], %[cnt], 1\n"
"s_cbranch_scc0 1b\n"
"2:\n"
: [c0] "+v"(c[0]), [c1] "+v"(c[1]), [r0] "+v"(r0), [cnt] "+s"(cnt), [m] "=&s"(m), "=&{v[232:247]}"(R0)
: [rs] "s"(rsw), [tag] "v"(tag), [oob] "v"(voob)
: "vcc", "scc", "memory");
      STAMP(11);
      float v = tsum32(__uint_as_float(c[0]), __uint_as_float(c[1]));
      v = tsum16(v, v);
      v += dpp<ROR8>(v);
      v += dpp<ROR4>(v);
      __builtin_amdgcn_raw_buffer_store_b16((u16)bfbits(v + blo(xo)), rs_out, ov, 0, 0);
      __builtin_amdgcn_raw_buffer_store_b32(tag, rs_ws, ev, 0, 16);
      asm volatile("s_waitcnt vmcnt(0)" :: "{v[232:247]}"(R0) : "memory");
    }
  }
}

extern "C" void kernel_launch(void* const* d_in, const int* in_sizes, int n_in,
                              void* d_out, int out_size, void* d_ws, size_t ws_size,
                              hipStream_t stream) {
  if (ws_size < WS_BYTES) return;
  fused_decode_kernel<<<256, 512, 0, stream>>>(
      (const u16*)d_in[0], (const u16*)d_in[1], (const u16*)d_in[2], (const u16*)d_in[3],
      (const u16*)d_in[4], (const u16*)d_in[6], (const u16*)d_in[7], (u16*)d_out, (u32*)d_ws);
}
```

```cpp
#include <hip/hip_runtime.h>
#include <hip/hip_bf16.h>
#include <stdint.h>

#define PACE 6
#define CL 6
#define KARR 1
#define CN 7
#define TAB 18, 17, 16, 16, 15, 14, 13, 12, 12, 11, 10, 9, 8, 8, 7, 6
#define THR 0
#define GAPT 8
#define SKIP 0
#define NC 8
#define CSTR 256
#define SWZ 1
#define GAPS 16
#define GAPS0 8
#define GAPS1 4
#define GAPX0 4
#define GAPX 4
#define GAPO0 5
#define GAPO 2
#define GAPY0 5
#define GAPY 2
#define QS 33
#define PB 2

typedef unsigned int u32;
typedef unsigned short u16;
typedef unsigned long long u64;
using u32x2 = __attribute__((ext_vector_type(2))) unsigned;
using u32x4 = __attribute__((ext_vector_type(4))) unsigned;
using u32x16 = __attribute__((ext_vector_type(16))) unsigned;
using f32x2 = __attribute__((ext_vector_type(2))) float;
using f32x4 = __attribute__((ext_vector_type(4))) float;
using bf16x2 = __attribute__((ext_vector_type(2))) __bf16;

constexpr int D = 640, DH = 192, SK = 32768, WLEN = 4096;
constexpr int NL = 27;
constexpr int NSLOT = 2 * NL;
constexpr u32 TABN[] = {TAB};
constexpr u32 NP = sizeof(TABN) / sizeof(u32);
constexpr u32 NTILE = 10 * NP, NROLE = NTILE + 10;
static_assert(NP <= 16, "one load per reducer poll");
constexpr u64 tab_pack(int lo, bool first) {
  u64 r = 0;
  u32 c = 0;
  for (int g = 0; g < (int)NP; ++g) {
    if (g >= lo && g < lo + 8) r |= (u64)(first ? c : TABN[g]) << (8 * (g - lo));
    c += TABN[g];
  }
  return r;
}
constexpr bool tab_ok() {
  u32 c = 0;
  for (int g = 0; g < (int)NP; ++g) { if (TABN[g] < 1 || TABN[g] > 24) return false; c += TABN[g]; }
  return c == 192;
}
static_assert(tab_ok(), "tile table");
constexpr u32 tab_max() {
  u32 c = 0;
  for (int g = 0; g < (int)NP; ++g) c = TABN[g] > c ? TABN[g] : c;
  return c;
}
constexpr u32 MAXN = tab_max();
constexpr u32 WS_F = 2560;
constexpr u32 WS_G = 3072;
constexpr u32 WS_S = 4096;
constexpr u32 WS_O = WS_S + 32u * 128u;
constexpr u32 WS_Y = WS_O + 256u * 768u;
constexpr u32 WS_X = WS_Y + 80u * 1024u;
constexpr u32 WS_C = WS_X + 240u * 512u;
constexpr u32 WS_BYTES = WS_C + NC * CSTR;
constexpr int SPIN_CAP = 200000;
constexpr float LOG2E = 1.4426950408889634f;
constexpr float SCALE = 0.07216878364870322f;

__device__ __forceinline__ float dot2(u32 a, u32 b, float acc) {
  return __builtin_amdgcn_fdot2_f32_bf16(__builtin_bit_cast(bf16x2, a), __builtin_bit_cast(bf16x2, b), acc, false);
}
template <int CTRL> __device__ __forceinline__ float dpp(float x) {
  return __builtin_bit_cast(float, __builtin_amdgcn_mov_dpp(__builtin_bit_cast(int, x), CTRL, 0xf, 0xf, true));
}
constexpr int XOR1 = 0xB1, XOR2 = 0x4E, XOR7 = 0x141, ROR8 = 0x128, ROR4 = 0x124;
__device__ __forceinline__ float sum8(float x) {
  x += dpp<XOR1>(x); x += dpp<XOR2>(x); x += dpp<XOR7>(x); return x;
}
__device__ __forceinline__ float fmx(float a, float b) {
  return __builtin_fmaxf(a, b);
}
__device__ __forceinline__ float max8(float x) {
  x = fmx(x, dpp<XOR1>(x)); x = fmx(x, dpp<XOR2>(x)); x = fmx(x, dpp<XOR7>(x)); return x;
}
__device__ __forceinline__ float xrow_sum(float x) {
  auto s = __builtin_amdgcn_permlane16_swap(__float_as_uint(x), __float_as_uint(x), false, false);
  x = __uint_as_float(s[0]) + __uint_as_float(s[1]);
  auto t = __builtin_amdgcn_permlane32_swap(__float_as_uint(x), __float_as_uint(x), false, false);
  return __uint_as_float(t[0]) + __uint_as_float(t[1]);
}
__device__ __forceinline__ float xrow_max(float x) {
  auto s = __builtin_amdgcn_permlane16_swap(__float_as_uint(x), __float_as_uint(x), false, false);
  x = fmx(__uint_as_float(s[0]), __uint_as_float(s[1]));
  auto t = __builtin_amdgcn_permlane32_swap(__float_as_uint(x), __float_as_uint(x), false, false);
  return fmx(__uint_as_float(t[0]), __uint_as_float(t[1]));
}
__device__ __forceinline__ float tsum32(float a, float b) {
  auto t = __builtin_amdgcn_permlane32_swap(__float_as_uint(a), __float_as_uint(b), false, false);
  return __uint_as_float(t[0]) + __uint_as_float(t[1]);
}
__device__ __forceinline__ float tsum16(float a, float b) {
  auto t = __builtin_amdgcn_permlane16_swap(__float_as_uint(a), __float_as_uint(b), false, false);
  return __uint_as_float(t[0]) + __uint_as_float(t[1]);
}
__device__ __forceinline__ float tsum8(float a, float b, bool b3) {
  const float snd = b3 ? a : b, kp = b3 ? b : a;
  return kp + dpp<ROR8>(snd);
}
__device__ __forceinline__ float tsum_hi8(float v0, float v1, float v2, float v3, float v4, float v5, float v6, float v7, bool b3) {
  return tsum8(tsum16(tsum32(v0, v4), tsum32(v2, v6)), tsum16(tsum32(v1, v5), tsum32(v3, v7)), b3);
}
__device__ __forceinline__ float hi_sum(float x) { x += dpp<ROR8>(x); return xrow_sum(x); }
__device__ __forceinline__ float hi_max(float x) { x = fmx(x, dpp<ROR8>(x)); return xrow_max(x); }
__device__ __forceinline__ float wave_sum(float x) { return hi_sum(sum8(x)); }
__device__ __forceinline__ u32 bfbits(float f) { return (u32)__builtin_bit_cast(u16, (__bf16)f); }
__device__ __forceinline__ float bfr(float f) { return __uint_as_float(bfbits(f) << 16); }
__device__ __forceinline__ u32 pack2(float a, float b) {
  bf16x2 p = {(__bf16)a, (__bf16)b};
  return __builtin_bit_cast(u32, p);
}
__device__ __forceinline__ float blo(u32 v) { return __uint_as_float(v << 16); }
__device__ __forceinline__ float bhi(u32 v) { return __uint_as_float(v & 0xffff0000u); }
__device__ __forceinline__ float ex2(float v) { return __builtin_amdgcn_exp2f(v); }
#define BAR() asm volatile("s_waitcnt lgkmcnt(0)\n\ts_barrier" ::: "memory")
#define PIN() __builtin_amdgcn_sched_barrier(0)
#define CBAR() asm volatile("" ::: "memory")
#define STAMP(i)
#define STR_(x) #x
#define STR(x) STR_(x)
#define KEEP(v) asm volatile("" : "+v"(v))
#define RSRC(p, n) __builtin_amdgcn_make_buffer_rsrc((void*)(p), 0, (n), 0x00020000)
#define LDW(p) __hip_atomic_load((p), __ATOMIC_RELAXED, __HIP_MEMORY_SCOPE_WORKGROUP)
#define STW(p, v) __hip_atomic_store((p), (v), __ATOMIC_RELAXED, __HIP_MEMORY_SCOPE_WORKGROUP)

#define WAITVM(n) __builtin_amdgcn_s_waitcnt(0x0F70 | ((n) & 15) | (((n) >> 4) << 14))

#define ST128(v, vo, so) do { __builtin_amdgcn_raw_buffer_store_b128((v), rs_ws, (vo), (so), 16); asm volatile("s_nop 1" :: "v"(v)); } while (0)

constexpr int L_FL = 0;
constexpr int L_ST = 16;
constexpr int L_QS = L_ST + 32;
constexpr int L_XN = L_QS + 100;
constexpr int L_PART = L_XN + 320;
constexpr int L_OT = L_PART + 8 * 192;
constexpr int L_WT = L_OT + 8 * 64;
constexpr int L_HD = L_WT + 64;
constexpr int L_Y = L_HD + 96;
constexpr int L_END = L_Y + 8 * 64;
constexpr u32 OOB = 0x80000000u;

struct Sched { int hi[NSLOT + 1]; };
constexpr int S3 = QS + 9;
constexpr Sched make_sched() {
  Sched t{};
  int j = 0;
  for (int n = S3; n < NSLOT; ++n) {
    int burst = 0;
    while (j < NL && burst < PB && n - (NL + j) >= PACE) { ++j; ++burst; }
    t.hi[n] = j;
  }
  t.hi[NSLOT] = NL;
  return t;
}
constexpr Sched SCH = make_sched();
static_assert(QS >= NL && S3 <= NSLOT - CL && CL >= 1, "schedule");

__device__ __forceinline__ float score8(const u32x4 k0, const u32x4 k1, const u32x4 k2, const u32x4 (&q)[3]) {
  float a = dot2(k0.x, q[0].x, 0.f), c = dot2(k0.y, q[0].y, 0.f), e = dot2(k0.z, q[0].z, 0.f), g = dot2(k0.w, q[0].w, 0.f);
  a = dot2(k1.x, q[1].x, a); c = dot2(k1.y, q[1].y, c); e = dot2(k1.z, q[1].z, e); g = dot2(k1.w, q[1].w, g);
  a = dot2(k2.x, q[2].x, a); c = dot2(k2.y, q[2].y, c); e = dot2(k2.z, q[2].z, e); g = dot2(k2.w, q[2].w, g);
  return bfr(bfr(sum8((a + c) + (e + g))) * SCALE);
}
__device__ __forceinline__ void pv_step(const u32x4 d, const float pp, f32x2 (&acc)[4]) {
#pragma unroll
  for (int c = 0; c < 4; ++c) {
    acc[c] = __builtin_elementwise_fma(f32x2{blo(d[c]), bhi(d[c])}, f32x2{pp, pp}, acc[c]);
    KEEP(acc[c]);
  }
}

#define PL(a, b, r) "buffer_load_dwordx4 v[" #a ":" #b "], %[" #r "], %[rs], 0 offen sc1\n"
#define PC(n, x, y, z, t, ca, cb, r) \
  "s_waitcnt vmcnt(" #n ")\n" \
  "v_cmp_eq_u32 vcc, v" #y ", %[tag]\n" \
  "v_cmp_eq_u32 %[m], v" #t ", %[tag]\n" \
  "s_and_b64 vcc, vcc, %[m]\n" \
  "v_cndmask_b32 %[" #ca "], %[" #ca "], v" #x ", vcc\n" \
  "v_cndmask_b32 %[" #cb "], %[" #cb "], v" #z ", vcc\n" \
  "v_cndmask_b32 %[" #r "], %[" #r "], %[oob], vcc\n"
#define PEND(t, gap) \
  "v_cmp_eq_u32 vcc, %[" #t "], %[oob]\n" \
  "s_cmp_eq_u64 vcc, -1\n" \
  "s_cbranch_scc1 2f\n" \
  "s_sleep " STR(gap) "\n"
#define P3_LD(a0, a3, b0, b3, d0, d3) PL(a0, a3, r0) PL(b0, b3, r1) PL(d0, d3, r2)
#define P3_CK(a0, a1, a2, a3, b0, b1, b2, b3, d0, d1, d2, d3) \
  PC(11, a0, a1, a2, a3, c0, c1, r0) PC(10, b0, b1, b2, b3, c2, c3, r1) PC(9, d0, d1, d2, d3, c4, c5, r2) \
  "v_min3_u32 %[t0], %[r0], %[r1], %[r2]\n" PEND(t0, GAPO) P3_LD(a0, a3, b0, b3, d0, d3)
#define P2_LD(a0, a3, b0, b3) PL(a0, a3, r0) PL(b0, b3, r1)
#define P2_CK(a0, a1, a2, a3, b0, b1, b2, b3) \
  PC(7, a0, a1, a2, a3, c0, c1, r0) PC(6, b0, b1, b2, b3, c2, c3, r1) \
  "v_and_b32 %[t0], %[r0], %[r1]\n" PEND(t0, GAPO) P2_LD(a0, a3, b0, b3)
#define P1_CK(gap, a0, a1, a2, a3) PC(3, a0, a1, a2, a3, c0, c1, r0) PEND(r0, gap) PL(a0, a3, r0)
#define SLP(n) "s_sleep " STR(n) "\n"
#define PLOOP(l) "s_sub_u32 %[cnt], %[cnt], 1\ns_cbranch_scc0 " #l "b\n"

__global__ void __launch_bounds__(512, 2)
fused_decode_kernel(const u16* __restrict__ x, const u16* __restrict__ kc, const u16* __restrict__ vc,
                    const u16* __restrict__ wq, const u16* __restrict__ wo, const u16* __restrict__ w1,
                    const u16* __restrict__ w2, u16* out, u32* __restrict__ ws) {
  __shared__ __attribute__((aligned(16))) float lds[L_END];
  const int tid = threadIdx.x;
  const int lane = tid & 63;
  const int w = __builtin_amdgcn_readfirstlane(tid >> 6);
  const int b = blockIdx.x;
  const int h = b >> 3;
  const int s = b & 7;
  const int l3 = lane >> 3, l7 = lane & 7;
  const bool b3 = lane & 8;
  const int perm8 = 4 * (lane >> 5) + 2 * ((lane >> 4) & 1) + (l3 & 1);

  const auto rs_ws = RSRC(ws, WS_BYTES);
  const u32x4 rsw = {(u32)(size_t)ws, (u32)((size_t)ws >> 32) & 0xffffu, WS_BYTES, 0x00020000u};
  const size_t kvoff = ((size_t)h * SK + (SK - WLEN) + (SWZ ? (s + h) & 7 : s) * 512) * DH;
  const auto rs_k = RSRC(kc + kvoff, 512 * DH * 2);
  const auto rs_v = RSRC(vc + kvoff, 512 * DH * 2);
  const int kvo = l3 * 384 + l7 * 16;
  char* const ldb = reinterpret_cast<char*>(lds);
  u32* const fl = reinterpret_cast<u32*>(lds + L_FL);
  u32* const qs = reinterpret_cast<u32*>(lds + L_QS);

#define PART(u) do { *reinterpret_cast<float*>(ldb + a_w + (u) * 256) = \
    tsum_hi8(acc[u][0].x, acc[u][0].y, acc[u][1].x, acc[u][1].y, acc[u][2].x, acc[u][2].y, acc[u][3].x, acc[u][3].y, b3); PIN(); } while (0)
  u32 role = 0xffffffffu;
  u32 a_w = (L_PART + w * 192 + 8 * l7 + perm8) * 4, a_r = (L_PART + (lane < 48 ? lane : 47) * 4) * 4;
  u32 o_voff = lane < 48 ? lane * 16 : OOB;
  f32x2 acc[3][4];
#pragma unroll
  for (int u = 0; u < 3; ++u)
#pragma unroll
    for (int c = 0; c < 4; ++c) acc[u][c] = f32x2{0.f, 0.f};
  u32 tag;
  u32 voob = OOB, vzero = 0u, vone = 1u;
  asm volatile("" : "+s"(out));

  if (w == 0) {
    __builtin_amdgcn_s_setprio(3);
    const bool noq = s == 7 && (h & 2);
    const auto rs_x = RSRC(x, D * 2);
    const u32x4 xa = __builtin_amdgcn_raw_buffer_load_b128(rs_x, lane * 20, 0, 0);
    const u32 xb = __builtin_amdgcn_raw_buffer_load_b32(rs_x, lane * 20 + 16, 0, 0);
    const int sec = s < 6 ? 12 * (h >> 1) + 2 * s + (h & 1) : 6 * (h >> 2) + (s == 6 ? (h & 3) : 4 + (h & 1));
    const u32 stp = s < 6 ? 12288u : 3072u;
    const int cg = sec >> 1, rg = sec & 1;
    const auto rs_t = RSRC((s < 6 ? wq : w1) + (size_t)rg * 320 * (stp / 2) + cg * 64, noq ? 0 : 319 * stp + 128);
    u32x4 wv[40];
    const u32 tv = l3 * stp + l7 * 16;
    const u32 E = __builtin_amdgcn_raw_buffer_load_b32(rs_ws, 0, 0, 16);
#pragma unroll
    for (int i = 0; i < 40; ++i) { wv[i] = __builtin_amdgcn_raw_buffer_load_b128(rs_t, tv, i * 8 * stp, 2); PIN(); }
    u32x4 kr[3], vr[3];
#pragma unroll
    for (int i = 0; i < 3; ++i) kr[i] = __builtin_amdgcn_raw_buffer_load_b128(rs_k, kvo + i * 128, 0, 2);
#pragma unroll
    for (int i = 0; i < 3; ++i) vr[i] = __builtin_amdgcn_raw_buffer_load_b128(rs_v, kvo + i * 128, 0, 2);
    if (lane < 48) fl[lane] = 0u;
    PIN();
    BAR();
    float xv[10];
    xv[0] = blo(xa.x); xv[1] = bhi(xa.x); xv[2] = blo(xa.y); xv[3] = bhi(xa.y);
    xv[4] = blo(xa.z); xv[5] = bhi(xa.z); xv[6] = blo(xa.w); xv[7] = bhi(xa.w);
    xv[8] = blo(xb); xv[9] = bhi(xb);
    const float sm = ((xv[0] + xv[1]) + (xv[2] + xv[3])) + ((xv[4] + xv[5]) + (xv[6] + xv[7])) + (xv[8] + xv[9]);
    const float mean = wave_sum(sm) * (1.0f / D);
    float sq = 0.f;
#pragma unroll
    for (int e = 0; e < 10; ++e) { const float d = xv[e] - mean; sq += d * d; }
    const float var = wave_sum(sq) * (1.0f / D);
    const float mb = bfr(mean);
    const float den = bfr(__builtin_amdgcn_sqrtf(bfr(bfr(var) + 1e-5f)));
    const float rinv = 1.0f / den;
    u32* xn = reinterpret_cast<u32*>(lds + L_XN);
#pragma unroll
    for (int e = 0; e < 10; ++e) {
      const u32 r = lane * 10 + e, rr = r % 320;
      reinterpret_cast<u16*>(xn)[r - rr + (rr % 8) * 40 + rr / 8] = (u16)bfbits(bfr(xv[e] - mb) * rinv);
    }
    u32x4 xs[5];
#pragma unroll
    for (int i = 0; i < 5; ++i) xs[i] = *reinterpret_cast<const u32x4*>(xn + rg * 160 + l3 * 20 + i * 4);
    STAMP(1);
    float a8[8];
#pragma unroll
    for (int e = 0; e < 8; ++e) a8[e] = 0.f;
#pragma unroll
    for (int i = 0; i < 40; ++i) {
      const u32 v = xs[i / 8][(i / 2) % 4];
      const u32 xl = (i & 1) ? v >> 16 : v & 0xffffu;
      const u32 xh = (i & 1) ? v & 0xffff0000u : v << 16;
#pragma unroll
      for (int d = 0; d < 4; ++d) {
        a8[2 * d] = dot2(wv[i][d], xl, a8[2 * d]);
        a8[2 * d + 1] = dot2(wv[i][d], xh, a8[2 * d + 1]);
        KEEP(a8[2 * d]); KEEP(a8[2 * d + 1]);
      }
      PIN();
    }
    tag = E + 1u + (E == 0xffffffffu);
    STAMP(2);
    {
      float e0 = tsum16(tsum32(a8[0], a8[4]), tsum32(a8[2], a8[6]));
      float e1 = tsum16(tsum32(a8[1], a8[5]), tsum32(a8[3], a8[7]));
      e0 += dpp<ROR8>(e0); e1 += dpp<ROR8>(e1);
      const int gs = s < 6 ? sec : 192 + sec;
      const u32x4 un = {__float_as_uint(e0), tag, __float_as_uint(e1), tag};
      const u32 xv_ = (b3 || noq) ? OOB : (4 * l7 + (lane >> 4 & 1) + 2 * (lane >> 5)) * 16;
      ST128(un, xv_, WS_X + gs * 512);
      __builtin_amdgcn_raw_buffer_store_b64(u32x2{0u, tag}, rs_ws, (b == 0 && lane < NC) ? WS_C + lane * CSTR : OOB, 0, 16);
      STAMP(3);
      u32 r[3], c[3][2];
#pragma unroll
      for (int j = 0; j < 3; ++j) { r[j] = (3 * h + j) * 1024 + lane * 16; c[j][0] = c[j][1] = 0u; }
      __builtin_amdgcn_s_sleep(GAPX0);
      int spins = 0;
      while (true) {
        CBAR();
        u32x4 v[3];
#pragma unroll
        for (int j = 0; j < 3; ++j) v[j] = __builtin_amdgcn_raw_buffer_load_b128(rs_ws, r[j], WS_X, 16);
#pragma unroll
        for (int j = 0; j < 3; ++j) {
          const bool ok = v[j].y == tag && v[j].w == tag;
          c[j][0] = ok ? v[j].x : c[j][0]; c[j][1] = ok ? v[j].z : c[j][1]; r[j] = ok ? OOB : r[j];
        }
        if (__all((r[0] & r[1] & r[2]) == OOB) || ++spins >= SPIN_CAP) break;
        __builtin_amdgcn_s_sleep(GAPX);
      }
#pragma unroll
      for (int j = 0; j < 3; ++j) {
        const float t = tsum32(__uint_as_float(c[j][0]), __uint_as_float(c[j][1]));
        reinterpret_cast<u16*>(qs)[j * 64 + 2 * (lane & 31) + (lane >> 5)] = (u16)bfbits(t);
      }
    }
    qs[96] = tag;
    CBAR();
    if (lane == 0) STW(fl, 1u);
    STAMP(4);
    __builtin_amdgcn_s_setprio(0);
    u32x4 q[3];
#pragma unroll
    for (int u = 0; u < 3; ++u) q[u] = *reinterpret_cast<const u32x4*>(qs + u * 32 + l7 * 4);
    const float sco = score8(kr[0], kr[1], kr[2], q);
    const float mw = hi_max(sco);
    const float p = ex2((sco - mw) * LOG2E);
    const float lw = hi_sum(p);
    if (lane == 0) *reinterpret_cast<f32x4*>(lds + L_ST) = f32x4{mw, lw, __uint_as_float(1u), 0.f};
    STAMP(5);
#pragma unroll
    for (int u = 0; u < 3; ++u) { pv_step(vr[u], p, acc[u]); PART(u); }
    {
      int spins = 0;
      while (true) {
        CBAR();
        const u32 t = __builtin_amdgcn_raw_buffer_load_b32(rs_ws, WS_C + 4, (h % NC) * CSTR, 16);
        if (__builtin_amdgcn_readfirstlane(t) == __builtin_amdgcn_readfirstlane(tag) || ++spins >= SPIN_CAP) break;
        __builtin_amdgcn_s_sleep(GAPS);
      }
    }
    f32x4 ml;
    int spins = 0;
    while (true) {
      CBAR();
      ml = *reinterpret_cast<const f32x4*>(lds + L_ST + 4 * l7);
      if (__all(__float_as_uint(ml.z) != 0u) || ++spins >= 100 * SPIN_CAP) break;
      __builtin_amdgcn_s_sleep(4);
    }
    const float M = max8(ml.x);
    const float f = ex2((ml.x - M) * LOG2E);
    const float L = sum8(f * ml.y);
    const u32x4 un = {__float_as_uint(M), tag, __float_as_uint(L), tag};
    ST128(un, lane == 0 ? 0u : OOB, WS_S + b * 16);
    if (lane < 8) lds[L_FL + 8 + lane] = f * __builtin_amdgcn_rcpf(L);
  } else {
    const int soff = (8 + (w - 1) * 72) * (DH * 2);
    u32x4 kr[NL], vr[NL];
#define ISSUE(n) do { \
      if ((n) < NL) kr[(n) < NL ? (n) : 0] = __builtin_amdgcn_raw_buffer_load_b128(rs_k, kvo + ((n) % 3) * 128, soff + (((n) / 3) % 9) * 3072, 2); \
      else vr[(n) >= NL ? (n) - NL : 0] = __builtin_amdgcn_raw_buffer_load_b128(rs_v, kvo + ((n) % 3) * 128, soff + (((n) / 3) % 9) * 3072, 2); \
      PIN(); } while (0)
#define GATE() WAITVM(PACE - 1)
#pragma unroll
    for (int n = 0; n < PACE; ++n) ISSUE(n);
    asm volatile("s_barrier" ::: "memory");
    KEEP(a_w); KEEP(a_r); KEEP(o_voff);
#pragma unroll
    for (int n = PACE; n < QS; ++n) { GATE(); ISSUE(n); }
    STAMP(3);
    {
      int spins = 0;
      while (LDW(fl) == 0u && ++spins < 100 * SPIN_CAP) __builtin_amdgcn_s_sleep(1);
      CBAR();
    }
    STAMP(4);
    u32x4 q[3];
#pragma unroll
    for (int u = 0; u < 3; ++u) q[u] = *reinterpret_cast<const u32x4*>(qs + u * 32 + l7 * 4);
    tag = qs[96];
    float sc[9];
#pragma unroll
    for (int i = 0; i < 9; ++i) {
      sc[i] = score8(kr[3 * i], kr[3 * i + 1], kr[3 * i + 2], q);
      KEEP(sc[i]);
      PIN();
      GATE(); ISSUE(QS + i);
    }
    const float mw = hi_max(fmx(fmx(fmx(sc[0], sc[1]), fmx(sc[2], sc[3])), fmx(fmx(fmx(sc[4], sc[5]), fmx(sc[6], sc[7])), sc[8])));
#pragma unroll
    for (int i = 0; i < 9; ++i) sc[i] = ex2((sc[i] - mw) * LOG2E);
    const float lw = hi_sum((((sc[0] + sc[1]) + (sc[2] + sc[3])) + ((sc[4] + sc[5]) + (sc[6] + sc[7]))) + sc[8]);
    if (lane == 0) *reinterpret_cast<f32x4*>(lds + L_ST + 4 * w) = f32x4{mw, lw, __uint_as_float(1u), 0.f};
    STAMP(5);
#pragma unroll
    for (int n = S3; n <= NSLOT; ++n) {
#pragma unroll
      for (int j = (n == S3 ? 0 : SCH.hi[n - 1]); j < SCH.hi[n]; ++j) {
        pv_step(vr[j], sc[j / 3], acc[j % 3]);
        PIN();
        if (j == NL - 1) STAMP(6);
        if (j >= NL - 3) PART(j % 3);
      }
      if (n < NSLOT) { GATE(); ISSUE(n); }
      if (n == NSLOT - CL) {
        if (lane == 0) __hip_atomic_fetch_add(fl + 1, 65536u, __ATOMIC_RELAXED, __HIP_MEMORY_SCOPE_WORKGROUP);
        PIN();
      }
    }
  }
  u32 old = 0;
  {
    asm volatile("s_mov_b64 exec, 1\n\tds_add_rtn_u32 %0, %1, %2 offset:4\n\ts_mov_b64 exec, -1" : "=&v"(old) : "v"(vzero), "v"(vone) : "memory");
    const f32x4 g0 = *reinterpret_cast<const f32x4*>(lds + L_FL + 8), g1 = *reinterpret_cast<const f32x4*>(lds + L_FL + 12);
    f32x4 o[8];
#pragma unroll
    for (int r = 0; r < 8; ++r) o[r] = *reinterpret_cast<const f32x4*>(ldb + a_r + r * 768);
#pragma unroll
    for (int r = 0; r < 8; ++r) asm volatile("" : "+v"(o[r]), "+v"(old));
    old = __builtin_amdgcn_readfirstlane(old);
    const f32x4 t = ((g0.x * o[0] + g0.y * o[1]) + (g0.z * o[2] + g0.w * o[3])) + ((g1.x * o[4] + g1.y * o[5]) + (g1.z * o[6] + g1.w * o[7]));
    const u32x4 un = {pack2(t.x, t.y), tag, pack2(t.z, t.w), tag};
    ST128(un, (old & 255u) == 7u ? o_voff : OOB, WS_O + b * 768);
    if ((old & 255u) == 7u) STAMP(7);
  }
  if (w) STAMP(1);
  KEEP(voob);
  if (w == 0) {
    int spins = 0;
    while (LDW(fl + 1) >> 16 < CN && ++spins < 100 * SPIN_CAP) __builtin_amdgcn_s_sleep(1);
    CBAR();
    STAMP(6);
    const u32 stag = __builtin_amdgcn_readfirstlane(tag);
    role = 255u;
    if (!SKIP || __builtin_amdgcn_readfirstlane(__builtin_amdgcn_raw_buffer_load_b32(rs_ws, WS_F, 0, 16)) != stag)
      role = __builtin_amdgcn_readfirstlane(__builtin_amdgcn_raw_ptr_buffer_atomic_add_i32(1, rs_ws, lane == 0 ? WS_C : OOB, (h % NC) * CSTR, 0)) * NC + h % NC;
    if (SKIP && role == NROLE - 1u) __builtin_amdgcn_raw_buffer_store_b32(tag, rs_ws, lane == 0 ? WS_F : OOB, 0, 16);
    if (THR && role == THR - 1u) __builtin_amdgcn_raw_buffer_store_b32(tag, rs_ws, lane == 0 ? WS_G : OOB, 0, 16);
    if (role + 1u < THR) {
      spins = 0;
      while (true) {
        __builtin_amdgcn_s_sleep(GAPT);
        CBAR();
        const u32 t = __builtin_amdgcn_raw_buffer_load_b32(rs_ws, WS_G, 0, 16);
        if (__builtin_amdgcn_readfirstlane(t) == stag || ++spins >= SPIN_CAP) break;
      }
    }
    if (lane == 0) STW(fl + 2, role + 1u);
    STAMP(14);
  } else {
    int spins = 0;
    while ((role = __builtin_amdgcn_readfirstlane(LDW(fl + 2)) - 1u) == 0xffffffffu && ++spins < 100 * SPIN_CAP) __builtin_amdgcn_s_sleep(1);
  }
  u32 t0, cnt = SPIN_CAP;
  u64 m;
  if (role < NTILE) {
    u32 tg, tc;
    asm("s_mul_i32 %0, %2, 205\n\ts_lshr_b32 %0, %0, 11\n\ts_mul_i32 %1, %0, 10\n\ts_sub_u32 %1, %2, %1"
        : "=&s"(tg), "=&s"(tc) : "s"(role) : "scc");
    const u32 c0 = (u32)((tg < 8u ? tab_pack(0, true) : tab_pack(8, true)) >> (8u * (tg & 7u))) & 255u;
    const u32 nc = (u32)((tg < 8u ? tab_pack(0, false) : tab_pack(8, false)) >> (8u * (tg & 7u))) & 255u;
    const u32 k = KARR ? old & 7u : w;
    const u32 nv = (nc + 7u - k) >> 3, ck = c0 + k, h0 = c0 / 6u;
    const auto rs_wo = RSRC(wo + (size_t)(ck * 32u * D + 64u * tc), 2 * 327680 + 31 * 1280 + 128);
    const auto rs_w2 = RSRC(w2 + (size_t)(ck * 8u * D + 64u * tc), 2 * 81920 + 7 * 1280 + 128);
    u32x4 wov[12], w2v[3], sv, hv[3];
    u32 so = 0u, ho[3] = {0u, 0u, 0u};
#define WISSUE(j) do { \
      w2v[j] = __builtin_amdgcn_raw_buffer_load_b128(rs_w2, l3 * 1280 + l7 * 16 + (j) * 81920, 0, 2); \
      _Pragma("unroll") for (int i = 0; i < 4; ++i) \
        wov[4 * (j) + i] = __builtin_amdgcn_raw_buffer_load_b128(rs_wo, l3 * 5120 + l7 * 16 + (j) * 327680 + i * 1280, 0, 2); \
    } while (0)
    if (w == 0) {
      so = 6u * (h0 + l3) < c0 + nc ? lane * 16 : OOB;
#pragma unroll
      for (int j = 0; j < 3; ++j) {
        const u32 g = 32 * j + (lane & 31), gp = 4u * c0 + g;
        ho[j] = ((gp >> 5) * 1024u + (lane >> 5) * 512 + (gp & 31u) * 16u) | (g < 4u * nc ? 0u : OOB);
      }
      KEEP(so); KEEP(ho[0]); KEEP(ho[1]); KEEP(ho[2]);
      asm volatile("buffer_load_dwordx4 %0, %4, %8, %9 offen sc1\n\tbuffer_load_dwordx4 %1, %5, %8, %10 offen sc1\n\t"
                   "buffer_load_dwordx4 %2, %6, %8, %10 offen sc1\n\tbuffer_load_dwordx4 %3, %7, %8, %10 offen sc1"
                   : "=&v"(sv), "=&v"(hv[0]), "=&v"(hv[1]), "=&v"(hv[2])
                   : "v"(so), "v"(ho[0]), "v"(ho[1]), "v"(ho[2]), "s"(rsw), "s"(WS_S + h0 * 128u), "s"(WS_X + 96u * 1024u) : "memory");
    }
    if (nv > 0u) {
      if (nv > 1u) {
        if (nv > 2u) WISSUE(2);
        WISSUE(1);
      }
      WISSUE(0);
    }
    PIN();
    if (w == 0) {
      asm volatile("s_waitcnt vmcnt(15)\n\ts_cmp_gt_u32 %4, 2\n\ts_cbranch_scc1 1f\n\t"
                   "s_waitcnt vmcnt(10)\n\ts_cmp_gt_u32 %4, 1\n\ts_cbranch_scc1 1f\n\t"
                   "s_waitcnt vmcnt(5)\n\ts_cmp_gt_u32 %4, 0\n\ts_cbranch_scc1 1f\n\t"
                   "s_waitcnt vmcnt(0)\n1:"
                   : "+v"(sv), "+v"(hv[0]), "+v"(hv[1]), "+v"(hv[2]) : "s"(nv) : "scc", "memory");
#define HBAD() (((ho[0] | ((hv[0].y ^ tag) | (hv[0].w ^ tag) ? 0u : OOB)) & (ho[1] | ((hv[1].y ^ tag) | (hv[1].w ^ tag) ? 0u : OOB)) & \
                 (ho[2] | ((hv[2].y ^ tag) | (hv[2].w ^ tag) ? 0u : OOB))) < OOB)
      if (__builtin_expect(__any(HBAD()), 0)) {
        int spins = 0;
        do {
          __builtin_amdgcn_s_sleep(GAPS);
          CBAR();
          _Pragma("unroll") for (int j = 0; j < 3; ++j) hv[j] = __builtin_amdgcn_raw_buffer_load_b128(rs_ws, ho[j], WS_X + 96u * 1024u, 16);
        } while (__any(HBAD()) && ++spins < SPIN_CAP);
      }
      u32 r0 = (so >= OOB || (sv.y == tag && sv.w == tag)) ? OOB : so + WS_S + h0 * 128u;
      u32x16 RS;
      asm volatile(
"v_cmp_eq_u32 vcc, %[r0], %[oob]\n"
"s_cmp_eq_u64 vcc, -1\n"
"s_cbranch_scc1 2f\n"
PL(232, 235, r0) SLP(GAPS0)
PL(236, 239, r0) SLP(GAPS0)
PL(240, 243, r0) SLP(GAPS0)
PL(244, 247, r0)
"1:\n"
P1_CK(GAPS1, 232, 233, 234, 235)
P1_CK(GAPS1, 236, 237, 238, 239)
P1_CK(GAPS1, 240, 241, 242, 243)
P1_CK(GAPS1, 244, 245, 246, 247)
PLOOP(1)
"2:\n"
: [c0] "+v"(sv.x), [c1] "+v"(sv.z), [r0] "+v"(r0), [cnt] "+s"(cnt), [m] "=&s"(m), "=&{v[232:247]}"(RS)
: [rs] "s"(rsw), [tag] "v"(tag), [oob] "v"(voob)
: "vcc", "scc", "memory");
      const float mp = __uint_as_float(sv.x);
      const float f = ex2((mp - max8(mp)) * LOG2E) * __uint_as_float(sv.z);
      lds[L_WT + lane] = f * __builtin_amdgcn_rcpf(sum8(f));
#pragma unroll
      for (int j = 0; j < 3; ++j)
        reinterpret_cast<u16*>(lds + L_HD)[64 * j + 2 * (lane & 31) + (lane >> 5)] =
            (u16)bfbits(fmaxf(tsum32(__uint_as_float(hv[j].x), __uint_as_float(hv[j].z)), 0.f));
      CBAR();
      if (lane == 0) STW(fl + 3, 1u);
      asm volatile("s_waitcnt vmcnt(0)" :: "{v[232:247]}"(RS) : "memory");
      cnt = SPIN_CAP;
    } else {
      int spins = 0;
      while (LDW(fl + 3) == 0u && ++spins < 100 * SPIN_CAP) __builtin_amdgcn_s_sleep(2);
    }
    CBAR();
    float wt[3], y[8];
    u32 hid[3], rr[3], wp[6][8];
#pragma unroll
    for (int j = 0; j < 3; ++j) {
      const u32 c = ck + 8 * j, hh = c / 6u;
      wt[j] = lds[L_WT + ((hh - h0) & 7u) * 8u + l3];
      hid[j] = reinterpret_cast<const u16*>(lds + L_HD)[(k + 8 * j) * 8u + l3];
      rr[j] = WS_O + (hh * 8u + l3) * 768u + (c - 6u * hh) * 128u + l7 * 16;
      KEEP(wt[j]); KEEP(rr[j]);
    }
#pragma unroll
    for (int e = 0; e < 8; ++e) y[e] = 0.f;
    u32 a_ow = (L_OT + w * 64) * 4 + l7 * 32 + (l3 >> 1) * 2, a_or = (L_OT + w * 64) * 4 + l3 * 32;
    u32 a_yw = (L_Y + k * 64 + 8 * l7 + perm8) * 4, a_yr = (L_Y + lane) * 4;
    u32 yv = WS_Y + (8 * tc + l3) * 1024 + tg * 64 + l7 * 8;
    KEEP(a_ow); KEEP(a_or); KEEP(a_yw); KEEP(a_yr); KEEP(yv);
    PIN();
#define WPREP(j) do { \
      _Pragma("unroll") for (int d = 0; d < 4; ++d) { \
        y[2 * d] = dot2(w2v[j][d], hid[j], y[2 * d]); \
        y[2 * d + 1] = dot2(w2v[j][d], hid[j] << 16, y[2 * d + 1]); \
        KEEP(y[2 * d]); KEEP(y[2 * d + 1]); \
      } \
      _Pragma("unroll") for (int i = 2 * (j); i < 2 * (j) + 2; ++i) { \
        _Pragma("unroll") for (int d = 0; d < 4; ++d) { \
          wp[i][2 * d] = __builtin_amdgcn_perm(wov[2 * i + 1][d], wov[2 * i][d], 0x05040100u); \
          wp[i][2 * d + 1] = __builtin_amdgcn_perm(wov[2 * i + 1][d], wov[2 * i][d], 0x07060302u); \
          KEEP(wp[i][2 * d]); KEEP(wp[i][2 * d + 1]); \
        } \
        PIN(); \
      } } while (0)
#define ORED(j) do { \
      float u = tsum16(tsum32(wt[j] * blo(c[2 * (j)]), wt[j] * blo(c[2 * (j) + 1])), tsum32(wt[j] * bhi(c[2 * (j)]), wt[j] * bhi(c[2 * (j) + 1]))); \
      u += dpp<ROR8>(u); \
      *reinterpret_cast<u16*>(ldb + a_ow + (j) * 8) = (u16)bfbits(u); } while (0)
#define YDOT(j, o0, o1) do { \
      _Pragma("unroll") for (int e = 0; e < 8; ++e) y[e] = dot2(wp[2 * (j)][e], o0, y[e]); \
      _Pragma("unroll") for (int e = 0; e < 8; ++e) y[e] = dot2(wp[2 * (j) + 1][e], o1, y[e]); } while (0)
    u32x16 R0, R1, R2;
    if (nv > 0u) {
      if (nv > 1u) {
        if (nv > 2u) WPREP(2);
        WPREP(1);
      }
      WPREP(0);
    }
    WAITVM(0);
    u32 nw = nv;
    asm volatile("" : "+s"(nw));
    if (nw > 0u) {
      if (w) STAMP(14);
      {
        int spins = 0;
        while ((LDW(fl + 1) & 255u) != 8u && ++spins < 100 * SPIN_CAP) __builtin_amdgcn_s_sleep(1);
        CBAR();
      }
      if (w) STAMP(2);
      u32 c[6] = {0, 0, 0, 0, 0, 0};
      asm volatile(
"s_cmp_lt_u32 %[nv], 2\n"
"s_cbranch_scc1 3f\n"
"s_cmp_lt_u32 %[nv], 3\n"
"s_cbranch_scc1 4f\n"
P3_LD(200, 203, 204, 207, 208, 211) SLP(GAPO0)
P3_LD(212, 215, 216, 219, 220, 223) SLP(GAPO0)
P3_LD(224, 227, 228, 231, 232, 235) SLP(GAPO0)
P3_LD(236, 239, 240, 243, 244, 247)
"1:\n"
P3_CK(200, 201, 202, 203, 204, 205, 206, 207, 208, 209, 210, 211)
P3_CK(212, 213, 214, 215, 216, 217, 218, 219, 220, 221, 222, 223)
P3_CK(224, 225, 226, 227, 228, 229, 230, 231, 232, 233, 234, 235)
P3_CK(236, 237, 238, 239, 240, 241, 242, 243, 244, 245, 246, 247)
PLOOP(1)
"s_branch 2f\n"
"3:\n"
PL(232, 235, r0) SLP(GAPO0)
PL(236, 239, r0) SLP(GAPO0)
PL(240, 243, r0) SLP(GAPO0)
PL(244, 247, r0)
"5:\n"
P1_CK(GAPO, 232, 233, 234, 235)
P1_CK(GAPO, 236, 237, 238, 239)
P1_CK(GAPO, 240, 241, 242, 243)
P1_CK(GAPO, 244, 245, 246, 247)
PLOOP(5)
"s_branch 2f\n"
"4:\n"
P2_LD(216, 219, 220, 223) SLP(GAPO0)
P2_LD(224, 227, 228, 231) SLP(GAPO0)
P2_LD(232, 235, 236, 239) SLP(GAPO0)
P2_LD(240, 243, 244, 247)
"6:\n"
P2_CK(216, 217, 218, 219, 220, 221, 222, 223)
P2_CK(224, 225, 226, 227, 228, 229, 230, 231)
P2_CK(232, 233, 234, 235, 236, 237, 238, 239)
P2_CK(240, 241, 242, 243, 244, 245, 246, 247)
PLOOP(6)
"2:\n"
: [c0] "+v"(c[0]), [c1] "+v"(c[1]), [c2] "+v"(c[2]), [c3] "+v"(c[3]), [c4] "+v"(c[4]), [c5] "+v"(c[5]),
  [r0] "+v"(rr[0]), [r1] "+v"(rr[1]), [r2] "+v"(rr[2]),
  [t0] "=&v"(t0), [cnt] "+s"(cnt), [m] "=&s"(m), "=&{v[200:215]}"(R0), "=&{v[216:231]}"(R1), "=&{v[232:247]}"(R2)
: [rs] "s"(rsw), [tag] "v"(tag), [oob] "v"(voob), [nv] "s"(nv)
: "vcc", "scc", "memory");
      STAMP(8);
      if (nv == 1u) {
        ORED(0);
        CBAR();
        const u32x2 ov = *reinterpret_cast<const u32x2*>(ldb + a_or);
        YDOT(0, ov[0], ov[1]);
      } else if (MAXN <= 16 || nv == 2u) {
        ORED(0); ORED(1);
        CBAR();
        const u32x4 ov = *reinterpret_cast<const u32x4*>(ldb + a_or);
        YDOT(0, ov[0], ov[1]);
        YDOT(1, ov[2], ov[3]);
      } else {
        ORED(0); ORED(1); ORED(2);
        CBAR();
        const u32x4 ov = *reinterpret_cast<const u32x4*>(ldb + a_or);
        const u32x2 ow = *reinterpret_cast<const u32x2*>(ldb + a_or + 16);
        YDOT(0, ov[0], ov[1]);
        YDOT(1, ov[2], ov[3]);
        YDOT(2, ow[0], ow[1]);
      }
    }
    {
      *reinterpret_cast<float*>(ldb + a_yw) = tsum_hi8(y[0], y[1], y[2], y[3], y[4], y[5], y[6], y[7], b3);
      BAR();
      STAMP(9);
      if (w == 0) {
        float e[8];
#pragma unroll
        for (int r = 0; r < 8; ++r) e[r] = *reinterpret_cast<const float*>(ldb + a_yr + r * 256);
        const u32x2 un = {__float_as_uint(((e[0] + e[1]) + (e[2] + e[3])) + ((e[4] + e[5]) + (e[6] + e[7]))), tag};
        __builtin_amdgcn_raw_buffer_store_b64(un, rs_ws, yv, 0, 16);
        STAMP(10);
      }
      asm volatile("s_waitcnt vmcnt(0)" :: "{v[200:215]}"(R0), "{v[216:231]}"(R1), "{v[232:247]}"(R2) : "memory");
    }
  } else if (role < NROLE) {
    const u32 ri = (role - NTILE) * 8u + w;
    const u32 rcol = 8 * ri + 2 * (lane & 3) + (lane >> 5);
    u32 xo = x[rcol];
    u32 ov = (lane & 28) ? OOB : rcol * 2;
    u32 r0 = lane < 4 * NP ? WS_Y + ri * 1024 + lane * 16 : OOB;
    u32 ev = (ri == 0 && lane == 0) ? 0u : OOB;
    const auto rs_out = RSRC(out, D * 2);
    KEEP(xo); KEEP(ov); KEEP(ev);
    {
      u32x16 R0;
      u32 c[2] = {0, 0};
      asm volatile(
PL(232, 235, r0) "s_sleep " STR(GAPY0) "\n"
PL(236, 239, r0) "s_sleep " STR(GAPY0) "\n"
PL(240, 243, r0) "s_sleep " STR(GAPY0) "\n"
PL(244, 247, r0)
"1:\n"
P1_CK(GAPY, 232, 233, 234, 235)
P1_CK(GAPY, 236, 237, 238, 239)
P1_CK(GAPY, 240, 241, 242, 243)
P1_CK(GAPY, 244, 245, 246, 247)
"s_sub_u32 %[cnt], %[cnt], 1\n"
"s_cbranch_scc0 1b\n"
"2:\n"
: [c0] "+v"(c[0]), [c1] "+v"(c[1]), [r0] "+v"(r0), [cnt] "+s"(cnt), [m] "=&s"(m), "=&{v[232:247]}"(R0)
: [rs] "s"(rsw), [tag] "v"(tag), [oob] "v"(voob)
: "vcc", "scc", "memory");
      STAMP(11);
      float v = tsum32(__uint_as_float(c[0]), __uint_as_float(c[1]));
      v = tsum16(v, v);
      v += dpp<ROR8>(v);
      v += dpp<ROR4>(v);
      __builtin_amdgcn_raw_buffer_store_b16((u16)bfbits(v + blo(xo)), rs_out, ov, 0, 0);
      __builtin_amdgcn_raw_buffer_store_b32(tag, rs_ws, ev, 0, 16);
      asm volatile("s_waitcnt vmcnt(0)" :: "{v[232:247]}"(R0) : "memory");
    }
  }
}

extern "C" void kernel_launch(void* const* d_in, const int* in_sizes, int n_in,
                              void* d_out, int out_size, void* d_ws, size_t ws_size,
                              hipStream_t stream) {
  if (ws_size < WS_BYTES) return;
  fused_decode_kernel<<<256, 512, 0, stream>>>(
      (const u16*)d_in[0], (const u16*)d_in[1], (const u16*)d_in[2], (const u16*)d_in[3],
      (const u16*)d_in[4], (const u16*)d_in[6], (const u16*)d_in[7], (u16*)d_out, (u32*)d_ws);
}
```

```cpp
#include <hip/hip_runtime.h>
#include <hip/hip_bf16.h>
#include <stdint.h>

#define PACE 6
#define CL 6
#define KARR 1
#define CN 7
#define TAB 16, 16, 15, 15, 14, 13, 13, 12, 12, 11, 11, 10, 9, 9, 8, 8
#define THR 0
#define GAPT 8
#define SKIP 0
#define NC 8
#define CSTR 256
#define SWZ 1
#define GAPS 16
#define GAPS0 8
#define GAPS1 4
#define GAPX0 4
#define GAPX 4
#define GAPO0 2
#define GAPO 1
#define GAPY0 5
#define GAPY 2
#define YAT 1
#define ASTR 64
#define RB 23
#define QS 33
#define PB 2

typedef unsigned int u32;
typedef unsigned short u16;
typedef unsigned long long u64;
using u32x2 = __attribute__((ext_vector_type(2))) unsigned;
using u32x4 = __attribute__((ext_vector_type(4))) unsigned;
using u32x16 = __attribute__((ext_vector_type(16))) unsigned;
using f32x2 = __attribute__((ext_vector_type(2))) float;
using f32x4 = __attribute__((ext_vector_type(4))) float;
using bf16x2 = __attribute__((ext_vector_type(2))) __bf16;

constexpr int D = 640, DH = 192, SK = 32768, WLEN = 4096;
constexpr int NL = 27;
constexpr int NSLOT = 2 * NL;
constexpr u32 TABN[] = {TAB};
constexpr u32 NP = sizeof(TABN) / sizeof(u32);
constexpr u32 NTILE = 10 * NP, NROLE = NTILE + (YAT ? 0 : 10);
static_assert(NP <= 16, "one load per reducer poll");
constexpr u64 tab_pack(int lo, bool first) {
  u64 r = 0;
  u32 c = 0;
  for (int g = 0; g < (int)NP; ++g) {
    if (g >= lo && g < lo + 8) r |= (u64)(first ? c : TABN[g]) << (8 * (g - lo));
    c += TABN[g];
  }
  return r;
}
constexpr bool tab_ok() {
  u32 c = 0;
  for (int g = 0; g < (int)NP; ++g) { if (TABN[g] < 1 || TABN[g] > 24) return false; c += TABN[g]; }
  return c == 192;
}
static_assert(tab_ok(), "tile table");
constexpr u32 tab_max() {
  u32 c = 0;
  for (int g = 0; g < (int)NP; ++g) c = TABN[g] > c ? TABN[g] : c;
  return c;
}
constexpr u32 MAXN = tab_max();
constexpr u32 WS_F = 2560;
constexpr u32 WS_G = 3072;
constexpr u32 WS_S = 4096;
constexpr u32 WS_O = WS_S + 32u * 128u;
constexpr u32 WS_Y = WS_O + 256u * 768u;
constexpr u32 WS_X = WS_Y + 80u * 1024u;
constexpr u32 WS_C = WS_X + 240u * 512u;
constexpr u32 WS_A = WS_C + NC * CSTR;
constexpr u32 WS_BYTES = WS_A + 80u * ASTR;
constexpr int SPIN_CAP = 200000;
constexpr float LOG2E = 1.4426950408889634f;
constexpr float SCALE = 0.07216878364870322f;

__device__ __forceinline__ float dot2(u32 a, u32 b, float acc) {
  return __builtin_amdgcn_fdot2_f32_bf16(__builtin_bit_cast(bf16x2, a), __builtin_bit_cast(bf16x2, b), acc, false);
}
template <int CTRL> __device__ __forceinline__ float dpp(float x) {
  return __builtin_bit_cast(float, __builtin_amdgcn_mov_dpp(__builtin_bit_cast(int, x), CTRL, 0xf, 0xf, true));
}
constexpr int XOR1 = 0xB1, XOR2 = 0x4E, XOR7 = 0x141, ROR8 = 0x128, ROR4 = 0x124;
__device__ __forceinline__ float sum8(float x) {
  x += dpp<XOR1>(x); x += dpp<XOR2>(x); x += dpp<XOR7>(x); return x;
}
__device__ __forceinline__ float fmx(float a, float b) {
  return __builtin_fmaxf(a, b);
}
__device__ __forceinline__ float max8(float x) {
  x = fmx(x, dpp<XOR1>(x)); x = fmx(x, dpp<XOR2>(x)); x = fmx(x, dpp<XOR7>(x)); return x;
}
__device__ __forceinline__ float xrow_sum(float x) {
  auto s = __builtin_amdgcn_permlane16_swap(__float_as_uint(x), __float_as_uint(x), false, false);
  x = __uint_as_float(s[0]) + __uint_as_float(s[1]);
  auto t = __builtin_amdgcn_permlane32_swap(__float_as_uint(x), __float_as_uint(x), false, false);
  return __uint_as_float(t[0]) + __uint_as_float(t[1]);
}
__device__ __forceinline__ float xrow_max(float x) {
  auto s = __builtin_amdgcn_permlane16_swap(__float_as_uint(x), __float_as_uint(x), false, false);
  x = fmx(__uint_as_float(s[0]), __uint_as_float(s[1]));
  auto t = __builtin_amdgcn_permlane32_swap(__float_as_uint(x), __float_as_uint(x), false, false);
  return fmx(__uint_as_float(t[0]), __uint_as_float(t[1]));
}
__device__ __forceinline__ float tsum32(float a, float b) {
  auto t = __builtin_amdgcn_permlane32_swap(__float_as_uint(a), __float_as_uint(b), false, false);
  return __uint_as_float(t[0]) + __uint_as_float(t[1]);
}
__device__ __forceinline__ float tsum16(float a, float b) {
  auto t = __builtin_amdgcn_permlane16_swap(__float_as_uint(a), __float_as_uint(b), false, false);
  return __uint_as_float(t[0]) + __uint_as_float(t[1]);
}
__device__ __forceinline__ float tsum8(float a, float b, bool b3) {
  const float snd = b3 ? a : b, kp = b3 ? b : a;
  return kp + dpp<ROR8>(snd);
}
__device__ __forceinline__ float tsum_hi8(float v0, float v1, float v2, float v3, float v4, float v5, float v6, float v7, bool b3) {
  return tsum8(tsum16(tsum32(v0, v4), tsum32(v2, v6)), tsum16(tsum32(v1, v5), tsum32(v3, v7)), b3);
}
__device__ __forceinline__ float hi_sum(float x) { x += dpp<ROR8>(x); return xrow_sum(x); }
__device__ __forceinline__ float hi_max(float x) { x = fmx(x, dpp<ROR8>(x)); return xrow_max(x); }
__device__ __forceinline__ float wave_sum(float x) { return hi_sum(sum8(x)); }
__device__ __forceinline__ u32 bfbits(float f) { return (u32)__builtin_bit_cast(u16, (__bf16)f); }
__device__ __forceinline__ float bfr(float f) { return __uint_as_float(bfbits(f) << 16); }
__device__ __forceinline__ u32 pack2(float a, float b) {
  bf16x2 p = {(__bf16)a, (__bf16)b};
  return __builtin_bit_cast(u32, p);
}
__device__ __forceinline__ float blo(u32 v) { return __uint_as_float(v << 16); }
__device__ __forceinline__ float bhi(u32 v) { return __uint_as_float(v & 0xffff0000u); }
__device__ __forceinline__ float ex2(float v) { return __builtin_amdgcn_exp2f(v); }
#define BAR() asm volatile("s_waitcnt lgkmcnt(0)\n\ts_barrier" ::: "memory")
#define PIN() __builtin_amdgcn_sched_barrier(0)
#define CBAR() asm volatile("" ::: "memory")
#define STAMP(i)
#define STR_(x) #x
#define STR(x) STR_(x)
#define KEEP(v) asm volatile("" : "+v"(v))
#define RSRC(p, n) __builtin_amdgcn_make_buffer_rsrc((void*)(p), 0, (n), 0x00020000)
#define LDW(p) __hip_atomic_load((p), __ATOMIC_RELAXED, __HIP_MEMORY_SCOPE_WORKGROUP)
#define STW(p, v) __hip_atomic_store((p), (v), __ATOMIC_RELAXED, __HIP_MEMORY_SCOPE_WORKGROUP)

#define WAITVM(n) __builtin_amdgcn_s_waitcnt(0x0F70 | ((n) & 15) | (((n) >> 4) << 14))

#define ST128(v, vo, so) do { __builtin_amdgcn_raw_buffer_store_b128((v), rs_ws, (vo), (so), 16); asm volatile("s_nop 1" :: "v"(v)); } while (0)

constexpr int L_FL = 0;
constexpr int L_ST = 16;
constexpr int L_QS = L_ST + 32;
constexpr int L_XN = L_QS + 100;
constexpr int L_PART = L_XN + 320;
constexpr int L_OT = L_PART + 8 * 192;
constexpr int L_WT = L_OT + 8 * 64;
constexpr int L_HD = L_WT + 64;
constexpr int L_Y = L_HD + 96;
constexpr int L_XR = L_Y + 8 * 64;
constexpr int L_END = L_XR + 320;
constexpr u32 OOB = 0x80000000u;

struct Sched { int hi[NSLOT + 1]; };
constexpr int S3 = QS + 9;
constexpr Sched make_sched() {
  Sched t{};
  int j = 0;
  for (int n = S3; n < NSLOT; ++n) {
    int burst = 0;
    while (j < NL && burst < PB && n - (NL + j) >= PACE) { ++j; ++burst; }
    t.hi[n] = j;
  }
  t.hi[NSLOT] = NL;
  return t;
}
constexpr Sched SCH = make_sched();
static_assert(QS >= NL && S3 <= NSLOT - CL && CL >= 1, "schedule");

__device__ __forceinline__ float score8(const u32x4 k0, const u32x4 k1, const u32x4 k2, const u32x4 (&q)[3]) {
  float a = dot2(k0.x, q[0].x, 0.f), c = dot2(k0.y, q[0].y, 0.f), e = dot2(k0.z, q[0].z, 0.f), g = dot2(k0.w, q[0].w, 0.f);
  a = dot2(k1.x, q[1].x, a); c = dot2(k1.y, q[1].y, c); e = dot2(k1.z, q[1].z, e); g = dot2(k1.w, q[1].w, g);
  a = dot2(k2.x, q[2].x, a); c = dot2(k2.y, q[2].y, c); e = dot2(k2.z, q[2].z, e); g = dot2(k2.w, q[2].w, g);
  return bfr(bfr(sum8((a + c) + (e + g))) * SCALE);
}
__device__ __forceinline__ void pv_step(const u32x4 d, const float pp, f32x2 (&acc)[4]) {
#pragma unroll
  for (int c = 0; c < 4; ++c) {
    acc[c] = __builtin_elementwise_fma(f32x2{blo(d[c]), bhi(d[c])}, f32x2{pp, pp}, acc[c]);
    KEEP(acc[c]);
  }
}

#define PL(a, b, r) "buffer_load_dwordx4 v[" #a ":" #b "], %[" #r "], %[rs], 0 offen sc1\n"
#define PC(n, x, y, z, t, ca, cb, r) \
  "s_waitcnt vmcnt(" #n ")\n" \
  "v_cmp_eq_u32 vcc, v" #y ", %[tag]\n" \
  "v_cmp_eq_u32 %[m], v" #t ", %[tag]\n" \
  "s_and_b64 vcc, vcc, %[m]\n" \
  "v_cndmask_b32 %[" #ca "], %[" #ca "], v" #x ", vcc\n" \
  "v_cndmask_b32 %[" #cb "], %[" #cb "], v" #z ", vcc\n" \
  "v_cndmask_b32 %[" #r "], %[" #r "], %[oob], vcc\n"
#define PEND(t, gap) \
  "v_cmp_eq_u32 vcc, %[" #t "], %[oob]\n" \
  "s_cmp_eq_u64 vcc, -1\n" \
  "s_cbranch_scc1 2f\n" \
  "s_sleep " STR(gap) "\n"
#define P3_LD(a0, a3, b0, b3, d0, d3) PL(a0, a3, r0) PL(b0, b3, r1) PL(d0, d3, r2)
#define P3_CK(a0, a1, a2, a3, b0, b1, b2, b3, d0, d1, d2, d3) \
  PC(11, a0, a1, a2, a3, c0, c1, r0) PC(10, b0, b1, b2, b3, c2, c3, r1) PC(9, d0, d1, d2, d3, c4, c5, r2) \
  "v_min3_u32 %[t0], %[r0], %[r1], %[r2]\n" PEND(t0, GAPO) P3_LD(a0, a3, b0, b3, d0, d3)
#define P2_LD(a0, a3, b0, b3) PL(a0, a3, r0) PL(b0, b3, r1)
#define P2_CK(a0, a1, a2, a3, b0, b1, b2, b3) \
  PC(7, a0, a1, a2, a3, c0, c1, r0) PC(6, b0, b1, b2, b3, c2, c3, r1) \
  "v_and_b32 %[t0], %[r0], %[r1]\n" PEND(t0, GAPO) P2_LD(a0, a3, b0, b3)
#define P1_CK(gap, a0, a1, a2, a3) PC(3, a0, a1, a2, a3, c0, c1, r0) PEND(r0, gap) PL(a0, a3, r0)
#define SLP(n) "s_sleep " STR(n) "\n"
#define PLOOP(l) "s_sub_u32 %[cnt], %[cnt], 1\ns_cbranch_scc0 " #l "b\n"

__global__ void __launch_bounds__(512, 2)
fused_decode_kernel(const u16* __restrict__ x, const u16* __restrict__ kc, const u16* __restrict__ vc,
                    const u16* __restrict__ wq, const u16* __restrict__ wo, const u16* __restrict__ w1,
                    const u16* __restrict__ w2, u16* out, u32* __restrict__ ws) {
  __shared__ __attribute__((aligned(16))) float lds[L_END];
  const int tid = threadIdx.x;
  const int lane = tid & 63;
  const int w = __builtin_amdgcn_readfirstlane(tid >> 6);
  const int b = blockIdx.x;
  const int h = b >> 3;
  const int s = b & 7;
  const int l3 = lane >> 3, l7 = lane & 7;
  const bool b3 = lane & 8;
  const int perm8 = 4 * (lane >> 5) + 2 * ((lane >> 4) & 1) + (l3 & 1);

  const auto rs_ws = RSRC(ws, WS_BYTES);
  const u32x4 rsw = {(u32)(size_t)ws, (u32)((size_t)ws >> 32) & 0xffffu, WS_BYTES, 0x00020000u};
  const size_t kvoff = ((size_t)h * SK + (SK - WLEN) + (SWZ ? (s + h) & 7 : s) * 512) * DH;
  const auto rs_k = RSRC(kc + kvoff, 512 * DH * 2);
  const auto rs_v = RSRC(vc + kvoff, 512 * DH * 2);
  const int kvo = l3 * 384 + l7 * 16;
  char* const ldb = reinterpret_cast<char*>(lds);
  u32* const fl = reinterpret_cast<u32*>(lds + L_FL);
  u32* const qs = reinterpret_cast<u32*>(lds + L_QS);

#define PART(u) do { *reinterpret_cast<float*>(ldb + a_w + (u) * 256) = \
    tsum_hi8(acc[u][0].x, acc[u][0].y, acc[u][1].x, acc[u][1].y, acc[u][2].x, acc[u][2].y, acc[u][3].x, acc[u][3].y, b3); PIN(); } while (0)
  u32 role = 0xffffffffu;
  u32 a_w = (L_PART + w * 192 + 8 * l7 + perm8) * 4, a_r = (L_PART + (lane < 48 ? lane : 47) * 4) * 4;
  u32 o_voff = lane < 48 ? lane * 16 : OOB;
  f32x2 acc[3][4];
#pragma unroll
  for (int u = 0; u < 3; ++u)
#pragma unroll
    for (int c = 0; c < 4; ++c) acc[u][c] = f32x2{0.f, 0.f};
  u32 tag;
  u32 voob = OOB, vzero = 0u, vone = 1u;
  asm volatile("" : "+s"(out));

  if (w == 0) {
    __builtin_amdgcn_s_setprio(3);
    const bool noq = s == 7 && (h & 2);
    const auto rs_x = RSRC(x, D * 2);
    const u32x4 xa = __builtin_amdgcn_raw_buffer_load_b128(rs_x, lane * 20, 0, 0);
    const u32 xb = __builtin_amdgcn_raw_buffer_load_b32(rs_x, lane * 20 + 16, 0, 0);
    const int sec = s < 6 ? 12 * (h >> 1) + 2 * s + (h & 1) : 6 * (h >> 2) + (s == 6 ? (h & 3) : 4 + (h & 1));
    const u32 stp = s < 6 ? 12288u : 3072u;
    const int cg = sec >> 1, rg = sec & 1;
    const auto rs_t = RSRC((s < 6 ? wq : w1) + (size_t)rg * 320 * (stp / 2) + cg * 64, noq ? 0 : 319 * stp + 128);
    u32x4 wv[40];
    const u32 tv = l3 * stp + l7 * 16;
    const u32 E = __builtin_amdgcn_raw_buffer_load_b32(rs_ws, 0, 0, 16);
#pragma unroll
    for (int i = 0; i < 40; ++i) { wv[i] = __builtin_amdgcn_raw_buffer_load_b128(rs_t, tv, i * 8 * stp, 2); PIN(); }
    u32x4 kr[3], vr[3];
#pragma unroll
    for (int i = 0; i < 3; ++i) kr[i] = __builtin_amdgcn_raw_buffer_load_b128(rs_k, kvo + i * 128, 0, 2);
#pragma unroll
    for (int i = 0; i < 3; ++i) vr[i] = __builtin_amdgcn_raw_buffer_load_b128(rs_v, kvo + i * 128, 0, 2);
    if (YAT && b == RB) {
      const u32x4 z = {0u, 0u, 0u, 0u};
#pragma unroll
      for (int i = 0; i < 5; ++i) ST128(z, (lane >> 2) * ASTR + (lane & 3) * 16, WS_A + i * 16 * ASTR);
    }
    if (lane < 48) fl[lane] = 0u;
    PIN();
    BAR();
    float xv[10];
    xv[0] = blo(xa.x); xv[1] = bhi(xa.x); xv[2] = blo(xa.y); xv[3] = bhi(xa.y);
    xv[4] = blo(xa.z); xv[5] = bhi(xa.z); xv[6] = blo(xa.w); xv[7] = bhi(xa.w);
    xv[8] = blo(xb); xv[9] = bhi(xb);
    const float sm = ((xv[0] + xv[1]) + (xv[2] + xv[3])) + ((xv[4] + xv[5]) + (xv[6] + xv[7])) + (xv[8] + xv[9]);
    const float mean = wave_sum(sm) * (1.0f / D);
    float sq = 0.f;
#pragma unroll
    for (int e = 0; e < 10; ++e) { const float d = xv[e] - mean; sq += d * d; }
    const float var = wave_sum(sq) * (1.0f / D);
    const float mb = bfr(mean);
    const float den = bfr(__builtin_amdgcn_sqrtf(bfr(bfr(var) + 1e-5f)));
    const float rinv = 1.0f / den;
    u32* xn = reinterpret_cast<u32*>(lds + L_XN);
    if (YAT) {
      u32* xr = reinterpret_cast<u32*>(lds + L_XR) + lane * 5;
      xr[0] = xa.x; xr[1] = xa.y; xr[2] = xa.z; xr[3] = xa.w; xr[4] = xb;
    }
#pragma unroll
    for (int e = 0; e < 10; ++e) {
      const u32 r = lane * 10 + e, rr = r % 320;
      reinterpret_cast<u16*>(xn)[r - rr + (rr % 8) * 40 + rr / 8] = (u16)bfbits(bfr(xv[e] - mb) * rinv);
    }
    u32x4 xs[5];
#pragma unroll
    for (int i = 0; i < 5; ++i) xs[i] = *reinterpret_cast<const u32x4*>(xn + rg * 160 + l3 * 20 + i * 4);
    STAMP(1);
    float a8[8];
#pragma unroll
    for (int e = 0; e < 8; ++e) a8[e] = 0.f;
#pragma unroll
    for (int i = 0; i < 40; ++i) {
      const u32 v = xs[i / 8][(i / 2) % 4];
      const u32 xl = (i & 1) ? v >> 16 : v & 0xffffu;
      const u32 xh = (i & 1) ? v & 0xffff0000u : v << 16;
#pragma unroll
      for (int d = 0; d < 4; ++d) {
        a8[2 * d] = dot2(wv[i][d], xl, a8[2 * d]);
        a8[2 * d + 1] = dot2(wv[i][d], xh, a8[2 * d + 1]);
        KEEP(a8[2 * d]); KEEP(a8[2 * d + 1]);
      }
      PIN();
    }
    tag = E + 1u + (E == 0xffffffffu);
    STAMP(2);
    {
      float e0 = tsum16(tsum32(a8[0], a8[4]), tsum32(a8[2], a8[6]));
      float e1 = tsum16(tsum32(a8[1], a8[5]), tsum32(a8[3], a8[7]));
      e0 += dpp<ROR8>(e0); e1 += dpp<ROR8>(e1);
      const int gs = s < 6 ? sec : 192 + sec;
      const u32x4 un = {__float_as_uint(e0), tag, __float_as_uint(e1), tag};
      const u32 xv_ = (b3 || noq) ? OOB : (4 * l7 + (lane >> 4 & 1) + 2 * (lane >> 5)) * 16;
      ST128(un, xv_, WS_X + gs * 512);
      if (b == RB) {
        asm volatile("s_waitcnt vmcnt(0)" ::: "memory");
        __builtin_amdgcn_raw_buffer_store_b64(u32x2{0u, tag}, rs_ws, lane < NC ? WS_C + lane * CSTR : OOB, 0, 16);
      }
      STAMP(3);
      u32 r[3], c[3][2];
#pragma unroll
      for (int j = 0; j < 3; ++j) { r[j] = (3 * h + j) * 1024 + lane * 16; c[j][0] = c[j][1] = 0u; }
      __builtin_amdgcn_s_sleep(GAPX0);
      int spins = 0;
      while (true) {
        CBAR();
        u32x4 v[3];
#pragma unroll
        for (int j = 0; j < 3; ++j) v[j] = __builtin_amdgcn_raw_buffer_load_b128(rs_ws, r[j], WS_X, 16);
#pragma unroll
        for (int j = 0; j < 3; ++j) {
          const bool ok = v[j].y == tag && v[j].w == tag;
          c[j][0] = ok ? v[j].x : c[j][0]; c[j][1] = ok ? v[j].z : c[j][1]; r[j] = ok ? OOB : r[j];
        }
        if (__all((r[0] & r[1] & r[2]) == OOB) || ++spins >= SPIN_CAP) break;
        __builtin_amdgcn_s_sleep(GAPX);
      }
#pragma unroll
      for (int j = 0; j < 3; ++j) {
        const float t = tsum32(__uint_as_float(c[j][0]), __uint_as_float(c[j][1]));
        reinterpret_cast<u16*>(qs)[j * 64 + 2 * (lane & 31) + (lane >> 5)] = (u16)bfbits(t);
      }
    }
    qs[96] = tag;
    CBAR();
    if (lane == 0) STW(fl, 1u);
    STAMP(4);
    __builtin_amdgcn_s_setprio(0);
    u32x4 q[3];
#pragma unroll
    for (int u = 0; u < 3; ++u) q[u] = *reinterpret_cast<const u32x4*>(qs + u * 32 + l7 * 4);
    const float sco = score8(kr[0], kr[1], kr[2], q);
    const float mw = hi_max(sco);
    const float p = ex2((sco - mw) * LOG2E);
    const float lw = hi_sum(p);
    if (lane == 0) *reinterpret_cast<f32x4*>(lds + L_ST) = f32x4{mw, lw, __uint_as_float(1u), 0.f};
    STAMP(5);
#pragma unroll
    for (int u = 0; u < 3; ++u) { pv_step(vr[u], p, acc[u]); PART(u); }
    {
      int spins = 0;
      while (true) {
        CBAR();
        const u32 t = __builtin_amdgcn_raw_buffer_load_b32(rs_ws, WS_C + 4, (h % NC) * CSTR, 16);
        if (__builtin_amdgcn_readfirstlane(t) == __builtin_amdgcn_readfirstlane(tag) || ++spins >= SPIN_CAP) break;
        __builtin_amdgcn_s_sleep(GAPS);
      }
    }
    f32x4 ml;
    int spins = 0;
    while (true) {
      CBAR();
      ml = *reinterpret_cast<const f32x4*>(lds + L_ST + 4 * l7);
      if (__all(__float_as_uint(ml.z) != 0u) || ++spins >= 100 * SPIN_CAP) break;
      __builtin_amdgcn_s_sleep(4);
    }
    const float M = max8(ml.x);
    const float f = ex2((ml.x - M) * LOG2E);
    const float L = sum8(f * ml.y);
    const u32x4 un = {__float_as_uint(M), tag, __float_as_uint(L), tag};
    ST128(un, lane == 0 ? 0u : OOB, WS_S + b * 16);
    if (lane < 8) lds[L_FL + 8 + lane] = f * __builtin_amdgcn_rcpf(L);
  } else {
    const int soff = (8 + (w - 1) * 72) * (DH * 2);
    u32x4 kr[NL], vr[NL];
#define ISSUE(n) do { \
      if ((n) < NL) kr[(n) < NL ? (n) : 0] = __builtin_amdgcn_raw_buffer_load_b128(rs_k, kvo + ((n) % 3) * 128, soff + (((n) / 3) % 9) * 3072, 2); \
      else vr[(n) >= NL ? (n) - NL : 0] = __builtin_amdgcn_raw_buffer_load_b128(rs_v, kvo + ((n) % 3) * 128, soff + (((n) / 3) % 9) * 3072, 2); \
      PIN(); } while (0)
#define GATE() WAITVM(PACE - 1)
#pragma unroll
    for (int n = 0; n < PACE; ++n) ISSUE(n);
    asm volatile("s_barrier" ::: "memory");
    KEEP(a_w); KEEP(a_r); KEEP(o_voff);
#pragma unroll
    for (int n = PACE; n < QS; ++n) { GATE(); ISSUE(n); }
    STAMP(3);
    {
      int spins = 0;
      while (LDW(fl) == 0u && ++spins < 100 * SPIN_CAP) __builtin_amdgcn_s_sleep(1);
      CBAR();
    }
    STAMP(4);
    u32x4 q[3];
#pragma unroll
    for (int u = 0; u < 3; ++u) q[u] = *reinterpret_cast<const u32x4*>(qs + u * 32 + l7 * 4);
    tag = qs[96];
    float sc[9];
#pragma unroll
    for (int i = 0; i < 9; ++i) {
      sc[i] = score8(kr[3 * i], kr[3 * i + 1], kr[3 * i + 2], q);
      KEEP(sc[i]);
      PIN();
      GATE(); ISSUE(QS + i);
    }
    const float mw = hi_max(fmx(fmx(fmx(sc[0], sc[1]), fmx(sc[2], sc[3])), fmx(fmx(fmx(sc[4], sc[5]), fmx(sc[6], sc[7])), sc[8])));
#pragma unroll
    for (int i = 0; i < 9; ++i) sc[i] = ex2((sc[i] - mw) * LOG2E);
    const float lw = hi_sum((((sc[0] + sc[1]) + (sc[2] + sc[3])) + ((sc[4] + sc[5]) + (sc[6] + sc[7]))) + sc[8]);
    if (lane == 0) *reinterpret_cast<f32x4*>(lds + L_ST + 4 * w) = f32x4{mw, lw, __uint_as_float(1u), 0.f};
    STAMP(5);
#pragma unroll
    for (int n = S3; n <= NSLOT; ++n) {
#pragma unroll
      for (int j = (n == S3 ? 0 : SCH.hi[n - 1]); j < SCH.hi[n]; ++j) {
        pv_step(vr[j], sc[j / 3], acc[j % 3]);
        PIN();
        if (j == NL - 1) STAMP(6);
        if (j >= NL - 3) PART(j % 3);
      }
      if (n < NSLOT) { GATE(); ISSUE(n); }
      if (n == NSLOT - CL) {
        if (lane == 0) __hip_atomic_fetch_add(fl + 1, 65536u, __ATOMIC_RELAXED, __HIP_MEMORY_SCOPE_WORKGROUP);
        PIN();
      }
    }
  }
  u32 old = 0;
  {
    asm volatile("s_mov_b64 exec, 1\n\tds_add_rtn_u32 %0, %1, %2 offset:4\n\ts_mov_b64 exec, -1" : "=&v"(old) : "v"(vzero), "v"(vone) : "memory");
    const f32x4 g0 = *reinterpret_cast<const f32x4*>(lds + L_FL + 8), g1 = *reinterpret_cast<const f32x4*>(lds + L_FL + 12);
    f32x4 o[8];
#pragma unroll
    for (int r = 0; r < 8; ++r) o[r] = *reinterpret_cast<const f32x4*>(ldb + a_r + r * 768);
#pragma unroll
    for (int r = 0; r < 8; ++r) asm volatile("" : "+v"(o[r]), "+v"(old));
    old = __builtin_amdgcn_readfirstlane(old);
    const f32x4 t = ((g0.x * o[0] + g0.y * o[1]) + (g0.z * o[2] + g0.w * o[3])) + ((g1.x * o[4] + g1.y * o[5]) + (g1.z * o[6] + g1.w * o[7]));
    const u32x4 un = {pack2(t.x, t.y), tag, pack2(t.z, t.w), tag};
    ST128(un, (old & 255u) == 7u ? o_voff : OOB, WS_O + b * 768);
    if ((old & 255u) == 7u) STAMP(7);
  }
  if (w) STAMP(1);
  KEEP(voob);
  if (w == 0) {
    int spins = 0;
    while (LDW(fl + 1) >> 16 < CN && ++spins < 100 * SPIN_CAP) __builtin_amdgcn_s_sleep(1);
    CBAR();
    STAMP(6);
    const u32 stag = __builtin_amdgcn_readfirstlane(tag);
    role = 255u;
    if (!SKIP || __builtin_amdgcn_readfirstlane(__builtin_amdgcn_raw_buffer_load_b32(rs_ws, WS_F, 0, 16)) != stag)
      role = __builtin_amdgcn_readfirstlane(__builtin_amdgcn_raw_ptr_buffer_atomic_add_i32(1, rs_ws, lane == 0 ? WS_C : OOB, (h % NC) * CSTR, 0)) * NC + h % NC;
    if (SKIP && role == NROLE - 1u) __builtin_amdgcn_raw_buffer_store_b32(tag, rs_ws, lane == 0 ? WS_F : OOB, 0, 16);
    if (THR && role == THR - 1u) __builtin_amdgcn_raw_buffer_store_b32(tag, rs_ws, lane == 0 ? WS_G : OOB, 0, 16);
    if (role + 1u < THR) {
      spins = 0;
      while (true) {
        __builtin_amdgcn_s_sleep(GAPT);
        CBAR();
        const u32 t = __builtin_amdgcn_raw_buffer_load_b32(rs_ws, WS_G, 0, 16);
        if (__builtin_amdgcn_readfirstlane(t) == stag || ++spins >= SPIN_CAP) break;
      }
    }
    if (lane == 0) STW(fl + 2, role + 1u);
    STAMP(14);
  } else {
    int spins = 0;
    while ((role = __builtin_amdgcn_readfirstlane(LDW(fl + 2)) - 1u) == 0xffffffffu && ++spins < 100 * SPIN_CAP) __builtin_amdgcn_s_sleep(1);
  }
  u32 t0, cnt = SPIN_CAP;
  u64 m;
  if (role < NTILE) {
    u32 tg, tc;
    asm("s_mul_i32 %0, %2, 205\n\ts_lshr_b32 %0, %0, 11\n\ts_mul_i32 %1, %0, 10\n\ts_sub_u32 %1, %2, %1"
        : "=&s"(tg), "=&s"(tc) : "s"(role) : "scc");
    const u32 c0 = (u32)((tg < 8u ? tab_pack(0, true) : tab_pack(8, true)) >> (8u * (tg & 7u))) & 255u;
    const u32 nc = (u32)((tg < 8u ? tab_pack(0, false) : tab_pack(8, false)) >> (8u * (tg & 7u))) & 255u;
    const u32 k = KARR ? old & 7u : w;
    const u32 nv = (nc + 7u - k) >> 3, ck = c0 + k, h0 = c0 / 6u;
    const auto rs_wo = RSRC(wo + (size_t)(ck * 32u * D + 64u * tc), 2 * 327680 + 31 * 1280 + 128);
    const auto rs_w2 = RSRC(w2 + (size_t)(ck * 8u * D + 64u * tc), 2 * 81920 + 7 * 1280 + 128);
    u32x4 wov[12], w2v[3], sv, hv[3];
    u32 so = 0u, ho[3] = {0u, 0u, 0u};
#define WISSUE(j) do { \
      w2v[j] = __builtin_amdgcn_raw_buffer_load_b128(rs_w2, l3 * 1280 + l7 * 16 + (j) * 81920, 0, 2); \
      _Pragma("unroll") for (int i = 0; i < 4; ++i) \
        wov[4 * (j) + i] = __builtin_amdgcn_raw_buffer_load_b128(rs_wo, l3 * 5120 + l7 * 16 + (j) * 327680 + i * 1280, 0, 2); \
    } while (0)
    if (w == 0) {
      so = 6u * (h0 + l3) < c0 + nc ? lane * 16 : OOB;
#pragma unroll
      for (int j = 0; j < 3; ++j) {
        const u32 g = 32 * j + (lane & 31), gp = 4u * c0 + g;
        ho[j] = ((gp >> 5) * 1024u + (lane >> 5) * 512 + (gp & 31u) * 16u) | (g < 4u * nc ? 0u : OOB);
      }
      KEEP(so); KEEP(ho[0]); KEEP(ho[1]); KEEP(ho[2]);
      asm volatile("buffer_load_dwordx4 %0, %4, %8, %9 offen sc1\n\tbuffer_load_dwordx4 %1, %5, %8, %10 offen sc1\n\t"
                   "buffer_load_dwordx4 %2, %6, %8, %10 offen sc1\n\tbuffer_load_dwordx4 %3, %7, %8, %10 offen sc1"
                   : "=&v"(sv), "=&v"(hv[0]), "=&v"(hv[1]), "=&v"(hv[2])
                   : "v"(so), "v"(ho[0]), "v"(ho[1]), "v"(ho[2]), "s"(rsw), "s"(WS_S + h0 * 128u), "s"(WS_X + 96u * 1024u) : "memory");
    }
    if (nv > 0u) {
      if (nv > 1u) {
        if (nv > 2u) WISSUE(2);
        WISSUE(1);
      }
      WISSUE(0);
    }
    PIN();
    if (w == 0) {
      asm volatile("s_waitcnt vmcnt(15)\n\ts_cmp_gt_u32 %4, 2\n\ts_cbranch_scc1 1f\n\t"
                   "s_waitcnt vmcnt(10)\n\ts_cmp_gt_u32 %4, 1\n\ts_cbranch_scc1 1f\n\t"
                   "s_waitcnt vmcnt(5)\n\ts_cmp_gt_u32 %4, 0\n\ts_cbranch_scc1 1f\n\t"
                   "s_waitcnt vmcnt(0)\n1:"
                   : "+v"(sv), "+v"(hv[0]), "+v"(hv[1]), "+v"(hv[2]) : "s"(nv) : "scc", "memory");
#define HBAD() (((ho[0] | ((hv[0].y ^ tag) | (hv[0].w ^ tag) ? 0u : OOB)) & (ho[1] | ((hv[1].y ^ tag) | (hv[1].w ^ tag) ? 0u : OOB)) & \
                 (ho[2] | ((hv[2].y ^ tag) | (hv[2].w ^ tag) ? 0u : OOB))) < OOB)
      if (__builtin_expect(__any(HBAD()), 0)) {
        int spins = 0;
        do {
          __builtin_amdgcn_s_sleep(GAPS);
          CBAR();
          _Pragma("unroll") for (int j = 0; j < 3; ++j) hv[j] = __builtin_amdgcn_raw_buffer_load_b128(rs_ws, ho[j], WS_X + 96u * 1024u, 16);
        } while (__any(HBAD()) && ++spins < SPIN_CAP);
      }
      u32 r0 = (so >= OOB || (sv.y == tag && sv.w == tag)) ? OOB : so + WS_S + h0 * 128u;
      u32x16 RS;
      asm volatile(
"v_cmp_eq_u32 vcc, %[r0], %[oob]\n"
"s_cmp_eq_u64 vcc, -1\n"
"s_cbranch_scc1 2f\n"
PL(232, 235, r0) SLP(GAPS0)
PL(236, 239, r0) SLP(GAPS0)
PL(240, 243, r0) SLP(GAPS0)
PL(244, 247, r0)
"1:\n"
P1_CK(GAPS1, 232, 233, 234, 235)
P1_CK(GAPS1, 236, 237, 238, 239)
P1_CK(GAPS1, 240, 241, 242, 243)
P1_CK(GAPS1, 244, 245, 246, 247)
PLOOP(1)
"2:\n"
: [c0] "+v"(sv.x), [c1] "+v"(sv.z), [r0] "+v"(r0), [cnt] "+s"(cnt), [m] "=&s"(m), "=&{v[232:247]}"(RS)
: [rs] "s"(rsw), [tag] "v"(tag), [oob] "v"(voob)
: "vcc", "scc", "memory");
      const float mp = __uint_as_float(sv.x);
      const float f = ex2((mp - max8(mp)) * LOG2E) * __uint_as_float(sv.z);
      lds[L_WT + lane] = f * __builtin_amdgcn_rcpf(sum8(f));
#pragma unroll
      for (int j = 0; j < 3; ++j)
        reinterpret_cast<u16*>(lds + L_HD)[64 * j + 2 * (lane & 31) + (lane >> 5)] =
            (u16)bfbits(fmaxf(tsum32(__uint_as_float(hv[j].x), __uint_as_float(hv[j].z)), 0.f));
      CBAR();
      if (lane == 0) STW(fl + 3, 1u);
      asm volatile("s_waitcnt vmcnt(0)" :: "{v[232:247]}"(RS) : "memory");
      cnt = SPIN_CAP;
    } else {
      int spins = 0;
      while (LDW(fl + 3) == 0u && ++spins < 100 * SPIN_CAP) __builtin_amdgcn_s_sleep(2);
    }
    CBAR();
    float wt[3], y[8];
    u32 hid[3], rr[3], wp[6][8];
#pragma unroll
    for (int j = 0; j < 3; ++j) {
      const u32 c = ck + 8 * j, hh = c / 6u;
      wt[j] = lds[L_WT + ((hh - h0) & 7u) * 8u + l3];
      hid[j] = reinterpret_cast<const u16*>(lds + L_HD)[(k + 8 * j) * 8u + l3];
      rr[j] = WS_O + (hh * 8u + l3) * 768u + (c - 6u * hh) * 128u + l7 * 16;
      KEEP(wt[j]); KEEP(rr[j]);
    }
#pragma unroll
    for (int e = 0; e < 8; ++e) y[e] = 0.f;
    u32 a_ow = (L_OT + w * 64) * 4 + l7 * 32 + (l3 >> 1) * 2, a_or = (L_OT + w * 64) * 4 + l3 * 32;
    u32 a_yw = (L_Y + k * 64 + 8 * l7 + perm8) * 4, a_yr = (L_Y + lane) * 4;
    u32 yv = WS_Y + (8 * tc + l3) * 1024 + tg * 64 + l7 * 8;
    KEEP(a_ow); KEEP(a_or); KEEP(a_yw); KEEP(a_yr); KEEP(yv);
    u32 av = WS_A + (8 * tc + l3) * ASTR + l7 * 8, ov = (64 * tc + lane) * 2, ev = (tc == 0 && lane == 0) ? 0u : OOB;
    float xo = blo(reinterpret_cast<const u16*>(lds + L_XR)[64 * tc + lane]);
    if (YAT) { KEEP(av); KEEP(ov); KEEP(ev); KEEP(xo); }
    PIN();
#define WPREP(j) do { \
      _Pragma("unroll") for (int d = 0; d < 4; ++d) { \
        y[2 * d] = dot2(w2v[j][d], hid[j], y[2 * d]); \
        y[2 * d + 1] = dot2(w2v[j][d], hid[j] << 16, y[2 * d + 1]); \
        KEEP(y[2 * d]); KEEP(y[2 * d + 1]); \
      } \
      _Pragma("unroll") for (int i = 2 * (j); i < 2 * (j) + 2; ++i) { \
        _Pragma("unroll") for (int d = 0; d < 4; ++d) { \
          wp[i][2 * d] = __builtin_amdgcn_perm(wov[2 * i + 1][d], wov[2 * i][d], 0x05040100u); \
          wp[i][2 * d + 1] = __builtin_amdgcn_perm(wov[2 * i + 1][d], wov[2 * i][d], 0x07060302u); \
          KEEP(wp[i][2 * d]); KEEP(wp[i][2 * d + 1]); \
        } \
        PIN(); \
      } } while (0)
#define ORED(j) do { \
      float u = tsum16(tsum32(wt[j] * blo(c[2 * (j)]), wt[j] * blo(c[2 * (j) + 1])), tsum32(wt[j] * bhi(c[2 * (j)]), wt[j] * bhi(c[2 * (j) + 1]))); \
      u += dpp<ROR8>(u); \
      *reinterpret_cast<u16*>(ldb + a_ow + (j) * 8) = (u16)bfbits(u); } while (0)
#define YDOT(j, o0, o1) do { \
      _Pragma("unroll") for (int e = 0; e < 8; ++e) y[e] = dot2(wp[2 * (j)][e], o0, y[e]); \
      _Pragma("unroll") for (int e = 0; e < 8; ++e) y[e] = dot2(wp[2 * (j) + 1][e], o1, y[e]); } while (0)
    u32x16 R0, R1, R2;
    if (nv > 0u) {
      if (nv > 1u) {
        if (nv > 2u) WPREP(2);
        WPREP(1);
      }
      WPREP(0);
    }
    WAITVM(0);
    u32 nw = nv;
    asm volatile("" : "+s"(nw));
    if (nw > 0u) {
      if (w) STAMP(14);
      {
        int spins = 0;
        while ((LDW(fl + 1) & 255u) != 8u && ++spins < 100 * SPIN_CAP) __builtin_amdgcn_s_sleep(1);
        CBAR();
      }
      if (w) STAMP(2);
      u32 c[6] = {0, 0, 0, 0, 0, 0};
      asm volatile(
"s_cmp_lt_u32 %[nv], 2\n"
"s_cbranch_scc1 3f\n"
"s_cmp_lt_u32 %[nv], 3\n"
"s_cbranch_scc1 4f\n"
P3_LD(200, 203, 204, 207, 208, 211) SLP(GAPO0)
P3_LD(212, 215, 216, 219, 220, 223) SLP(GAPO0)
P3_LD(224, 227, 228, 231, 232, 235) SLP(GAPO0)
P3_LD(236, 239, 240, 243, 244, 247)
"1:\n"
P3_CK(200, 201, 202, 203, 204, 205, 206, 207, 208, 209, 210, 211)
P3_CK(212, 213, 214, 215, 216, 217, 218, 219, 220, 221, 222, 223)
P3_CK(224, 225, 226, 227, 228, 229, 230, 231, 232, 233, 234, 235)
P3_CK(236, 237, 238, 239, 240, 241, 242, 243, 244, 245, 246, 247)
PLOOP(1)
"s_branch 2f\n"
"3:\n"
PL(232, 235, r0) SLP(GAPO0)
PL(236, 239, r0) SLP(GAPO0)
PL(240, 243, r0) SLP(GAPO0)
PL(244, 247, r0)
"5:\n"
P1_CK(GAPO, 232, 233, 234, 235)
P1_CK(GAPO, 236, 237, 238, 239)
P1_CK(GAPO, 240, 241, 242, 243)
P1_CK(GAPO, 244, 245, 246, 247)
PLOOP(5)
"s_branch 2f\n"
"4:\n"
P2_LD(216, 219, 220, 223) SLP(GAPO0)
P2_LD(224, 227, 228, 231) SLP(GAPO0)
P2_LD(232, 235, 236, 239) SLP(GAPO0)
P2_LD(240, 243, 244, 247)
"6:\n"
P2_CK(216, 217, 218, 219, 220, 221, 222, 223)
P2_CK(224, 225, 226, 227, 228, 229, 230, 231)
P2_CK(232, 233, 234, 235, 236, 237, 238, 239)
P2_CK(240, 241, 242, 243, 244, 245, 246, 247)
PLOOP(6)
"2:\n"
: [c0] "+v"(c[0]), [c1] "+v"(c[1]), [c2] "+v"(c[2]), [c3] "+v"(c[3]), [c4] "+v"(c[4]), [c5] "+v"(c[5]),
  [r0] "+v"(rr[0]), [r1] "+v"(rr[1]), [r2] "+v"(rr[2]),
  [t0] "=&v"(t0), [cnt] "+s"(cnt), [m] "=&s"(m), "=&{v[200:215]}"(R0), "=&{v[216:231]}"(R1), "=&{v[232:247]}"(R2)
: [rs] "s"(rsw), [tag] "v"(tag), [oob] "v"(voob), [nv] "s"(nv)
: "vcc", "scc", "memory");
      STAMP(8);
      if (nv == 1u) {
        ORED(0);
        CBAR();
        const u32x2 ov = *reinterpret_cast<const u32x2*>(ldb + a_or);
        YDOT(0, ov[0], ov[1]);
      } else if (MAXN <= 16 || nv == 2u) {
        ORED(0); ORED(1);
        CBAR();
        const u32x4 ov = *reinterpret_cast<const u32x4*>(ldb + a_or);
        YDOT(0, ov[0], ov[1]);
        YDOT(1, ov[2], ov[3]);
      } else {
        ORED(0); ORED(1); ORED(2);
        CBAR();
        const u32x4 ov = *reinterpret_cast<const u32x4*>(ldb + a_or);
        const u32x2 ow = *reinterpret_cast<const u32x2*>(ldb + a_or + 16);
        YDOT(0, ov[0], ov[1]);
        YDOT(1, ov[2], ov[3]);
        YDOT(2, ow[0], ow[1]);
      }
    }
    {
      *reinterpret_cast<float*>(ldb + a_yw) = tsum_hi8(y[0], y[1], y[2], y[3], y[4], y[5], y[6], y[7], b3);
      BAR();
      STAMP(9);
      if (w < (YAT ? 2 : 1)) {
        float e[8];
#pragma unroll
        for (int r = 0; r < 8; ++r) e[r] = *reinterpret_cast<const float*>(ldb + a_yr + r * 256);
        const float es = ((e[0] + e[1]) + (e[2] + e[3])) + ((e[4] + e[5]) + (e[6] + e[7]));
        if (YAT && w == 0) {
          const bool ok = __builtin_fabsf(es) < 131000.f;
          const u32x2 fx = __builtin_bit_cast(u32x2, (double)es + 1179648.0);
          u32x2 ol = {ok ? fx.x : 0u, ok ? fx.y + ((1u << 22) - 0x41300000u) : (1u << 22) + (1u << 27)};
          u64 mine = __builtin_bit_cast(u64, ol);
          asm volatile("buffer_atomic_add_x2 %0, %1, %2, 0 offen sc0" : "+v"(ol) : "v"(av), "s"(rsw) : "memory");
          STAMP(10);
          mine += 0x80000000ull;
          KEEP(mine);
          asm volatile("s_waitcnt vmcnt(0)" : "+v"(ol) :: "memory");
          STAMP(11);
          const u64 tt = __builtin_bit_cast(u64, ol) + mine;
          const u32 th = (u32)(tt >> 32);
          const bool last = ((th >> 22) & 31u) == NP;
          float r = xo + __builtin_fmaf((float)(int)((u32)tt ^ 0x80000000u), 2.3283064365386963e-10f, (float)(int)((th & 0x3fffffu) - NP * 131072u));
          if (__builtin_expect(__any(last && ((th >> 27) != 0u || __builtin_fabsf(r) < 6.103515625e-05f)), 0)) {
            float p[NP];
            int spins = 0;
            bool got;
            do {
              CBAR();
              got = true;
#pragma unroll
              for (int g = 0; g < (int)NP; ++g) {
                const u32x2 v = __builtin_amdgcn_raw_buffer_load_b64(rs_ws, last ? yv - tg * 64 + g * 64 : OOB, 0, 16);
                p[g] = __uint_as_float(v.x);
                got = got && v.y == tag;
              }
            } while (__any(last && !got) && ++spins < SPIN_CAP);
            float a = p[0];
#pragma unroll
            for (int g = 1; g < (int)NP; ++g) a += p[g];
            if ((th >> 27) != 0u || __builtin_fabsf(r) < 6.103515625e-05f) r = xo + a;
          }
          const auto rs_out = RSRC(out, D * 2);
          __builtin_amdgcn_raw_buffer_store_b16((u16)bfbits(r), rs_out, last ? ov : OOB, 0, 0);
          __builtin_amdgcn_raw_buffer_store_b32(tag, rs_ws, last ? ev : OOB, 0, 16);
        } else {
          const u32x2 un = {__float_as_uint(es), tag};
          __builtin_amdgcn_raw_buffer_store_b64(un, rs_ws, yv, 0, 16);
          if (!YAT) STAMP(10);
        }
      }
      asm volatile("s_waitcnt vmcnt(0)" :: "{v[200:215]}"(R0), "{v[216:231]}"(R1), "{v[232:247]}"(R2) : "memory");
    }
  } else if (!YAT && role < NROLE) {
    const u32 ri = (role - NTILE) * 8u + w;
    const u32 rcol = 8 * ri + 2 * (lane & 3) + (lane >> 5);
    u32 xo = x[rcol];
    u32 ov = (lane & 28) ? OOB : rcol * 2;
    u32 r0 = lane < 4 * NP ? WS_Y + ri * 1024 + lane * 16 : OOB;
    u32 ev = (ri == 0 && lane == 0) ? 0u : OOB;
    const auto rs_out = RSRC(out, D * 2);
    KEEP(xo); KEEP(ov); KEEP(ev);
    {
      u32x16 R0;
      u32 c[2] = {0, 0};
      asm volatile(
PL(232, 235, r0) "s_sleep " STR(GAPY0) "\n"
PL(236, 239, r0) "s_sleep " STR(GAPY0) "\n"
PL(240, 243, r0) "s_sleep " STR(GAPY0) "\n"
PL(244, 247, r0)
"1:\n"
P1_CK(GAPY, 232, 233, 234, 235)
P1_CK(GAPY, 236, 237, 238, 239)
P1_CK(GAPY, 240, 241, 242, 243)
P1_CK(GAPY, 244, 245, 246, 247)
"s_sub_u32 %[cnt], %[cnt], 1\n"
"s_cbranch_scc0 1b\n"
"2:\n"
: [c0] "+v"(c[0]), [c1] "+v"(c[1]), [r0] "+v"(r0), [cnt] "+s"(cnt), [m] "=&s"(m), "=&{v[232:247]}"(R0)
: [rs] "s"(rsw), [tag] "v"(tag), [oob] "v"(voob)
: "vcc", "scc", "memory");
      STAMP(11);
      float v = tsum32(__uint_as_float(c[0]), __uint_as_float(c[1]));
      v = tsum16(v, v);
      v += dpp<ROR8>(v);
      v += dpp<ROR4>(v);
      __builtin_amdgcn_raw_buffer_store_b16((u16)bfbits(v + blo(xo)), rs_out, ov, 0, 0);
      __builtin_amdgcn_raw_buffer_store_b32(tag, rs_ws, ev, 0, 16);
      asm volatile("s_waitcnt vmcnt(0)" :: "{v[232:247]}"(R0) : "memory");
    }
  }
}

extern "C" void kernel_launch(void* const* d_in, const int* in_sizes, int n_in,
                              void* d_out, int out_size, void* d_ws, size_t ws_size,
                              hipStream_t stream) {
  if (ws_size < WS_BYTES) return;
  fused_decode_kernel<<<256, 512, 0, stream>>>(
      (const u16*)d_in[0], (const u16*)d_in[1], (const u16*)d_in[2], (const u16*)d_in[3],
      (const u16*)d_in[4], (const u16*)d_in[6], (const u16*)d_in[7], (u16*)d_out, (u32*)d_ws);
}
```

```cpp
#include <hip/hip_runtime.h>
#include <hip/hip_bf16.h>
#include <stdint.h>

#define PACE 6
#define CL 6
#define KARR 1
#define CN 7
#define TAB 16, 16, 15, 15, 14, 13, 13, 12, 12, 11, 11, 10, 9, 9, 8, 8
#define THR 0
#define GAPT 8
#define SKIP 0
#define NC 8
#define CSTR 256
#define SWZ 1
#define IL 1
#define ROT 0
#define EV 0
#define GAPS 16
#define GAPS0 8
#define GAPS1 4
#define GAPX0 4
#define GAPX 4
#define GAPO0 2
#define GAPO 1
#define GAPY0 5
#define GAPY 2
#define YAT 1
#define ASTR 64
#define RB 23
#define QS 33
#define PB 2

typedef unsigned int u32;
typedef unsigned short u16;
typedef unsigned long long u64;
using u32x2 = __attribute__((ext_vector_type(2))) unsigned;
using u32x4 = __attribute__((ext_vector_type(4))) unsigned;
using u32x16 = __attribute__((ext_vector_type(16))) unsigned;
using f32x2 = __attribute__((ext_vector_type(2))) float;
using f32x4 = __attribute__((ext_vector_type(4))) float;
using bf16x2 = __attribute__((ext_vector_type(2))) __bf16;

constexpr int D = 640, DH = 192, SK = 32768, WLEN = 4096;
constexpr int NL = 27;
constexpr int NSLOT = 2 * NL;
constexpr u32 TABN[] = {TAB};
constexpr u32 NP = sizeof(TABN) / sizeof(u32);
constexpr u32 NTILE = 10 * NP, NROLE = NTILE + (YAT ? 0 : 10);
static_assert(NP <= 16, "one load per reducer poll");
constexpr u64 tab_pack(int lo, bool first) {
  u64 r = 0;
  u32 c = 0;
  for (int g = 0; g < (int)NP; ++g) {
    if (g >= lo && g < lo + 8) r |= (u64)(first ? c : TABN[g]) << (8 * (g - lo));
    c += TABN[g];
  }
  return r;
}
constexpr bool tab_ok() {
  u32 c = 0;
  for (int g = 0; g < (int)NP; ++g) { if (TABN[g] < 1 || TABN[g] > 24) return false; c += TABN[g]; }
  return c == 192;
}
static_assert(tab_ok(), "tile table");
constexpr u32 tab_max() {
  u32 c = 0;
  for (int g = 0; g < (int)NP; ++g) c = TABN[g] > c ? TABN[g] : c;
  return c;
}
constexpr u32 MAXN = tab_max();
constexpr u32 WS_F = 2560;
constexpr u32 WS_G = 3072;
constexpr u32 WS_S = 4096;
constexpr u32 WS_O = WS_S + 32u * 128u;
constexpr u32 WS_Y = WS_O + 256u * 768u;
constexpr u32 WS_X = WS_Y + 80u * 1024u;
constexpr u32 WS_C = WS_X + 240u * 512u;
constexpr u32 WS_A = WS_C + NC * CSTR;
constexpr u32 WS_BYTES = WS_A + 80u * ASTR;
constexpr int SPIN_CAP = 200000;
constexpr float LOG2E = 1.4426950408889634f;
constexpr float SCALE = 0.07216878364870322f;

__device__ __forceinline__ float dot2(u32 a, u32 b, float acc) {
  return __builtin_amdgcn_fdot2_f32_bf16(__builtin_bit_cast(bf16x2, a), __builtin_bit_cast(bf16x2, b), acc, false);
}
template <int CTRL> __device__ __forceinline__ float dpp(float x) {
  return __builtin_bit_cast(float, __builtin_amdgcn_mov_dpp(__builtin_bit_cast(int, x), CTRL, 0xf, 0xf, true));
}
constexpr int XOR1 = 0xB1, XOR2 = 0x4E, XOR7 = 0x141, ROR8 = 0x128, ROR4 = 0x124;
__device__ __forceinline__ float sum8(float x) {
  x += dpp<XOR1>(x); x += dpp<XOR2>(x); x += dpp<XOR7>(x); return x;
}
__device__ __forceinline__ float fmx(float a, float b) {
  return __builtin_fmaxf(a, b);
}
__device__ __forceinline__ float max8(float x) {
  x = fmx(x, dpp<XOR1>(x)); x = fmx(x, dpp<XOR2>(x)); x = fmx(x, dpp<XOR7>(x)); return x;
}
__device__ __forceinline__ float xrow_sum(float x) {
  auto s = __builtin_amdgcn_permlane16_swap(__float_as_uint(x), __float_as_uint(x), false, false);
  x = __uint_as_float(s[0]) + __uint_as_float(s[1]);
  auto t = __builtin_amdgcn_permlane32_swap(__float_as_uint(x), __float_as_uint(x), false, false);
  return __uint_as_float(t[0]) + __uint_as_float(t[1]);
}
__device__ __forceinline__ float xrow_max(float x) {
  auto s = __builtin_amdgcn_permlane16_swap(__float_as_uint(x), __float_as_uint(x), false, false);
  x = fmx(__uint_as_float(s[0]), __uint_as_float(s[1]));
  auto t = __builtin_amdgcn_permlane32_swap(__float_as_uint(x), __float_as_uint(x), false, false);
  return fmx(__uint_as_float(t[0]), __uint_as_float(t[1]));
}
__device__ __forceinline__ float tsum32(float a, float b) {
  auto t = __builtin_amdgcn_permlane32_swap(__float_as_uint(a), __float_as_uint(b), false, false);
  return __uint_as_float(t[0]) + __uint_as_float(t[1]);
}
__device__ __forceinline__ float tsum16(float a, float b) {
  auto t = __builtin_amdgcn_permlane16_swap(__float_as_uint(a), __float_as_uint(b), false, false);
  return __uint_as_float(t[0]) + __uint_as_float(t[1]);
}
__device__ __forceinline__ float tsum8(float a, float b, bool b3) {
  const float snd = b3 ? a : b, kp = b3 ? b : a;
  return kp + dpp<ROR8>(snd);
}
__device__ __forceinline__ float tsum_hi8(float v0, float v1, float v2, float v3, float v4, float v5, float v6, float v7, bool b3) {
  return tsum8(tsum16(tsum32(v0, v4), tsum32(v2, v6)), tsum16(tsum32(v1, v5), tsum32(v3, v7)), b3);
}
__device__ __forceinline__ float hi_sum(float x) { x += dpp<ROR8>(x); return xrow_sum(x); }
__device__ __forceinline__ float hi_max(float x) { x = fmx(x, dpp<ROR8>(x)); return xrow_max(x); }
__device__ __forceinline__ float wave_sum(float x) { return hi_sum(sum8(x)); }
__device__ __forceinline__ u32 bfbits(float f) { return (u32)__builtin_bit_cast(u16, (__bf16)f); }
__device__ __forceinline__ float bfr(float f) { return __uint_as_float(bfbits(f) << 16); }
__device__ __forceinline__ u32 pack2(float a, float b) {
  bf16x2 p = {(__bf16)a, (__bf16)b};
  return __builtin_bit_cast(u32, p);
}
__device__ __forceinline__ float blo(u32 v) { return __uint_as_float(v << 16); }
__device__ __forceinline__ float bhi(u32 v) { return __uint_as_float(v & 0xffff0000u); }
__device__ __forceinline__ float ex2(float v) { return __builtin_amdgcn_exp2f(v); }
#define BAR() asm volatile("s_waitcnt lgkmcnt(0)\n\ts_barrier" ::: "memory")
#define PIN() __builtin_amdgcn_sched_barrier(0)
#define CBAR() asm volatile("" ::: "memory")
#define STAMP(i)
#define ESTAMP(i)
#define STR_(x) #x
#define STR(x) STR_(x)
#define KEEP(v) asm volatile("" : "+v"(v))
#define RSRC(p, n) __builtin_amdgcn_make_buffer_rsrc((void*)(p), 0, (n), 0x00020000)
#define LDW(p) __hip_atomic_load((p), __ATOMIC_RELAXED, __HIP_MEMORY_SCOPE_WORKGROUP)
#define STW(p, v) __hip_atomic_store((p), (v), __ATOMIC_RELAXED, __HIP_MEMORY_SCOPE_WORKGROUP)

#define WAITVM(n) __builtin_amdgcn_s_waitcnt(0x0F70 | ((n) & 15) | (((n) >> 4) << 14))

#define ST128(v, vo, so) do { __builtin_amdgcn_raw_buffer_store_b128((v), rs_ws, (vo), (so), 16); asm volatile("s_nop 1" :: "v"(v)); } while (0)

constexpr int L_FL = 0;
constexpr int L_ST = 16;
constexpr int L_QS = L_ST + 32;
constexpr int L_XN = L_QS + 100;
constexpr int L_PART = L_XN + 320;
constexpr int L_OT = L_PART + 8 * 192;
constexpr int L_WT = L_OT + 8 * 64;
constexpr int L_HD = L_WT + 64;
constexpr int L_Y = L_HD + 96;
constexpr int L_XR = L_Y + 8 * 64;
constexpr int L_END = L_XR + 320;
constexpr u32 OOB = 0x80000000u;

struct Sched { int hi[NSLOT + 1]; };
constexpr int S3 = QS + 9;
constexpr Sched make_sched() {
  Sched t{};
  int j = 0;
  for (int n = S3; n < NSLOT; ++n) {
    int burst = 0;
    while (j < NL && burst < PB && n - (NL + j) >= PACE) { ++j; ++burst; }
    t.hi[n] = j;
  }
  t.hi[NSLOT] = NL;
  return t;
}
constexpr Sched SCH = make_sched();
static_assert(QS >= NL && S3 <= NSLOT - CL && CL >= 1, "schedule");

__device__ __forceinline__ float score8(const u32x4 k0, const u32x4 k1, const u32x4 k2, const u32x4 (&q)[3]) {
  float a = dot2(k0.x, q[0].x, 0.f), c = dot2(k0.y, q[0].y, 0.f), e = dot2(k0.z, q[0].z, 0.f), g = dot2(k0.w, q[0].w, 0.f);
  a = dot2(k1.x, q[1].x, a); c = dot2(k1.y, q[1].y, c); e = dot2(k1.z, q[1].z, e); g = dot2(k1.w, q[1].w, g);
  a = dot2(k2.x, q[2].x, a); c = dot2(k2.y, q[2].y, c); e = dot2(k2.z, q[2].z, e); g = dot2(k2.w, q[2].w, g);
  return bfr(bfr(sum8((a + c) + (e + g))) * SCALE);
}
__device__ __forceinline__ void pv_step(const u32x4 d, const float pp, f32x2 (&acc)[4]) {
#pragma unroll
  for (int c = 0; c < 4; ++c) {
    acc[c] = __builtin_elementwise_fma(f32x2{blo(d[c]), bhi(d[c])}, f32x2{pp, pp}, acc[c]);
    KEEP(acc[c]);
  }
}

#define PL(a, b, r) "buffer_load_dwordx4 v[" #a ":" #b "], %[" #r "], %[rs], 0 offen sc1\n"
#define PC(n, x, y, z, t, ca, cb, r) \
  "s_waitcnt vmcnt(" #n ")\n" \
  "v_cmp_eq_u32 vcc, v" #y ", %[tag]\n" \
  "v_cmp_eq_u32 %[m], v" #t ", %[tag]\n" \
  "s_and_b64 vcc, vcc, %[m]\n" \
  "v_cndmask_b32 %[" #ca "], %[" #ca "], v" #x ", vcc\n" \
  "v_cndmask_b32 %[" #cb "], %[" #cb "], v" #z ", vcc\n" \
  "v_cndmask_b32 %[" #r "], %[" #r "], %[oob], vcc\n"
#define PEND(t, gap) \
  "v_cmp_eq_u32 vcc, %[" #t "], %[oob]\n" \
  "s_cmp_eq_u64 vcc, -1\n" \
  "s_cbranch_scc1 2f\n" \
  "s_sleep " STR(gap) "\n"
#define P3_LD(a0, a3, b0, b3, d0, d3) PL(a0, a3, r0) PL(b0, b3, r1) PL(d0, d3, r2)
#define P3_CK(a0, a1, a2, a3, b0, b1, b2, b3, d0, d1, d2, d3) \
  PC(11, a0, a1, a2, a3, c0, c1, r0) PC(10, b0, b1, b2, b3, c2, c3, r1) PC(9, d0, d1, d2, d3, c4, c5, r2) \
  "v_min3_u32 %[t0], %[r0], %[r1], %[r2]\n" PEND(t0, GAPO) P3_LD(a0, a3, b0, b3, d0, d3)
#define P2_LD(a0, a3, b0, b3) PL(a0, a3, r0) PL(b0, b3, r1)
#define P2_CK(a0, a1, a2, a3, b0, b1, b2, b3) \
  PC(7, a0, a1, a2, a3, c0, c1, r0) PC(6, b0, b1, b2, b3, c2, c3, r1) \
  "v_and_b32 %[t0], %[r0], %[r1]\n" PEND(t0, GAPO) P2_LD(a0, a3, b0, b3)
#define P1_CK(gap, a0, a1, a2, a3) PC(3, a0, a1, a2, a3, c0, c1, r0) PEND(r0, gap) PL(a0, a3, r0)
#define SLP(n) "s_sleep " STR(n) "\n"
#define PLOOP(l) "s_sub_u32 %[cnt], %[cnt], 1\ns_cbranch_scc0 " #l "b\n"

__global__ void __launch_bounds__(512, 2)
fused_decode_kernel(const u16* __restrict__ x, const u16* __restrict__ kc, const u16* __restrict__ vc,
                    const u16* __restrict__ wq, const u16* __restrict__ wo, const u16* __restrict__ w1,
                    const u16* __restrict__ w2, u16* out, u32* __restrict__ ws) {
  __shared__ __attribute__((aligned(16))) float lds[L_END];
  const int tid = threadIdx.x;
  const int lane = tid & 63;
  const int w = __builtin_amdgcn_readfirstlane(tid >> 6);
  const int b = blockIdx.x;
  const int h = b >> 3;
  const int s = b & 7;
  const int l3 = lane >> 3, l7 = lane & 7;
  const bool b3 = lane & 8;
  const int perm8 = 4 * (lane >> 5) + 2 * ((lane >> 4) & 1) + (l3 & 1);

  const auto rs_ws = RSRC(ws, WS_BYTES);
  const u32x4 rsw = {(u32)(size_t)ws, (u32)((size_t)ws >> 32) & 0xffffu, WS_BYTES, 0x00020000u};
  const int sh = SWZ ? (s + h) & 7 : s;
  const size_t kvoff = ((size_t)h * SK + (SK - WLEN) + (IL ? 0 : sh * 512)) * DH;
  const auto rs_k = RSRC(kc + kvoff, (IL ? WLEN : 512) * DH * 2);
  const auto rs_v = RSRC(vc + kvoff, (IL ? WLEN : 512) * DH * 2);
  const int kvo = l3 * 384 + l7 * 16;
  char* const ldb = reinterpret_cast<char*>(lds);
  u32* const fl = reinterpret_cast<u32*>(lds + L_FL);
  u32* const qs = reinterpret_cast<u32*>(lds + L_QS);

#define PART(u) do { *reinterpret_cast<float*>(ldb + a_w + (u) * 256) = \
    tsum_hi8(acc[u][0].x, acc[u][0].y, acc[u][1].x, acc[u][1].y, acc[u][2].x, acc[u][2].y, acc[u][3].x, acc[u][3].y, b3); PIN(); } while (0)
  u32 role = 0xffffffffu;
  u32 a_w = (L_PART + w * 192 + 8 * l7 + perm8) * 4, a_r = (L_PART + (lane < 48 ? lane : 47) * 4) * 4;
  u32 o_voff = lane < 48 ? lane * 16 : OOB;
  f32x2 acc[3][4];
#pragma unroll
  for (int u = 0; u < 3; ++u)
#pragma unroll
    for (int c = 0; c < 4; ++c) acc[u][c] = f32x2{0.f, 0.f};
  u32 tag;
  u32 voob = OOB, vzero = 0u, vone = 1u;
  asm volatile("" : "+s"(out));

  if (w == 0) {
    __builtin_amdgcn_s_setprio(3);
    ESTAMP(8);
    const bool noq = s == 7 && (h & 2);
    const auto rs_x = RSRC(x, D * 2);
    const u32x4 xa = __builtin_amdgcn_raw_buffer_load_b128(rs_x, lane * 20, 0, 0);
    const u32 xb = __builtin_amdgcn_raw_buffer_load_b32(rs_x, lane * 20 + 16, 0, 0);
    const int sec = s < 6 ? 12 * (h >> 1) + 2 * s + (h & 1) : 6 * (h >> 2) + (s == 6 ? (h & 3) : 4 + (h & 1));
    const u32 stp = s < 6 ? 12288u : 3072u;
    const int cg = sec >> 1, rg = sec & 1;
    const auto rs_t = RSRC((s < 6 ? wq : w1) + (size_t)rg * 320 * (stp / 2) + cg * 64, noq ? 0 : 319 * stp + 128);
    u32x4 wv[40];
    const u32 tv = l3 * stp + l7 * 16;
    const u32 E = __builtin_amdgcn_raw_buffer_load_b32(rs_ws, 0, 0, 16);
    ESTAMP(9);
#pragma unroll
    for (int i = 0; i < 40; ++i) { wv[i] = __builtin_amdgcn_raw_buffer_load_b128(rs_t, tv, i * 8 * stp, 2); PIN();
    }
    u32x4 kr[3], vr[3];
#pragma unroll
    for (int i = 0; i < 3; ++i) kr[i] = __builtin_amdgcn_raw_buffer_load_b128(rs_k, kvo + i * 128, IL ? sh * 3072 : 0, 2);
#pragma unroll
    for (int i = 0; i < 3; ++i) vr[i] = __builtin_amdgcn_raw_buffer_load_b128(rs_v, kvo + i * 128, IL ? sh * 3072 : 0, 2);
    if (YAT && b == RB) {
      const u32x4 z = {0u, 0u, 0u, 0u};
#pragma unroll
      for (int i = 0; i < 5; ++i) ST128(z, (lane >> 2) * ASTR + (lane & 3) * 16, WS_A + i * 16 * ASTR);
    }
    if (lane < 48) fl[lane] = 0u;
    PIN();
    ESTAMP(10);
    BAR();
    float xv[10];
    xv[0] = blo(xa.x); xv[1] = bhi(xa.x); xv[2] = blo(xa.y); xv[3] = bhi(xa.y);
    xv[4] = blo(xa.z); xv[5] = bhi(xa.z); xv[6] = blo(xa.w); xv[7] = bhi(xa.w);
    xv[8] = blo(xb); xv[9] = bhi(xb);
    const float sm = ((xv[0] + xv[1]) + (xv[2] + xv[3])) + ((xv[4] + xv[5]) + (xv[6] + xv[7])) + (xv[8] + xv[9]);
    const float mean = wave_sum(sm) * (1.0f / D);
    float sq = 0.f;
#pragma unroll
    for (int e = 0; e < 10; ++e) { const float d = xv[e] - mean; sq += d * d; }
    const float var = wave_sum(sq) * (1.0f / D);
    const float mb = bfr(mean);
    const float den = bfr(__builtin_amdgcn_sqrtf(bfr(bfr(var) + 1e-5f)));
    const float rinv = 1.0f / den;
    u32* xn = reinterpret_cast<u32*>(lds + L_XN);
    if (YAT) {
      u32* xr = reinterpret_cast<u32*>(lds + L_XR) + lane * 5;
      xr[0] = xa.x; xr[1] = xa.y; xr[2] = xa.z; xr[3] = xa.w; xr[4] = xb;
    }
#pragma unroll
    for (int e = 0; e < 10; ++e) {
      const u32 r = lane * 10 + e, rr = r % 320;
      reinterpret_cast<u16*>(xn)[r - rr + (rr % 8) * 40 + rr / 8] = (u16)bfbits(bfr(xv[e] - mb) * rinv);
    }
    u32x4 xs[5];
#pragma unroll
    for (int i = 0; i < 5; ++i) xs[i] = *reinterpret_cast<const u32x4*>(xn + rg * 160 + l3 * 20 + i * 4);
    STAMP(1);
    float a8[8];
#pragma unroll
    for (int e = 0; e < 8; ++e) a8[e] = 0.f;
#pragma unroll
    for (int i = 0; i < 40; ++i) {
      const u32 v = xs[i / 8][(i / 2) % 4];
      const u32 xl = (i & 1) ? v >> 16 : v & 0xffffu;
      const u32 xh = (i & 1) ? v & 0xffff0000u : v << 16;
#pragma unroll
      for (int d = 0; d < 4; ++d) {
        a8[2 * d] = dot2(wv[i][d], xl, a8[2 * d]);
        a8[2 * d + 1] = dot2(wv[i][d], xh, a8[2 * d + 1]);
        KEEP(a8[2 * d]); KEEP(a8[2 * d + 1]);
      }
      PIN();
    }
    tag = E + 1u + (E == 0xffffffffu);
    STAMP(2);
    {
      float e0 = tsum16(tsum32(a8[0], a8[4]), tsum32(a8[2], a8[6]));
      float e1 = tsum16(tsum32(a8[1], a8[5]), tsum32(a8[3], a8[7]));
      e0 += dpp<ROR8>(e0); e1 += dpp<ROR8>(e1);
      const int gs = s < 6 ? sec : 192 + sec;
      const u32x4 un = {__float_as_uint(e0), tag, __float_as_uint(e1), tag};
      const u32 xv_ = (b3 || noq) ? OOB : (4 * l7 + (lane >> 4 & 1) + 2 * (lane >> 5)) * 16;
      ST128(un, xv_, WS_X + gs * 512);
      if (b == RB) {
        asm volatile("s_waitcnt vmcnt(0)" ::: "memory");
        __builtin_amdgcn_raw_buffer_store_b64(u32x2{0u, tag}, rs_ws, lane < NC ? WS_C + lane * CSTR : OOB, 0, 16);
      }
      STAMP(3);
      u32 r[3], c[3][2];
#pragma unroll
      for (int j = 0; j < 3; ++j) { r[j] = (3 * h + j) * 1024 + lane * 16; c[j][0] = c[j][1] = 0u; }
      __builtin_amdgcn_s_sleep(GAPX0);
      int spins = 0;
      while (true) {
        CBAR();
        u32x4 v[3];
#pragma unroll
        for (int j = 0; j < 3; ++j) v[j] = __builtin_amdgcn_raw_buffer_load_b128(rs_ws, r[j], WS_X, 16);
#pragma unroll
        for (int j = 0; j < 3; ++j) {
          const bool ok = v[j].y == tag && v[j].w == tag;
          c[j][0] = ok ? v[j].x : c[j][0]; c[j][1] = ok ? v[j].z : c[j][1]; r[j] = ok ? OOB : r[j];
        }
        if (__all((r[0] & r[1] & r[2]) == OOB) || ++spins >= SPIN_CAP) break;
        __builtin_amdgcn_s_sleep(GAPX);
      }
#pragma unroll
      for (int j = 0; j < 3; ++j) {
        const float t = tsum32(__uint_as_float(c[j][0]), __uint_as_float(c[j][1]));
        reinterpret_cast<u16*>(qs)[j * 64 + 2 * (lane & 31) + (lane >> 5)] = (u16)bfbits(t);
      }
    }
    qs[96] = tag;
    CBAR();
    if (lane == 0) STW(fl, 1u);
    STAMP(4);
    __builtin_amdgcn_s_setprio(0);
    u32x4 q[3];
#pragma unroll
    for (int u = 0; u < 3; ++u) q[u] = *reinterpret_cast<const u32x4*>(qs + u * 32 + l7 * 4);
    const float sco = score8(kr[0], kr[1], kr[2], q);
    const float mw = hi_max(sco);
    const float p = ex2((sco - mw) * LOG2E);
    const float lw = hi_sum(p);
    if (lane == 0) *reinterpret_cast<f32x4*>(lds + L_ST) = f32x4{mw, lw, __uint_as_float(1u), 0.f};
    STAMP(5);
#pragma unroll
    for (int u = 0; u < 3; ++u) { pv_step(vr[u], p, acc[u]); PART(u); }
    {
      int spins = 0;
      while (true) {
        CBAR();
        const u32 t = __builtin_amdgcn_raw_buffer_load_b32(rs_ws, WS_C + 4, (h % NC) * CSTR, 16);
        if (__builtin_amdgcn_readfirstlane(t) == __builtin_amdgcn_readfirstlane(tag) || ++spins >= SPIN_CAP) break;
        __builtin_amdgcn_s_sleep(GAPS);
      }
    }
    f32x4 ml;
    int spins = 0;
    while (true) {
      CBAR();
      ml = *reinterpret_cast<const f32x4*>(lds + L_ST + 4 * l7);
      if (__all(__float_as_uint(ml.z) != 0u) || ++spins >= 100 * SPIN_CAP) break;
      __builtin_amdgcn_s_sleep(4);
    }
    const float M = max8(ml.x);
    const float f = ex2((ml.x - M) * LOG2E);
    const float L = sum8(f * ml.y);
    const u32x4 un = {__float_as_uint(M), tag, __float_as_uint(L), tag};
    ST128(un, lane == 0 ? 0u : OOB, WS_S + b * 16);
    if (lane < 8) lds[L_FL + 8 + lane] = f * __builtin_amdgcn_rcpf(L);
  } else {
    constexpr int GS = IL ? 56 * 3072 : 3072;
    const int soff = IL ? (7 + sh * 7 + w) * 3072 : (8 + (w - 1) * 72) * (DH * 2);
    int so[9];
    so[0] = soff + (ROT == 0 ? 0 : ROT == 1 ? h % 9 : ROT == 5 ? (h & 3) * 2 : h & 7) * GS;
#pragma unroll
    for (int i = 1; i < 9; ++i) { const int t = so[i - 1] + GS; so[i] = ROT && t >= soff + 9 * GS ? soff : t; }
    u32x4 kr[NL], vr[NL];
#define ISSUE(n) do { \
      if ((n) < NL) kr[(n) < NL ? (n) : 0] = __builtin_amdgcn_raw_buffer_load_b128(rs_k, kvo + ((n) % 3) * 128, so[((n) / 3) % 9], 2); \
      else vr[(n) >= NL ? (n) - NL : 0] = __builtin_amdgcn_raw_buffer_load_b128(rs_v, kvo + ((n) % 3) * 128, so[((n) / 3) % 9], 2); \
      PIN(); } while (0)
#define GATE() WAITVM(PACE - 1)
#pragma unroll
    for (int n = 0; n < PACE; ++n) { if (n == 0) ESTAMP(8); ISSUE(n); }
    ESTAMP(9);
    asm volatile("s_barrier" ::: "memory");
    ESTAMP(10);
    KEEP(a_w); KEEP(a_r); KEEP(o_voff);
#pragma unroll
    for (int n = PACE; n < QS; ++n) { GATE(); if (n == PACE) ESTAMP(11); if (n == 2 * PACE) ESTAMP(12); ISSUE(n); }
    STAMP(3);
    {
      int spins = 0;
      while (LDW(fl) == 0u && ++spins < 100 * SPIN_CAP) __builtin_amdgcn_s_sleep(1);
      CBAR();
    }
    STAMP(4);
    u32x4 q[3];
#pragma unroll
    for (int u = 0; u < 3; ++u) q[u] = *reinterpret_cast<const u32x4*>(qs + u * 32 + l7 * 4);
    tag = qs[96];
    float sc[9];
#pragma unroll
    for (int i = 0; i < 9; ++i) {
      sc[i] = score8(kr[3 * i], kr[3 * i + 1], kr[3 * i + 2], q);
      KEEP(sc[i]);
      PIN();
      GATE(); ISSUE(QS + i);
    }
    const float mw = hi_max(fmx(fmx(fmx(sc[0], sc[1]), fmx(sc[2], sc[3])), fmx(fmx(fmx(sc[4], sc[5]), fmx(sc[6], sc[7])), sc[8])));
#pragma unroll
    for (int i = 0; i < 9; ++i) sc[i] = ex2((sc[i] - mw) * LOG2E);
    const float lw = hi_sum((((sc[0] + sc[1]) + (sc[2] + sc[3])) + ((sc[4] + sc[5]) + (sc[6] + sc[7]))) + sc[8]);
    if (lane == 0) *reinterpret_cast<f32x4*>(lds + L_ST + 4 * w) = f32x4{mw, lw, __uint_as_float(1u), 0.f};
    STAMP(5);
#pragma unroll
    for (int n = S3; n <= NSLOT; ++n) {
#pragma unroll
      for (int j = (n == S3 ? 0 : SCH.hi[n - 1]); j < SCH.hi[n]; ++j) {
        pv_step(vr[j], sc[j / 3], acc[j % 3]);
        PIN();
        if (j == NL - 1) STAMP(6);
        if (j >= NL - 3) PART(j % 3);
      }
      if (n < NSLOT) { GATE(); ISSUE(n); }
      if (n == NSLOT - CL) {
        if (lane == 0) __hip_atomic_fetch_add(fl + 1, 65536u, __ATOMIC_RELAXED, __HIP_MEMORY_SCOPE_WORKGROUP);
        PIN();
      }
    }
  }
  u32 old = 0;
  {
    asm volatile("s_mov_b64 exec, 1\n\tds_add_rtn_u32 %0, %1, %2 offset:4\n\ts_mov_b64 exec, -1" : "=&v"(old) : "v"(vzero), "v"(vone) : "memory");
    const f32x4 g0 = *reinterpret_cast<const f32x4*>(lds + L_FL + 8), g1 = *reinterpret_cast<const f32x4*>(lds + L_FL + 12);
    f32x4 o[8];
#pragma unroll
    for (int r = 0; r < 8; ++r) o[r] = *reinterpret_cast<const f32x4*>(ldb + a_r + r * 768);
#pragma unroll
    for (int r = 0; r < 8; ++r) asm volatile("" : "+v"(o[r]), "+v"(old));
    old = __builtin_amdgcn_readfirstlane(old);
    const f32x4 t = ((g0.x * o[0] + g0.y * o[1]) + (g0.z * o[2] + g0.w * o[3])) + ((g1.x * o[4] + g1.y * o[5]) + (g1.z * o[6] + g1.w * o[7]));
    const u32x4 un = {pack2(t.x, t.y), tag, pack2(t.z, t.w), tag};
    ST128(un, (old & 255u) == 7u ? o_voff : OOB, WS_O + b * 768);
    if ((old & 255u) == 7u) STAMP(7);
  }
  if (w) STAMP(1);
  KEEP(voob);
  if (w == 0) {
    int spins = 0;
    while (LDW(fl + 1) >> 16 < CN && ++spins < 100 * SPIN_CAP) __builtin_amdgcn_s_sleep(1);
    CBAR();
    STAMP(6);
    const u32 stag = __builtin_amdgcn_readfirstlane(tag);
    role = 255u;
    if (!SKIP || __builtin_amdgcn_readfirstlane(__builtin_amdgcn_raw_buffer_load_b32(rs_ws, WS_F, 0, 16)) != stag)
      role = __builtin_amdgcn_readfirstlane(__builtin_amdgcn_raw_ptr_buffer_atomic_add_i32(1, rs_ws, lane == 0 ? WS_C : OOB, (h % NC) * CSTR, 0)) * NC + h % NC;
    if (SKIP && role == NROLE - 1u) __builtin_amdgcn_raw_buffer_store_b32(tag, rs_ws, lane == 0 ? WS_F : OOB, 0, 16);
    if (THR && role == THR - 1u) __builtin_amdgcn_raw_buffer_store_b32(tag, rs_ws, lane == 0 ? WS_G : OOB, 0, 16);
    if (role + 1u < THR) {
      spins = 0;
      while (true) {
        __builtin_amdgcn_s_sleep(GAPT);
        CBAR();
        const u32 t = __builtin_amdgcn_raw_buffer_load_b32(rs_ws, WS_G, 0, 16);
        if (__builtin_amdgcn_readfirstlane(t) == stag || ++spins >= SPIN_CAP) break;
      }
    }
    if (lane == 0) STW(fl + 2, role + 1u);
    STAMP(14);
    if (EV && role == 255u) {
      spins = 0;
      while (true) {
        CBAR();
        const u32 t = __builtin_amdgcn_raw_buffer_load_b32(rs_ws, lane < NC ? WS_C + lane * CSTR : OOB, 0, 16);
        if (__all(lane >= NC || t == 256u / NC) || ++spins >= SPIN_CAP) break;
        __builtin_amdgcn_s_sleep(GAPS1);
      }
      __builtin_amdgcn_raw_buffer_store_b32(tag, rs_ws, lane == 0 ? 0u : OOB, 0, 16);
    }
  } else {
    int spins = 0;
    while ((role = __builtin_amdgcn_readfirstlane(LDW(fl + 2)) - 1u) == 0xffffffffu && ++spins < 100 * SPIN_CAP) __builtin_amdgcn_s_sleep(1);
  }
  u32 t0, cnt = SPIN_CAP;
  u64 m;
  if (role < NTILE) {
    u32 tg, tc;
    asm("s_mul_i32 %0, %2, 205\n\ts_lshr_b32 %0, %0, 11\n\ts_mul_i32 %1, %0, 10\n\ts_sub_u32 %1, %2, %1"
        : "=&s"(tg), "=&s"(tc) : "s"(role) : "scc");
    const u32 c0 = (u32)((tg < 8u ? tab_pack(0, true) : tab_pack(8, true)) >> (8u * (tg & 7u))) & 255u;
    const u32 nc = (u32)((tg < 8u ? tab_pack(0, false) : tab_pack(8, false)) >> (8u * (tg & 7u))) & 255u;
    const u32 k = KARR ? old & 7u : w;
    const u32 nv = (nc + 7u - k) >> 3, ck = c0 + k, h0 = c0 / 6u;
    const auto rs_wo = RSRC(wo + (size_t)(ck * 32u * D + 64u * tc), 2 * 327680 + 31 * 1280 + 128);
    const auto rs_w2 = RSRC(w2 + (size_t)(ck * 8u * D + 64u * tc), 2 * 81920 + 7 * 1280 + 128);
    u32x4 wov[12], w2v[3], sv, hv[3];
    u32 so = 0u, ho[3] = {0u, 0u, 0u};
#define WISSUE(j) do { \
      w2v[j] = __builtin_amdgcn_raw_buffer_load_b128(rs_w2, l3 * 1280 + l7 * 16 + (j) * 81920, 0, 2); \
      _Pragma("unroll") for (int i = 0; i < 4; ++i) \
        wov[4 * (j) + i] = __builtin_amdgcn_raw_buffer_load_b128(rs_wo, l3 * 5120 + l7 * 16 + (j) * 327680 + i * 1280, 0, 2); \
    } while (0)
    if (w == 0) {
      so = 6u * (h0 + l3) < c0 + nc ? lane * 16 : OOB;
#pragma unroll
      for (int j = 0; j < 3; ++j) {
        const u32 g = 32 * j + (lane & 31), gp = 4u * c0 + g;
        ho[j] = ((gp >> 5) * 1024u + (lane >> 5) * 512 + (gp & 31u) * 16u) | (g < 4u * nc ? 0u : OOB);
      }
      KEEP(so); KEEP(ho[0]); KEEP(ho[1]); KEEP(ho[2]);
      asm volatile("buffer_load_dwordx4 %0, %4, %8, %9 offen sc1\n\tbuffer_load_dwordx4 %1, %5, %8, %10 offen sc1\n\t"
                   "buffer_load_dwordx4 %2, %6, %8, %10 offen sc1\n\tbuffer_load_dwordx4 %3, %7, %8, %10 offen sc1"
                   : "=&v"(sv), "=&v"(hv[0]), "=&v"(hv[1]), "=&v"(hv[2])
                   : "v"(so), "v"(ho[0]), "v"(ho[1]), "v"(ho[2]), "s"(rsw), "s"(WS_S + h0 * 128u), "s"(WS_X + 96u * 1024u) : "memory");
    }
    if (nv > 0u) {
      if (nv > 1u) {
        if (nv > 2u) WISSUE(2);
        WISSUE(1);
      }
      WISSUE(0);
    }
    PIN();
    if (w == 0) {
      asm volatile("s_waitcnt vmcnt(15)\n\ts_cmp_gt_u32 %4, 2\n\ts_cbranch_scc1 1f\n\t"
                   "s_waitcnt vmcnt(10)\n\ts_cmp_gt_u32 %4, 1\n\ts_cbranch_scc1 1f\n\t"
                   "s_waitcnt vmcnt(5)\n\ts_cmp_gt_u32 %4, 0\n\ts_cbranch_scc1 1f\n\t"
                   "s_waitcnt vmcnt(0)\n1:"
                   : "+v"(sv), "+v"(hv[0]), "+v"(hv[1]), "+v"(hv[2]) : "s"(nv) : "scc", "memory");
#define HBAD() (((ho[0] | ((hv[0].y ^ tag) | (hv[0].w ^ tag) ? 0u : OOB)) & (ho[1] | ((hv[1].y ^ tag) | (hv[1].w ^ tag) ? 0u : OOB)) & \
                 (ho[2] | ((hv[2].y ^ tag) | (hv[2].w ^ tag) ? 0u : OOB))) < OOB)
      if (__builtin_expect(__any(HBAD()), 0)) {
        int spins = 0;
        do {
          __builtin_amdgcn_s_sleep(GAPS);
          CBAR();
          _Pragma("unroll") for (int j = 0; j < 3; ++j) hv[j] = __builtin_amdgcn_raw_buffer_load_b128(rs_ws, ho[j], WS_X + 96u * 1024u, 16);
        } while (__any(HBAD()) && ++spins < SPIN_CAP);
      }
      u32 r0 = (so >= OOB || (sv.y == tag && sv.w == tag)) ? OOB : so + WS_S + h0 * 128u;
      u32x16 RS;
      asm volatile(
"v_cmp_eq_u32 vcc, %[r0], %[oob]\n"
"s_cmp_eq_u64 vcc, -1\n"
"s_cbranch_scc1 2f\n"
PL(232, 235, r0) SLP(GAPS0)
PL(236, 239, r0) SLP(GAPS0)
PL(240, 243, r0) SLP(GAPS0)
PL(244, 247, r0)
"1:\n"
P1_CK(GAPS1, 232, 233, 234, 235)
P1_CK(GAPS1, 236, 237, 238, 239)
P1_CK(GAPS1, 240, 241, 242, 243)
P1_CK(GAPS1, 244, 245, 246, 247)
PLOOP(1)
"2:\n"
: [c0] "+v"(sv.x), [c1] "+v"(sv.z), [r0] "+v"(r0), [cnt] "+s"(cnt), [m] "=&s"(m), "=&{v[232:247]}"(RS)
: [rs] "s"(rsw), [tag] "v"(tag), [oob] "v"(voob)
: "vcc", "scc", "memory");
      const float mp = __uint_as_float(sv.x);
      const float f = ex2((mp - max8(mp)) * LOG2E) * __uint_as_float(sv.z);
      lds[L_WT + lane] = f * __builtin_amdgcn_rcpf(sum8(f));
#pragma unroll
      for (int j = 0; j < 3; ++j)
        reinterpret_cast<u16*>(lds + L_HD)[64 * j + 2 * (lane & 31) + (lane >> 5)] =
            (u16)bfbits(fmaxf(tsum32(__uint_as_float(hv[j].x), __uint_as_float(hv[j].z)), 0.f));
      CBAR();
      if (lane == 0) STW(fl + 3, 1u);
      asm volatile("s_waitcnt vmcnt(0)" :: "{v[232:247]}"(RS) : "memory");
      cnt = SPIN_CAP;
    } else {
      int spins = 0;
      while (LDW(fl + 3) == 0u && ++spins < 100 * SPIN_CAP) __builtin_amdgcn_s_sleep(2);
    }
    CBAR();
    float wt[3], y[8];
    u32 hid[3], rr[3], wp[6][8];
#pragma unroll
    for (int j = 0; j < 3; ++j) {
      const u32 c = ck + 8 * j, hh = c / 6u;
      wt[j] = lds[L_WT + ((hh - h0) & 7u) * 8u + l3];
      hid[j] = reinterpret_cast<const u16*>(lds + L_HD)[(k + 8 * j) * 8u + l3];
      rr[j] = WS_O + (hh * 8u + l3) * 768u + (c - 6u * hh) * 128u + l7 * 16;
      KEEP(wt[j]); KEEP(rr[j]);
    }
#pragma unroll
    for (int e = 0; e < 8; ++e) y[e] = 0.f;
    u32 a_ow = (L_OT + w * 64) * 4 + l7 * 32 + (l3 >> 1) * 2, a_or = (L_OT + w * 64) * 4 + l3 * 32;
    u32 a_yw = (L_Y + k * 64 + 8 * l7 + perm8) * 4, a_yr = (L_Y + lane) * 4;
    u32 yv = WS_Y + (8 * tc + l3) * 1024 + tg * 64 + l7 * 8;
    KEEP(a_ow); KEEP(a_or); KEEP(a_yw); KEEP(a_yr); KEEP(yv);
    u32 av = WS_A + (8 * tc + l3) * ASTR + l7 * 8, ov = (64 * tc + lane) * 2, ev = (tc == 0 && lane == 0) ? 0u : OOB;
    float xo = blo(reinterpret_cast<const u16*>(lds + L_XR)[64 * tc + lane]);
    float thr = __builtin_fabsf(xo) < 6.103515625e-05f ? 6.103515625e-05f : 0.f;
    if (YAT) { KEEP(av); KEEP(ov); KEEP(xo); KEEP(thr); if (!EV) KEEP(ev); }
    PIN();
#define WPREP(j) do { \
      _Pragma("unroll") for (int d = 0; d < 4; ++d) { \
        y[2 * d] = dot2(w2v[j][d], hid[j], y[2 * d]); \
        y[2 * d + 1] = dot2(w2v[j][d], hid[j] << 16, y[2 * d + 1]); \
        KEEP(y[2 * d]); KEEP(y[2 * d + 1]); \
      } \
      _Pragma("unroll") for (int i = 2 * (j); i < 2 * (j) + 2; ++i) { \
        _Pragma("unroll") for (int d = 0; d < 4; ++d) { \
          wp[i][2 * d] = __builtin_amdgcn_perm(wov[2 * i + 1][d], wov[2 * i][d], 0x05040100u); \
          wp[i][2 * d + 1] = __builtin_amdgcn_perm(wov[2 * i + 1][d], wov[2 * i][d], 0x07060302u); \
          KEEP(wp[i][2 * d]); KEEP(wp[i][2 * d + 1]); \
        } \
        PIN(); \
      } } while (0)
#define ORED(j) do { \
      float u = tsum16(tsum32(wt[j] * blo(c[2 * (j)]), wt[j] * blo(c[2 * (j) + 1])), tsum32(wt[j] * bhi(c[2 * (j)]), wt[j] * bhi(c[2 * (j) + 1]))); \
      u += dpp<ROR8>(u); \
      *reinterpret_cast<u16*>(ldb + a_ow + (j) * 8) = (u16)bfbits(u); } while (0)
#define YDOT(j, o0, o1) do { \
      _Pragma("unroll") for (int e = 0; e < 8; ++e) y[e] = dot2(wp[2 * (j)][e], o0, y[e]); \
      _Pragma("unroll") for (int e = 0; e < 8; ++e) y[e] = dot2(wp[2 * (j) + 1][e], o1, y[e]); } while (0)
    u32x16 R0, R1, R2;
    if (nv > 0u) {
      if (nv > 1u) {
        if (nv > 2u) WPREP(2);
        WPREP(1);
      }
      WPREP(0);
    }
    WAITVM(0);
    u32 nw = nv;
    asm volatile("" : "+s"(nw));
    if (nw > 0u) {
      if (w) STAMP(14);
      {
        int spins = 0;
        while ((LDW(fl + 1) & 255u) != 8u && ++spins < 100 * SPIN_CAP) __builtin_amdgcn_s_sleep(1);
        CBAR();
      }
      if (w) STAMP(2);
      u32 c[6] = {0, 0, 0, 0, 0, 0};
      asm volatile(
"s_cmp_lt_u32 %[nv], 2\n"
"s_cbranch_scc1 3f\n"
"s_cmp_lt_u32 %[nv], 3\n"
"s_cbranch_scc1 4f\n"
P3_LD(200, 203, 204, 207, 208, 211) SLP(GAPO0)
P3_LD(212, 215, 216, 219, 220, 223) SLP(GAPO0)
P3_LD(224, 227, 228, 231, 232, 235) SLP(GAPO0)
P3_LD(236, 239, 240, 243, 244, 247)
"1:\n"
P3_CK(200, 201, 202, 203, 204, 205, 206, 207, 208, 209, 210, 211)
P3_CK(212, 213, 214, 215, 216, 217, 218, 219, 220, 221, 222, 223)
P3_CK(224, 225, 226, 227, 228, 229, 230, 231, 232, 233, 234, 235)
P3_CK(236, 237, 238, 239, 240, 241, 242, 243, 244, 245, 246, 247)
PLOOP(1)
"s_branch 2f\n"
"3:\n"
PL(232, 235, r0) SLP(GAPO0)
PL(236, 239, r0) SLP(GAPO0)
PL(240, 243, r0) SLP(GAPO0)
PL(244, 247, r0)
"5:\n"
P1_CK(GAPO, 232, 233, 234, 235)
P1_CK(GAPO, 236, 237, 238, 239)
P1_CK(GAPO, 240, 241, 242, 243)
P1_CK(GAPO, 244, 245, 246, 247)
PLOOP(5)
"s_branch 2f\n"
"4:\n"
P2_LD(216, 219, 220, 223) SLP(GAPO0)
P2_LD(224, 227, 228, 231) SLP(GAPO0)
P2_LD(232, 235, 236, 239) SLP(GAPO0)
P2_LD(240, 243, 244, 247)
"6:\n"
P2_CK(216, 217, 218, 219, 220, 221, 222, 223)
P2_CK(224, 225, 226, 227, 228, 229, 230, 231)
P2_CK(232, 233, 234, 235, 236, 237, 238, 239)
P2_CK(240, 241, 242, 243, 244, 245, 246, 247)
PLOOP(6)
"2:\n"
: [c0] "+v"(c[0]), [c1] "+v"(c[1]), [c2] "+v"(c[2]), [c3] "+v"(c[3]), [c4] "+v"(c[4]), [c5] "+v"(c[5]),
  [r0] "+v"(rr[0]), [r1] "+v"(rr[1]), [r2] "+v"(rr[2]),
  [t0] "=&v"(t0), [cnt] "+s"(cnt), [m] "=&s"(m), "=&{v[200:215]}"(R0), "=&{v[216:231]}"(R1), "=&{v[232:247]}"(R2)
: [rs] "s"(rsw), [tag] "v"(tag), [oob] "v"(voob), [nv] "s"(nv)
: "vcc", "scc", "memory");
      STAMP(8);
      if (nv == 1u) {
        ORED(0);
        CBAR();
        const u32x2 ov = *reinterpret_cast<const u32x2*>(ldb + a_or);
        YDOT(0, ov[0], ov[1]);
      } else if (MAXN <= 16 || nv == 2u) {
        ORED(0); ORED(1);
        CBAR();
        const u32x4 ov = *reinterpret_cast<const u32x4*>(ldb + a_or);
        YDOT(0, ov[0], ov[1]);
        YDOT(1, ov[2], ov[3]);
      } else {
        ORED(0); ORED(1); ORED(2);
        CBAR();
        const u32x4 ov = *reinterpret_cast<const u32x4*>(ldb + a_or);
        const u32x2 ow = *reinterpret_cast<const u32x2*>(ldb + a_or + 16);
        YDOT(0, ov[0], ov[1]);
        YDOT(1, ov[2], ov[3]);
        YDOT(2, ow[0], ow[1]);
      }
    }
    {
      *reinterpret_cast<float*>(ldb + a_yw) = tsum_hi8(y[0], y[1], y[2], y[3], y[4], y[5], y[6], y[7], b3);
      BAR();
      STAMP(9);
      if (w < (YAT ? 2 : 1)) {
        float e[8];
#pragma unroll
        for (int r = 0; r < 8; ++r) e[r] = *reinterpret_cast<const float*>(ldb + a_yr + r * 256);
        const float es = ((e[0] + e[1]) + (e[2] + e[3])) + ((e[4] + e[5]) + (e[6] + e[7]));
        if (YAT && w == 0) {
          const bool ok = __builtin_fabsf(es) < 131000.f;
          const u32x2 fx = __builtin_bit_cast(u32x2, (double)es + 1179648.0);
          u32x2 ol = {ok ? fx.x : 0u, ok ? fx.y + ((1u << 22) - 0x41300000u) : (1u << 22) + (1u << 27)};
          u64 mine = __builtin_bit_cast(u64, ol);
          asm volatile("buffer_atomic_add_x2 %0, %1, %2, 0 offen sc0" : "+v"(ol) : "v"(av), "s"(rsw) : "memory");
          STAMP(10);
          mine += 0x80000000ull;
          KEEP(mine);
          asm volatile("s_waitcnt vmcnt(0)" : "+v"(ol) :: "memory");
          STAMP(11);
          const u64 tt = __builtin_bit_cast(u64, ol) + mine;
          const u32 th = (u32)(tt >> 32);
          const bool last = ((th >> 22) & 31u) == NP;
          float r = xo + __builtin_fmaf((float)(int)((u32)tt ^ 0x80000000u), 2.3283064365386963e-10f, (float)(int)((th & 0x3fffffu) - NP * 131072u));
          if (__builtin_expect(__any(last && ((th >> 27) != 0u || __builtin_fabsf(r) < thr)), 0)) {
            float p[NP];
            int spins = 0;
            bool got;
            do {
              CBAR();
              got = true;
#pragma unroll
              for (int g = 0; g < (int)NP; ++g) {
                const u32x2 v = __builtin_amdgcn_raw_buffer_load_b64(rs_ws, last ? yv - tg * 64 + g * 64 : OOB, 0, 16);
                p[g] = __uint_as_float(v.x);
                got = got && v.y == tag;
              }
            } while (__any(last && !got) && ++spins < SPIN_CAP);
            float a = p[0];
#pragma unroll
            for (int g = 1; g < (int)NP; ++g) a += p[g];
            if ((th >> 27) != 0u || __builtin_fabsf(r) < thr) r = xo + a;
          }
          const auto rs_out = RSRC(out, D * 2);
          __builtin_amdgcn_raw_buffer_store_b16((u16)bfbits(r), rs_out, last ? ov : OOB, 0, 0);
          if (!EV) __builtin_amdgcn_raw_buffer_store_b32(tag, rs_ws, last ? ev : OOB, 0, 16);
        } else {
          const u32x2 un = {__float_as_uint(es), tag};
          __builtin_amdgcn_raw_buffer_store_b64(un, rs_ws, yv, 0, 16);
          if (!YAT) STAMP(10);
        }
      }
      asm volatile("s_waitcnt vmcnt(0)" :: "{v[200:215]}"(R0), "{v[216:231]}"(R1), "{v[232:247]}"(R2) : "memory");
    }
  } else if (!YAT && role < NROLE) {
    const u32 ri = (role - NTILE) * 8u + w;
    const u32 rcol = 8 * ri + 2 * (lane & 3) + (lane >> 5);
    u32 xo = x[rcol];
    u32 ov = (lane & 28) ? OOB : rcol * 2;
    u32 r0 = lane < 4 * NP ? WS_Y + ri * 1024 + lane * 16 : OOB;
    u32 ev = (ri == 0 && lane == 0) ? 0u : OOB;
    const auto rs_out = RSRC(out, D * 2);
    KEEP(xo); KEEP(ov); KEEP(ev);
    {
      u32x16 R0;
      u32 c[2] = {0, 0};
      asm volatile(
PL(232, 235, r0) "s_sleep " STR(GAPY0) "\n"
PL(236, 239, r0) "s_sleep " STR(GAPY0) "\n"
PL(240, 243, r0) "s_sleep " STR(GAPY0) "\n"
PL(244, 247, r0)
"1:\n"
P1_CK(GAPY, 232, 233, 234, 235)
P1_CK(GAPY, 236, 237, 238, 239)
P1_CK(GAPY, 240, 241, 242, 243)
P1_CK(GAPY, 244, 245, 246, 247)
"s_sub_u32 %[cnt], %[cnt], 1\n"
"s_cbranch_scc0 1b\n"
"2:\n"
: [c0] "+v"(c[0]), [c1] "+v"(c[1]), [r0] "+v"(r0), [cnt] "+s"(cnt), [m] "=&s"(m), "=&{v[232:247]}"(R0)
: [rs] "s"(rsw), [tag] "v"(tag), [oob] "v"(voob)
: "vcc", "scc", "memory");
      STAMP(11);
      float v = tsum32(__uint_as_float(c[0]), __uint_as_float(c[1]));
      v = tsum16(v, v);
      v += dpp<ROR8>(v);
      v += dpp<ROR4>(v);
      __builtin_amdgcn_raw_buffer_store_b16((u16)bfbits(v + blo(xo)), rs_out, ov, 0, 0);
      __builtin_amdgcn_raw_buffer_store_b32(tag, rs_ws, ev, 0, 16);
      asm volatile("s_waitcnt vmcnt(0)" :: "{v[232:247]}"(R0) : "memory");
    }
  }
}

extern "C" void kernel_launch(void* const* d_in, const int* in_sizes, int n_in,
                              void* d_out, int out_size, void* d_ws, size_t ws_size,
                              hipStream_t stream) {
  if (ws_size < WS_BYTES) return;
  fused_decode_kernel<<<256, 512, 0, stream>>>(
      (const u16*)d_in[0], (const u16*)d_in[1], (const u16*)d_in[2], (const u16*)d_in[3],
      (const u16*)d_in[4], (const u16*)d_in[6], (const u16*)d_in[7], (u16*)d_out, (u32*)d_ws);
}
```

```cpp
#include <hip/hip_runtime.h>
#include <hip/hip_bf16.h>
#include <stdint.h>

#define PACE 6
#define CL 6
#define KARR 1
#define CN 7
#define TAB 16, 16, 15, 15, 14, 13, 13, 12, 12, 11, 11, 10, 9, 9, 8, 8
#define THR 0
#define GAPT 8
#define SKIP 0
#define NC 8
#define CSTR 256
#define SWZ 1
#define IL 1
#define ROT 0
#define EV 1
#define GAPS 16
#define GAPS0 8
#define GAPS1 4
#define GAPX0 4
#define GAPX 4
#define GAPO0 2
#define GAPO 1
#define GAPY0 5
#define GAPY 2
#define YAT 1
#define ASTR 64
#define RB 23
#define QS 33
#define PB 2

typedef unsigned int u32;
typedef unsigned short u16;
typedef unsigned long long u64;
using u32x2 = __attribute__((ext_vector_type(2))) unsigned;
using u32x4 = __attribute__((ext_vector_type(4))) unsigned;
using u32x16 = __attribute__((ext_vector_type(16))) unsigned;
using f32x2 = __attribute__((ext_vector_type(2))) float;
using f32x4 = __attribute__((ext_vector_type(4))) float;
using bf16x2 = __attribute__((ext_vector_type(2))) __bf16;

constexpr int D = 640, DH = 192, SK = 32768, WLEN = 4096;
constexpr int NL = 27;
constexpr int NSLOT = 2 * NL;
constexpr u32 TABN[] = {TAB};
constexpr u32 NP = sizeof(TABN) / sizeof(u32);
constexpr u32 NTILE = 10 * NP, NROLE = NTILE + (YAT ? 0 : 10);
static_assert(NP <= 16, "one load per reducer poll");
constexpr u64 tab_pack(int lo, bool first) {
  u64 r = 0;
  u32 c = 0;
  for (int g = 0; g < (int)NP; ++g) {
    if (g >= lo && g < lo + 8) r |= (u64)(first ? c : TABN[g]) << (8 * (g - lo));
    c += TABN[g];
  }
  return r;
}
constexpr bool tab_ok() {
  u32 c = 0;
  for (int g = 0; g < (int)NP; ++g) { if (TABN[g] < 1 || TABN[g] > 24) return false; c += TABN[g]; }
  return c == 192;
}
static_assert(tab_ok(), "tile table");
constexpr u32 tab_max() {
  u32 c = 0;
  for (int g = 0; g < (int)NP; ++g) c = TABN[g] > c ? TABN[g] : c;
  return c;
}
constexpr u32 MAXN = tab_max();
constexpr u32 WS_F = 2560;
constexpr u32 WS_G = 3072;
constexpr u32 WS_S = 4096;
constexpr u32 WS_O = WS_S + 32u * 128u;
constexpr u32 WS_Y = WS_O + 256u * 768u;
constexpr u32 WS_X = WS_Y + 80u * 1024u;
constexpr u32 WS_C = WS_X + 240u * 512u;
constexpr u32 WS_A = WS_C + NC * CSTR;
constexpr u32 WS_BYTES = WS_A + 80u * ASTR;
constexpr int SPIN_CAP = 200000;
constexpr float LOG2E = 1.4426950408889634f;
constexpr float SCALE = 0.07216878364870322f;

__device__ __forceinline__ float dot2(u32 a, u32 b, float acc) {
  return __builtin_amdgcn_fdot2_f32_bf16(__builtin_bit_cast(bf16x2, a), __builtin_bit_cast(bf16x2, b), acc, false);
}
template <int CTRL> __device__ __forceinline__ float dpp(float x) {
  return __builtin_bit_cast(float, __builtin_amdgcn_mov_dpp(__builtin_bit_cast(int, x), CTRL, 0xf, 0xf, true));
}
constexpr int XOR1 = 0xB1, XOR2 = 0x4E, XOR7 = 0x141, ROR8 = 0x128, ROR4 = 0x124;
__device__ __forceinline__ float sum8(float x) {
  x += dpp<XOR1>(x); x += dpp<XOR2>(x); x += dpp<XOR7>(x); return x;
}
__device__ __forceinline__ float fmx(float a, float b) {
  return __builtin_fmaxf(a, b);
}
__device__ __forceinline__ float max8(float x) {
  x = fmx(x, dpp<XOR1>(x)); x = fmx(x, dpp<XOR2>(x)); x = fmx(x, dpp<XOR7>(x)); return x;
}
__device__ __forceinline__ float xrow_sum(float x) {
  auto s = __builtin_amdgcn_permlane16_swap(__float_as_uint(x), __float_as_uint(x), false, false);
  x = __uint_as_float(s[0]) + __uint_as_float(s[1]);
  auto t = __builtin_amdgcn_permlane32_swap(__float_as_uint(x), __float_as_uint(x), false, false);
  return __uint_as_float(t[0]) + __uint_as_float(t[1]);
}
__device__ __forceinline__ float xrow_max(float x) {
  auto s = __builtin_amdgcn_permlane16_swap(__float_as_uint(x), __float_as_uint(x), false, false);
  x = fmx(__uint_as_float(s[0]), __uint_as_float(s[1]));
  auto t = __builtin_amdgcn_permlane32_swap(__float_as_uint(x), __float_as_uint(x), false, false);
  return fmx(__uint_as_float(t[0]), __uint_as_float(t[1]));
}
__device__ __forceinline__ float tsum32(float a, float b) {
  auto t = __builtin_amdgcn_permlane32_swap(__float_as_uint(a), __float_as_uint(b), false, false);
  return __uint_as_float(t[0]) + __uint_as_float(t[1]);
}
__device__ __forceinline__ float tsum16(float a, float b) {
  auto t = __builtin_amdgcn_permlane16_swap(__float_as_uint(a), __float_as_uint(b), false, false);
  return __uint_as_float(t[0]) + __uint_as_float(t[1]);
}
__device__ __forceinline__ float tsum8(float a, float b, bool b3) {
  const float snd = b3 ? a : b, kp = b3 ? b : a;
  return kp + dpp<ROR8>(snd);
}
__device__ __forceinline__ float tsum_hi8(float v0, float v1, float v2, float v3, float v4, float v5, float v6, float v7, bool b3) {
  return tsum8(tsum16(tsum32(v0, v4), tsum32(v2, v6)), tsum16(tsum32(v1, v5), tsum32(v3, v7)), b3);
}
__device__ __forceinline__ float hi_sum(float x) { x += dpp<ROR8>(x); return xrow_sum(x); }
__device__ __forceinline__ float hi_max(float x) { x = fmx(x, dpp<ROR8>(x)); return xrow_max(x); }
__device__ __forceinline__ float wave_sum(float x) { return hi_sum(sum8(x)); }
__device__ __forceinline__ u32 bfbits(float f) { return (u32)__builtin_bit_cast(u16, (__bf16)f); }
__device__ __forceinline__ float bfr(float f) { return __uint_as_float(bfbits(f) << 16); }
__device__ __forceinline__ u32 pack2(float a, float b) {
  bf16x2 p = {(__bf16)a, (__bf16)b};
  return __builtin_bit_cast(u32, p);
}
__device__ __forceinline__ float blo(u32 v) { return __uint_as_float(v << 16); }
__device__ __forceinline__ float bhi(u32 v) { return __uint_as_float(v & 0xffff0000u); }
__device__ __forceinline__ float ex2(float v) { return __builtin_amdgcn_exp2f(v); }
#define BAR() asm volatile("s_waitcnt lgkmcnt(0)\n\ts_barrier" ::: "memory")
#define PIN() __builtin_amdgcn_sched_barrier(0)
#define CBAR() asm volatile("" ::: "memory")
#define STAMP(i)
#define ESTAMP(i)
#define STR_(x) #x
#define STR(x) STR_(x)
#define KEEP(v) asm volatile("" : "+v"(v))
#define RSRC(p, n) __builtin_amdgcn_make_buffer_rsrc((void*)(p), 0, (n), 0x00020000)
#define LDW(p) __hip_atomic_load((p), __ATOMIC_RELAXED, __HIP_MEMORY_SCOPE_WORKGROUP)
#define STW(p, v) __hip_atomic_store((p), (v), __ATOMIC_RELAXED, __HIP_MEMORY_SCOPE_WORKGROUP)

#define WAITVM(n) __builtin_amdgcn_s_waitcnt(0x0F70 | ((n) & 15) | (((n) >> 4) << 14))

#define ST128(v, vo, so) do { __builtin_amdgcn_raw_buffer_store_b128((v), rs_ws, (vo), (so), 16); asm volatile("s_nop 1" :: "v"(v)); } while (0)

constexpr int L_FL = 0;
constexpr int L_ST = 16;
constexpr int L_QS = L_ST + 32;
constexpr int L_XN = L_QS + 100;
constexpr int L_PART = L_XN + 320;
constexpr int L_OT = L_PART + 8 * 192;
constexpr int L_WT = L_OT + 8 * 64;
constexpr int L_HD = L_WT + 64;
constexpr int L_Y = L_HD + 96;
constexpr int L_XR = L_Y + 8 * 64;
constexpr int L_END = L_XR + 320;
constexpr u32 OOB = 0x80000000u;

struct Sched { int hi[NSLOT + 1]; };
constexpr int S3 = QS + 9;
constexpr Sched make_sched() {
  Sched t{};
  int j = 0;
  for (int n = S3; n < NSLOT; ++n) {
    int burst = 0;
    while (j < NL && burst < PB && n - (NL + j) >= PACE) { ++j; ++burst; }
    t.hi[n] = j;
  }
  t.hi[NSLOT] = NL;
  return t;
}
constexpr Sched SCH = make_sched();
static_assert(QS >= NL && S3 <= NSLOT - CL && CL >= 1, "schedule");

__device__ __forceinline__ float score8(const u32x4 k0, const u32x4 k1, const u32x4 k2, const u32x4 (&q)[3]) {
  float a = dot2(k0.x, q[0].x, 0.f), c = dot2(k0.y, q[0].y, 0.f), e = dot2(k0.z, q[0].z, 0.f), g = dot2(k0.w, q[0].w, 0.f);
  a = dot2(k1.x, q[1].x, a); c = dot2(k1.y, q[1].y, c); e = dot2(k1.z, q[1].z, e); g = dot2(k1.w, q[1].w, g);
  a = dot2(k2.x, q[2].x, a); c = dot2(k2.y, q[2].y, c); e = dot2(k2.z, q[2].z, e); g = dot2(k2.w, q[2].w, g);
  return bfr(bfr(sum8((a + c) + (e + g))) * SCALE);
}
__device__ __forceinline__ void pv_step(const u32x4 d, const float pp, f32x2 (&acc)[4]) {
#pragma unroll
  for (int c = 0; c < 4; ++c) {
    acc[c] = __builtin_elementwise_fma(f32x2{blo(d[c]), bhi(d[c])}, f32x2{pp, pp}, acc[c]);
    KEEP(acc[c]);
  }
}

#define PL(a, b, r) "buffer_load_dwordx4 v[" #a ":" #b "], %[" #r "], %[rs], 0 offen sc1\n"
#define PC(n, x, y, z, t, ca, cb, r) \
  "s_waitcnt vmcnt(" #n ")\n" \
  "v_cmp_eq_u32 vcc, v" #y ", %[tag]\n" \
  "v_cmp_eq_u32 %[m], v" #t ", %[tag]\n" \
  "s_and_b64 vcc, vcc, %[m]\n" \
  "v_cndmask_b32 %[" #ca "], %[" #ca "], v" #x ", vcc\n" \
  "v_cndmask_b32 %[" #cb "], %[" #cb "], v" #z ", vcc\n" \
  "v_cndmask_b32 %[" #r "], %[" #r "], %[oob], vcc\n"
#define PEND(t, gap) \
  "v_cmp_eq_u32 vcc, %[" #t "], %[oob]\n" \
  "s_cmp_eq_u64 vcc, -1\n" \
  "s_cbranch_scc1 2f\n" \
  "s_sleep " STR(gap) "\n"
#define P3_LD(a0, a3, b0, b3, d0, d3) PL(a0, a3, r0) PL(b0, b3, r1) PL(d0, d3, r2)
#define P3_CK(a0, a1, a2, a3, b0, b1, b2, b3, d0, d1, d2, d3) \
  PC(11, a0, a1, a2, a3, c0, c1, r0) PC(10, b0, b1, b2, b3, c2, c3, r1) PC(9, d0, d1, d2, d3, c4, c5, r2) \
  "v_min3_u32 %[t0], %[r0], %[r1], %[r2]\n" PEND(t0, GAPO) P3_LD(a0, a3, b0, b3, d0, d3)
#define P2_LD(a0, a3, b0, b3) PL(a0, a3, r0) PL(b0, b3, r1)
#define P2_CK(a0, a1, a2, a3, b0, b1, b2, b3) \
  PC(7, a0, a1, a2, a3, c0, c1, r0) PC(6, b0, b1, b2, b3, c2, c3, r1) \
  "v_and_b32 %[t0], %[r0], %[r1]\n" PEND(t0, GAPO) P2_LD(a0, a3, b0, b3)
#define P1_CK(gap, a0, a1, a2, a3) PC(3, a0, a1, a2, a3, c0, c1, r0) PEND(r0, gap) PL(a0, a3, r0)
#define SLP(n) "s_sleep " STR(n) "\n"
#define PLOOP(l) "s_sub_u32 %[cnt], %[cnt], 1\ns_cbranch_scc0 " #l "b\n"

__global__ void __launch_bounds__(512, 2)
fused_decode_kernel(const u16* __restrict__ x, const u16* __restrict__ kc, const u16* __restrict__ vc,
                    const u16* __restrict__ wq, const u16* __restrict__ wo, const u16* __restrict__ w1,
                    const u16* __restrict__ w2, u16* out, u32* __restrict__ ws) {
  __shared__ __attribute__((aligned(16))) float lds[L_END];
  const int tid = threadIdx.x;
  const int lane = tid & 63;
  const int w = __builtin_amdgcn_readfirstlane(tid >> 6);
  const int b = blockIdx.x;
  const int h = b >> 3;
  const int s = b & 7;
  const int l3 = lane >> 3, l7 = lane & 7;
  const bool b3 = lane & 8;
  const int perm8 = 4 * (lane >> 5) + 2 * ((lane >> 4) & 1) + (l3 & 1);

  const auto rs_ws = RSRC(ws, WS_BYTES);
  const u32x4 rsw = {(u32)(size_t)ws, (u32)((size_t)ws >> 32) & 0xffffu, WS_BYTES, 0x00020000u};
  const int sh = SWZ ? (s + h) & 7 : s;
  const size_t kvoff = ((size_t)h * SK + (SK - WLEN) + (IL ? 0 : sh * 512)) * DH;
  const auto rs_k = RSRC(kc + kvoff, (IL ? WLEN : 512) * DH * 2);
  const auto rs_v = RSRC(vc + kvoff, (IL ? WLEN : 512) * DH * 2);
  const int kvo = l3 * 384 + l7 * 16;
  char* const ldb = reinterpret_cast<char*>(lds);
  u32* const fl = reinterpret_cast<u32*>(lds + L_FL);
  u32* const qs = reinterpret_cast<u32*>(lds + L_QS);

#define PART(u) do { *reinterpret_cast<float*>(ldb + a_w + (u) * 256) = \
    tsum_hi8(acc[u][0].x, acc[u][0].y, acc[u][1].x, acc[u][1].y, acc[u][2].x, acc[u][2].y, acc[u][3].x, acc[u][3].y, b3); PIN(); } while (0)
  u32 role = 0xffffffffu;
  u32 a_w = (L_PART + w * 192 + 8 * l7 + perm8) * 4, a_r = (L_PART + (lane < 48 ? lane : 47) * 4) * 4;
  u32 o_voff = lane < 48 ? lane * 16 : OOB;
  f32x2 acc[3][4];
#pragma unroll
  for (int u = 0; u < 3; ++u)
#pragma unroll
    for (int c = 0; c < 4; ++c) acc[u][c] = f32x2{0.f, 0.f};
  u32 tag;
  u32 voob = OOB, vzero = 0u, vone = 1u;
  asm volatile("" : "+s"(out));

  if (w == 0) {
    __builtin_amdgcn_s_setprio(3);
    ESTAMP(8);
    const bool noq = s == 7 && (h & 2);
    const auto rs_x = RSRC(x, D * 2);
    const u32x4 xa = __builtin_amdgcn_raw_buffer_load_b128(rs_x, lane * 20, 0, 0);
    const u32 xb = __builtin_amdgcn_raw_buffer_load_b32(rs_x, lane * 20 + 16, 0, 0);
    const int sec = s < 6 ? 12 * (h >> 1) + 2 * s + (h & 1) : 6 * (h >> 2) + (s == 6 ? (h & 3) : 4 + (h & 1));
    const u32 stp = s < 6 ? 12288u : 3072u;
    const int cg = sec >> 1, rg = sec & 1;
    const auto rs_t = RSRC((s < 6 ? wq : w1) + (size_t)rg * 320 * (stp / 2) + cg * 64, noq ? 0 : 319 * stp + 128);
    u32x4 wv[40];
    const u32 tv = l3 * stp + l7 * 16;
    const u32 E = __builtin_amdgcn_raw_buffer_load_b32(rs_ws, 0, 0, 16);
    ESTAMP(9);
#pragma unroll
    for (int i = 0; i < 40; ++i) { wv[i] = __builtin_amdgcn_raw_buffer_load_b128(rs_t, tv, i * 8 * stp, 2); PIN();
    }
    u32x4 kr[3], vr[3];
#pragma unroll
    for (int i = 0; i < 3; ++i) kr[i] = __builtin_amdgcn_raw_buffer_load_b128(rs_k, kvo + i * 128, IL ? sh * 3072 : 0, 2);
#pragma unroll
    for (int i = 0; i < 3; ++i) vr[i] = __builtin_amdgcn_raw_buffer_load_b128(rs_v, kvo + i * 128, IL ? sh * 3072 : 0, 2);
    if (YAT && b == RB) {
      const u32x4 z = {0u, 0u, 0u, 0u};
#pragma unroll
      for (int i = 0; i < 5; ++i) ST128(z, (lane >> 2) * ASTR + (lane & 3) * 16, WS_A + i * 16 * ASTR);
    }
    if (lane < 48) fl[lane] = 0u;
    PIN();
    ESTAMP(10);
    BAR();
    float xv[10];
    xv[0] = blo(xa.x); xv[1] = bhi(xa.x); xv[2] = blo(xa.y); xv[3] = bhi(xa.y);
    xv[4] = blo(xa.z); xv[5] = bhi(xa.z); xv[6] = blo(xa.w); xv[7] = bhi(xa.w);
    xv[8] = blo(xb); xv[9] = bhi(xb);
    const float sm = ((xv[0] + xv[1]) + (xv[2] + xv[3])) + ((xv[4] + xv[5]) + (xv[6] + xv[7])) + (xv[8] + xv[9]);
    const float mean = wave_sum(sm) * (1.0f / D);
    float sq = 0.f;
#pragma unroll
    for (int e = 0; e < 10; ++e) { const float d = xv[e] - mean; sq += d * d; }
    const float var = wave_sum(sq) * (1.0f / D);
    const float mb = bfr(mean);
    const float den = bfr(__builtin_amdgcn_sqrtf(bfr(bfr(var) + 1e-5f)));
    const float rinv = 1.0f / den;
    u32* xn = reinterpret_cast<u32*>(lds + L_XN);
    if (YAT) {
      u32* xr = reinterpret_cast<u32*>(lds + L_XR) + lane * 5;
      xr[0] = xa.x; xr[1] = xa.y; xr[2] = xa.z; xr[3] = xa.w; xr[4] = xb;
    }
#pragma unroll
    for (int e = 0; e < 10; ++e) {
      const u32 r = lane * 10 + e, rr = r % 320;
      reinterpret_cast<u16*>(xn)[r - rr + (rr % 8) * 40 + rr / 8] = (u16)bfbits(bfr(xv[e] - mb) * rinv);
    }
    u32x4 xs[5];
#pragma unroll
    for (int i = 0; i < 5; ++i) xs[i] = *reinterpret_cast<const u32x4*>(xn + rg * 160 + l3 * 20 + i * 4);
    STAMP(1);
    float a8[8];
#pragma unroll
    for (int e = 0; e < 8; ++e) a8[e] = 0.f;
#pragma unroll
    for (int i = 0; i < 40; ++i) {
      const u32 v = xs[i / 8][(i / 2) % 4];
      const u32 xl = (i & 1) ? v >> 16 : v & 0xffffu;
      const u32 xh = (i & 1) ? v & 0xffff0000u : v << 16;
#pragma unroll
      for (int d = 0; d < 4; ++d) {
        a8[2 * d] = dot2(wv[i][d], xl, a8[2 * d]);
        a8[2 * d + 1] = dot2(wv[i][d], xh, a8[2 * d + 1]);
        KEEP(a8[2 * d]); KEEP(a8[2 * d + 1]);
      }
      PIN();
    }
    tag = E + 1u + (E == 0xffffffffu);
    STAMP(2);
    {
      float e0 = tsum16(tsum32(a8[0], a8[4]), tsum32(a8[2], a8[6]));
      float e1 = tsum16(tsum32(a8[1], a8[5]), tsum32(a8[3], a8[7]));
      e0 += dpp<ROR8>(e0); e1 += dpp<ROR8>(e1);
      const int gs = s < 6 ? sec : 192 + sec;
      const u32x4 un = {__float_as_uint(e0), tag, __float_as_uint(e1), tag};
      const u32 xv_ = (b3 || noq) ? OOB : (4 * l7 + (lane >> 4 & 1) + 2 * (lane >> 5)) * 16;
      ST128(un, xv_, WS_X + gs * 512);
      if (b == RB) {
        asm volatile("s_waitcnt vmcnt(0)" ::: "memory");
        __builtin_amdgcn_raw_buffer_store_b64(u32x2{0u, tag}, rs_ws, lane < NC ? WS_C + lane * CSTR : OOB, 0, 16);
      }
      STAMP(3);
      u32 r[3], c[3][2];
#pragma unroll
      for (int j = 0; j < 3; ++j) { r[j] = (3 * h + j) * 1024 + lane * 16; c[j][0] = c[j][1] = 0u; }
      __builtin_amdgcn_s_sleep(GAPX0);
      int spins = 0;
      while (true) {
        CBAR();
        u32x4 v[3];
#pragma unroll
        for (int j = 0; j < 3; ++j) v[j] = __builtin_amdgcn_raw_buffer_load_b128(rs_ws, r[j], WS_X, 16);
#pragma unroll
        for (int j = 0; j < 3; ++j) {
          const bool ok = v[j].y == tag && v[j].w == tag;
          c[j][0] = ok ? v[j].x : c[j][0]; c[j][1] = ok ? v[j].z : c[j][1]; r[j] = ok ? OOB : r[j];
        }
        if (__all((r[0] & r[1] & r[2]) == OOB) || ++spins >= SPIN_CAP) break;
        __builtin_amdgcn_s_sleep(GAPX);
      }
#pragma unroll
      for (int j = 0; j < 3; ++j) {
        const float t = tsum32(__uint_as_float(c[j][0]), __uint_as_float(c[j][1]));
        reinterpret_cast<u16*>(qs)[j * 64 + 2 * (lane & 31) + (lane >> 5)] = (u16)bfbits(t);
      }
    }
    qs[96] = tag;
    CBAR();
    if (lane == 0) STW(fl, 1u);
    STAMP(4);
    __builtin_amdgcn_s_setprio(0);
    u32x4 q[3];
#pragma unroll
    for (int u = 0; u < 3; ++u) q[u] = *reinterpret_cast<const u32x4*>(qs + u * 32 + l7 * 4);
    const float sco = score8(kr[0], kr[1], kr[2], q);
    const float mw = hi_max(sco);
    const float p = ex2((sco - mw) * LOG2E);
    const float lw = hi_sum(p);
    if (lane == 0) *reinterpret_cast<f32x4*>(lds + L_ST) = f32x4{mw, lw, __uint_as_float(1u), 0.f};
    STAMP(5);
#pragma unroll
    for (int u = 0; u < 3; ++u) { pv_step(vr[u], p, acc[u]); PART(u); }
    {
      int spins = 0;
      while (true) {
        CBAR();
        const u32 t = __builtin_amdgcn_raw_buffer_load_b32(rs_ws, WS_C + 4, (h % NC) * CSTR, 16);
        if (__builtin_amdgcn_readfirstlane(t) == __builtin_amdgcn_readfirstlane(tag) || ++spins >= SPIN_CAP) break;
        __builtin_amdgcn_s_sleep(GAPS);
      }
    }
    f32x4 ml;
    int spins = 0;
    while (true) {
      CBAR();
      ml = *reinterpret_cast<const f32x4*>(lds + L_ST + 4 * l7);
      if (__all(__float_as_uint(ml.z) != 0u) || ++spins >= 100 * SPIN_CAP) break;
      __builtin_amdgcn_s_sleep(4);
    }
    const float M = max8(ml.x);
    const float f = ex2((ml.x - M) * LOG2E);
    const float L = sum8(f * ml.y);
    const u32x4 un = {__float_as_uint(M), tag, __float_as_uint(L), tag};
    ST128(un, lane == 0 ? 0u : OOB, WS_S + b * 16);
    if (lane < 8) lds[L_FL + 8 + lane] = f * __builtin_amdgcn_rcpf(L);
  } else {
    constexpr int GS = IL ? 56 * 3072 : 3072;
    const int soff = IL ? (7 + sh * 7 + w) * 3072 : (8 + (w - 1) * 72) * (DH * 2);
    int so[9];
    so[0] = soff + (ROT == 0 ? 0 : ROT == 1 ? h % 9 : ROT == 5 ? (h & 3) * 2 : h & 7) * GS;
#pragma unroll
    for (int i = 1; i < 9; ++i) { const int t = so[i - 1] + GS; so[i] = ROT && t >= soff + 9 * GS ? soff : t; }
    u32x4 kr[NL], vr[NL];
#define ISSUE(n) do { \
      if ((n) < NL) kr[(n) < NL ? (n) : 0] = __builtin_amdgcn_raw_buffer_load_b128(rs_k, kvo + ((n) % 3) * 128, so[((n) / 3) % 9], 2); \
      else vr[(n) >= NL ? (n) - NL : 0] = __builtin_amdgcn_raw_buffer_load_b128(rs_v, kvo + ((n) % 3) * 128, so[((n) / 3) % 9], 2); \
      PIN(); } while (0)
#define GATE() WAITVM(PACE - 1)
#pragma unroll
    for (int n = 0; n < PACE; ++n) { if (n == 0) ESTAMP(8); ISSUE(n); }
    ESTAMP(9);
    asm volatile("s_barrier" ::: "memory");
    ESTAMP(10);
    KEEP(a_w); KEEP(a_r); KEEP(o_voff);
#pragma unroll
    for (int n = PACE; n < QS; ++n) { GATE(); if (n == PACE) ESTAMP(11); if (n == 2 * PACE) ESTAMP(12); ISSUE(n); }
    STAMP(3);
    {
      int spins = 0;
      while (LDW(fl) == 0u && ++spins < 100 * SPIN_CAP) __builtin_amdgcn_s_sleep(1);
      CBAR();
    }
    STAMP(4);
    u32x4 q[3];
#pragma unroll
    for (int u = 0; u < 3; ++u) q[u] = *reinterpret_cast<const u32x4*>(qs + u * 32 + l7 * 4);
    tag = qs[96];
    float sc[9];
#pragma unroll
    for (int i = 0; i < 9; ++i) {
      sc[i] = score8(kr[3 * i], kr[3 * i + 1], kr[3 * i + 2], q);
      KEEP(sc[i]);
      PIN();
      GATE(); ISSUE(QS + i);
    }
    const float mw = hi_max(fmx(fmx(fmx(sc[0], sc[1]), fmx(sc[2], sc[3])), fmx(fmx(fmx(sc[4], sc[5]), fmx(sc[6], sc[7])), sc[8])));
#pragma unroll
    for (int i = 0; i < 9; ++i) sc[i] = ex2((sc[i] - mw) * LOG2E);
    const float lw = hi_sum((((sc[0] + sc[1]) + (sc[2] + sc[3])) + ((sc[4] + sc[5]) + (sc[6] + sc[7]))) + sc[8]);
    if (lane == 0) *reinterpret_cast<f32x4*>(lds + L_ST + 4 * w) = f32x4{mw, lw, __uint_as_float(1u), 0.f};
    STAMP(5);
#pragma unroll
    for (int n = S3; n <= NSLOT; ++n) {
#pragma unroll
      for (int j = (n == S3 ? 0 : SCH.hi[n - 1]); j < SCH.hi[n]; ++j) {
        pv_step(vr[j], sc[j / 3], acc[j % 3]);
        PIN();
        if (j == NL - 1) STAMP(6);
        if (j >= NL - 3) PART(j % 3);
      }
      if (n < NSLOT) { GATE(); ISSUE(n); }
      if (n == NSLOT - CL) {
        if (lane == 0) __hip_atomic_fetch_add(fl + 1, 65536u, __ATOMIC_RELAXED, __HIP_MEMORY_SCOPE_WORKGROUP);
        PIN();
      }
    }
  }
  u32 old = 0;
  {
    asm volatile("s_mov_b64 exec, 1\n\tds_add_rtn_u32 %0, %1, %2 offset:4\n\ts_mov_b64 exec, -1" : "=&v"(old) : "v"(vzero), "v"(vone) : "memory");
    const f32x4 g0 = *reinterpret_cast<const f32x4*>(lds + L_FL + 8), g1 = *reinterpret_cast<const f32x4*>(lds + L_FL + 12);
    f32x4 o[8];
#pragma unroll
    for (int r = 0; r < 8; ++r) o[r] = *reinterpret_cast<const f32x4*>(ldb + a_r + r * 768);
#pragma unroll
    for (int r = 0; r < 8; ++r) asm volatile("" : "+v"(o[r]), "+v"(old));
    old = __builtin_amdgcn_readfirstlane(old);
    const f32x4 t = ((g0.x * o[0] + g0.y * o[1]) + (g0.z * o[2] + g0.w * o[3])) + ((g1.x * o[4] + g1.y * o[5]) + (g1.z * o[6] + g1.w * o[7]));
    const u32x4 un = {pack2(t.x, t.y), tag, pack2(t.z, t.w), tag};
    ST128(un, (old & 255u) == 7u ? o_voff : OOB, WS_O + b * 768);
    if ((old & 255u) == 7u) STAMP(7);
  }
  if (w) STAMP(1);
  KEEP(voob);
  if (w == 0) {
    int spins = 0;
    while (LDW(fl + 1) >> 16 < CN && ++spins < 100 * SPIN_CAP) __builtin_amdgcn_s_sleep(1);
    CBAR();
    STAMP(6);
    const u32 stag = __builtin_amdgcn_readfirstlane(tag);
    role = 255u;
    if (!SKIP || __builtin_amdgcn_readfirstlane(__builtin_amdgcn_raw_buffer_load_b32(rs_ws, WS_F, 0, 16)) != stag)
      role = __builtin_amdgcn_readfirstlane(__builtin_amdgcn_raw_ptr_buffer_atomic_add_i32(1, rs_ws, lane == 0 ? WS_C : OOB, (h % NC) * CSTR, 0)) * NC + h % NC;
    if (SKIP && role == NROLE - 1u) __builtin_amdgcn_raw_buffer_store_b32(tag, rs_ws, lane == 0 ? WS_F : OOB, 0, 16);
    if (THR && role == THR - 1u) __builtin_amdgcn_raw_buffer_store_b32(tag, rs_ws, lane == 0 ? WS_G : OOB, 0, 16);
    if (role + 1u < THR) {
      spins = 0;
      while (true) {
        __builtin_amdgcn_s_sleep(GAPT);
        CBAR();
        const u32 t = __builtin_amdgcn_raw_buffer_load_b32(rs_ws, WS_G, 0, 16);
        if (__builtin_amdgcn_readfirstlane(t) == stag || ++spins >= SPIN_CAP) break;
      }
    }
    if (lane == 0) STW(fl + 2, role + 1u);
    STAMP(14);
    if (EV && role == 255u) {
      spins = 0;
      while (true) {
        CBAR();
        const u32x2 t = __builtin_amdgcn_raw_buffer_load_b64(rs_ws, lane < NC ? WS_C + lane * CSTR : OOB, 0, 16);
        if (__all(lane >= NC || (t.x == 256u / NC && t.y == tag)) || ++spins >= SPIN_CAP) break;
        __builtin_amdgcn_s_sleep(GAPS1);
      }
      __builtin_amdgcn_raw_buffer_store_b32(tag, rs_ws, lane == 0 ? 0u : OOB, 0, 16);
    }
  } else {
    int spins = 0;
    while ((role = __builtin_amdgcn_readfirstlane(LDW(fl + 2)) - 1u) == 0xffffffffu && ++spins < 100 * SPIN_CAP) __builtin_amdgcn_s_sleep(1);
  }
  u32 t0, cnt = SPIN_CAP;
  u64 m;
  if (role < NTILE) {
    u32 tg, tc;
    asm("s_mul_i32 %0, %2, 205\n\ts_lshr_b32 %0, %0, 11\n\ts_mul_i32 %1, %0, 10\n\ts_sub_u32 %1, %2, %1"
        : "=&s"(tg), "=&s"(tc) : "s"(role) : "scc");
    const u32 c0 = (u32)((tg < 8u ? tab_pack(0, true) : tab_pack(8, true)) >> (8u * (tg & 7u))) & 255u;
    const u32 nc = (u32)((tg < 8u ? tab_pack(0, false) : tab_pack(8, false)) >> (8u * (tg & 7u))) & 255u;
    const u32 k = KARR ? old & 7u : w;
    const u32 nv = (nc + 7u - k) >> 3, ck = c0 + k, h0 = c0 / 6u;
    const auto rs_wo = RSRC(wo + (size_t)(ck * 32u * D + 64u * tc), 2 * 327680 + 31 * 1280 + 128);
    const auto rs_w2 = RSRC(w2 + (size_t)(ck * 8u * D + 64u * tc), 2 * 81920 + 7 * 1280 + 128);
    u32x4 wov[12], w2v[3], sv, hv[3];
    u32 so = 0u, ho[3] = {0u, 0u, 0u};
#define WISSUE(j) do { \
      w2v[j] = __builtin_amdgcn_raw_buffer_load_b128(rs_w2, l3 * 1280 + l7 * 16 + (j) * 81920, 0, 2); \
      _Pragma("unroll") for (int i = 0; i < 4; ++i) \
        wov[4 * (j) + i] = __builtin_amdgcn_raw_buffer_load_b128(rs_wo, l3 * 5120 + l7 * 16 + (j) * 327680 + i * 1280, 0, 2); \
    } while (0)
    if (w == 0) {
      so = 6u * (h0 + l3) < c0 + nc ? lane * 16 : OOB;
#pragma unroll
      for (int j = 0; j < 3; ++j) {
        const u32 g = 32 * j + (lane & 31), gp = 4u * c0 + g;
        ho[j] = ((gp >> 5) * 1024u + (lane >> 5) * 512 + (gp & 31u) * 16u) | (g < 4u * nc ? 0u : OOB);
      }
      KEEP(so); KEEP(ho[0]); KEEP(ho[1]); KEEP(ho[2]);
      asm volatile("buffer_load_dwordx4 %0, %4, %8, %9 offen sc1\n\tbuffer_load_dwordx4 %1, %5, %8, %10 offen sc1\n\t"
                   "buffer_load_dwordx4 %2, %6, %8, %10 offen sc1\n\tbuffer_load_dwordx4 %3, %7, %8, %10 offen sc1"
                   : "=&v"(sv), "=&v"(hv[0]), "=&v"(hv[1]), "=&v"(hv[2])
                   : "v"(so), "v"(ho[0]), "v"(ho[1]), "v"(ho[2]), "s"(rsw), "s"(WS_S + h0 * 128u), "s"(WS_X + 96u * 1024u) : "memory");
    }
    if (nv > 0u) {
      if (nv > 1u) {
        if (nv > 2u) WISSUE(2);
        WISSUE(1);
      }
      WISSUE(0);
    }
    PIN();
    if (w == 0) {
      asm volatile("s_waitcnt vmcnt(15)\n\ts_cmp_gt_u32 %4, 2\n\ts_cbranch_scc1 1f\n\t"
                   "s_waitcnt vmcnt(10)\n\ts_cmp_gt_u32 %4, 1\n\ts_cbranch_scc1 1f\n\t"
                   "s_waitcnt vmcnt(5)\n\ts_cmp_gt_u32 %4, 0\n\ts_cbranch_scc1 1f\n\t"
                   "s_waitcnt vmcnt(0)\n1:"
                   : "+v"(sv), "+v"(hv[0]), "+v"(hv[1]), "+v"(hv[2]) : "s"(nv) : "scc", "memory");
#define HBAD() (((ho[0] | ((hv[0].y ^ tag) | (hv[0].w ^ tag) ? 0u : OOB)) & (ho[1] | ((hv[1].y ^ tag) | (hv[1].w ^ tag) ? 0u : OOB)) & \
                 (ho[2] | ((hv[2].y ^ tag) | (hv[2].w ^ tag) ? 0u : OOB))) < OOB)
      if (__builtin_expect(__any(HBAD()), 0)) {
        int spins = 0;
        do {
          __builtin_amdgcn_s_sleep(GAPS);
          CBAR();
          _Pragma("unroll") for (int j = 0; j < 3; ++j) hv[j] = __builtin_amdgcn_raw_buffer_load_b128(rs_ws, ho[j], WS_X + 96u * 1024u, 16);
        } while (__any(HBAD()) && ++spins < SPIN_CAP);
      }
      u32 r0 = (so >= OOB || (sv.y == tag && sv.w == tag)) ? OOB : so + WS_S + h0 * 128u;
      u32x16 RS;
      asm volatile(
"v_cmp_eq_u32 vcc, %[r0], %[oob]\n"
"s_cmp_eq_u64 vcc, -1\n"
"s_cbranch_scc1 2f\n"
PL(232, 235, r0) SLP(GAPS0)
PL(236, 239, r0) SLP(GAPS0)
PL(240, 243, r0) SLP(GAPS0)
PL(244, 247, r0)
"1:\n"
P1_CK(GAPS1, 232, 233, 234, 235)
P1_CK(GAPS1, 236, 237, 238, 239)
P1_CK(GAPS1, 240, 241, 242, 243)
P1_CK(GAPS1, 244, 245, 246, 247)
PLOOP(1)
"2:\n"
: [c0] "+v"(sv.x), [c1] "+v"(sv.z), [r0] "+v"(r0), [cnt] "+s"(cnt), [m] "=&s"(m), "=&{v[232:247]}"(RS)
: [rs] "s"(rsw), [tag] "v"(tag), [oob] "v"(voob)
: "vcc", "scc", "memory");
      const float mp = __uint_as_float(sv.x);
      const float f = ex2((mp - max8(mp)) * LOG2E) * __uint_as_float(sv.z);
      lds[L_WT + lane] = f * __builtin_amdgcn_rcpf(sum8(f));
#pragma unroll
      for (int j = 0; j < 3; ++j)
        reinterpret_cast<u16*>(lds + L_HD)[64 * j + 2 * (lane & 31) + (lane >> 5)] =
            (u16)bfbits(fmaxf(tsum32(__uint_as_float(hv[j].x), __uint_as_float(hv[j].z)), 0.f));
      CBAR();
      if (lane == 0) STW(fl + 3, 1u);
      asm volatile("s_waitcnt vmcnt(0)" :: "{v[232:247]}"(RS) : "memory");
      cnt = SPIN_CAP;
    } else {
      int spins = 0;
      while (LDW(fl + 3) == 0u && ++spins < 100 * SPIN_CAP) __builtin_amdgcn_s_sleep(2);
    }
    CBAR();
    float wt[3], y[8];
    u32 hid[3], rr[3], wp[6][8];
#pragma unroll
    for (int j = 0; j < 3; ++j) {
      const u32 c = ck + 8 * j, hh = c / 6u;
      wt[j] = lds[L_WT + ((hh - h0) & 7u) * 8u + l3];
      hid[j] = reinterpret_cast<const u16*>(lds + L_HD)[(k + 8 * j) * 8u + l3];
      rr[j] = WS_O + (hh * 8u + l3) * 768u + (c - 6u * hh) * 128u + l7 * 16;
      KEEP(wt[j]); KEEP(rr[j]);
    }
#pragma unroll
    for (int e = 0; e < 8; ++e) y[e] = 0.f;
    u32 a_ow = (L_OT + w * 64) * 4 + l7 * 32 + (l3 >> 1) * 2, a_or = (L_OT + w * 64) * 4 + l3 * 32;
    u32 a_yw = (L_Y + k * 64 + 8 * l7 + perm8) * 4, a_yr = (L_Y + lane) * 4;
    u32 yv = WS_Y + (8 * tc + l3) * 1024 + tg * 64 + l7 * 8;
    KEEP(a_ow); KEEP(a_or); KEEP(a_yw); KEEP(a_yr); KEEP(yv);
    u32 av = WS_A + (8 * tc + l3) * ASTR + l7 * 8, ov = (64 * tc + lane) * 2, ev = (tc == 0 && lane == 0) ? 0u : OOB;
    float xo = blo(reinterpret_cast<const u16*>(lds + L_XR)[64 * tc + lane]);
    float thr = __builtin_fabsf(xo) < 6.103515625e-05f ? 6.103515625e-05f : 0.f;
    if (YAT) { KEEP(av); KEEP(ov); KEEP(xo); KEEP(thr); if (!EV) KEEP(ev); }
    PIN();
#define WPREP(j) do { \
      _Pragma("unroll") for (int d = 0; d < 4; ++d) { \
        y[2 * d] = dot2(w2v[j][d], hid[j], y[2 * d]); \
        y[2 * d + 1] = dot2(w2v[j][d], hid[j] << 16, y[2 * d + 1]); \
        KEEP(y[2 * d]); KEEP(y[2 * d + 1]); \
      } \
      _Pragma("unroll") for (int i = 2 * (j); i < 2 * (j) + 2; ++i) { \
        _Pragma("unroll") for (int d = 0; d < 4; ++d) { \
          wp[i][2 * d] = __builtin_amdgcn_perm(wov[2 * i + 1][d], wov[2 * i][d], 0x05040100u); \
          wp[i][2 * d + 1] = __builtin_amdgcn_perm(wov[2 * i + 1][d], wov[2 * i][d], 0x07060302u); \
          KEEP(wp[i][2 * d]); KEEP(wp[i][2 * d + 1]); \
        } \
        PIN(); \
      } } while (0)
#define ORED(j) do { \
      float u = tsum16(tsum32(wt[j] * blo(c[2 * (j)]), wt[j] * blo(c[2 * (j) + 1])), tsum32(wt[j] * bhi(c[2 * (j)]), wt[j] * bhi(c[2 * (j) + 1]))); \
      u += dpp<ROR8>(u); \
      *reinterpret_cast<u16*>(ldb + a_ow + (j) * 8) = (u16)bfbits(u); } while (0)
#define YDOT(j, o0, o1) do { \
      _Pragma("unroll") for (int e = 0; e < 8; ++e) y[e] = dot2(wp[2 * (j)][e], o0, y[e]); \
      _Pragma("unroll") for (int e = 0; e < 8; ++e) y[e] = dot2(wp[2 * (j) + 1][e], o1, y[e]); } while (0)
    u32x16 R0, R1, R2;
    if (nv > 0u) {
      if (nv > 1u) {
        if (nv > 2u) WPREP(2);
        WPREP(1);
      }
      WPREP(0);
    }
    WAITVM(0);
    u32 nw = nv;
    asm volatile("" : "+s"(nw));
    if (nw > 0u) {
      if (w) STAMP(14);
      {
        int spins = 0;
        while ((LDW(fl + 1) & 255u) != 8u && ++spins < 100 * SPIN_CAP) __builtin_amdgcn_s_sleep(1);
        CBAR();
      }
      if (w) STAMP(2);
      u32 c[6] = {0, 0, 0, 0, 0, 0};
      asm volatile(
"s_cmp_lt_u32 %[nv], 2\n"
"s_cbranch_scc1 3f\n"
"s_cmp_lt_u32 %[nv], 3\n"
"s_cbranch_scc1 4f\n"
P3_LD(200, 203, 204, 207, 208, 211) SLP(GAPO0)
P3_LD(212, 215, 216, 219, 220, 223) SLP(GAPO0)
P3_LD(224, 227, 228, 231, 232, 235) SLP(GAPO0)
P3_LD(236, 239, 240, 243, 244, 247)
"1:\n"
P3_CK(200, 201, 202, 203, 204, 205, 206, 207, 208, 209, 210, 211)
P3_CK(212, 213, 214, 215, 216, 217, 218, 219, 220, 221, 222, 223)
P3_CK(224, 225, 226, 227, 228, 229, 230, 231, 232, 233, 234, 235)
P3_CK(236, 237, 238, 239, 240, 241, 242, 243, 244, 245, 246, 247)
PLOOP(1)
"s_branch 2f\n"
"3:\n"
PL(232, 235, r0) SLP(GAPO0)
PL(236, 239, r0) SLP(GAPO0)
PL(240, 243, r0) SLP(GAPO0)
PL(244, 247, r0)
"5:\n"
P1_CK(GAPO, 232, 233, 234, 235)
P1_CK(GAPO, 236, 237, 238, 239)
P1_CK(GAPO, 240, 241, 242, 243)
P1_CK(GAPO, 244, 245, 246, 247)
PLOOP(5)
"s_branch 2f\n"
"4:\n"
P2_LD(216, 219, 220, 223) SLP(GAPO0)
P2_LD(224, 227, 228, 231) SLP(GAPO0)
P2_LD(232, 235, 236, 239) SLP(GAPO0)
P2_LD(240, 243, 244, 247)
"6:\n"
P2_CK(216, 217, 218, 219, 220, 221, 222, 223)
P2_CK(224, 225, 226, 227, 228, 229, 230, 231)
P2_CK(232, 233, 234, 235, 236, 237, 238, 239)
P2_CK(240, 241, 242, 243, 244, 245, 246, 247)
PLOOP(6)
"2:\n"
: [c0] "+v"(c[0]), [c1] "+v"(c[1]), [c2] "+v"(c[2]), [c3] "+v"(c[3]), [c4] "+v"(c[4]), [c5] "+v"(c[5]),
  [r0] "+v"(rr[0]), [r1] "+v"(rr[1]), [r2] "+v"(rr[2]),
  [t0] "=&v"(t0), [cnt] "+s"(cnt), [m] "=&s"(m), "=&{v[200:215]}"(R0), "=&{v[216:231]}"(R1), "=&{v[232:247]}"(R2)
: [rs] "s"(rsw), [tag] "v"(tag), [oob] "v"(voob), [nv] "s"(nv)
: "vcc", "scc", "memory");
      STAMP(8);
      if (nv == 1u) {
        ORED(0);
        CBAR();
        const u32x2 ov = *reinterpret_cast<const u32x2*>(ldb + a_or);
        YDOT(0, ov[0], ov[1]);
      } else if (MAXN <= 16 || nv == 2u) {
        ORED(0); ORED(1);
        CBAR();
        const u32x4 ov = *reinterpret_cast<const u32x4*>(ldb + a_or);
        YDOT(0, ov[0], ov[1]);
        YDOT(1, ov[2], ov[3]);
      } else {
        ORED(0); ORED(1); ORED(2);
        CBAR();
        const u32x4 ov = *reinterpret_cast<const u32x4*>(ldb + a_or);
        const u32x2 ow = *reinterpret_cast<const u32x2*>(ldb + a_or + 16);
        YDOT(0, ov[0], ov[1]);
        YDOT(1, ov[2], ov[3]);
        YDOT(2, ow[0], ow[1]);
      }
    }
    {
      *reinterpret_cast<float*>(ldb + a_yw) = tsum_hi8(y[0], y[1], y[2], y[3], y[4], y[5], y[6], y[7], b3);
      BAR();
      STAMP(9);
      if (w < (YAT ? 2 : 1)) {
        float e[8];
#pragma unroll
        for (int r = 0; r < 8; ++r) e[r] = *reinterpret_cast<const float*>(ldb + a_yr + r * 256);
        const float es = ((e[0] + e[1]) + (e[2] + e[3])) + ((e[4] + e[5]) + (e[6] + e[7]));
        if (YAT && w == 0) {
          const bool ok = __builtin_fabsf(es) < 131000.f;
          const u32x2 fx = __builtin_bit_cast(u32x2, (double)es + 1179648.0);
          u32x2 ol = {ok ? fx.x : 0u, ok ? fx.y + ((1u << 22) - 0x41300000u) : (1u << 22) + (1u << 27)};
          u64 mine = __builtin_bit_cast(u64, ol);
          asm volatile("buffer_atomic_add_x2 %0, %1, %2, 0 offen sc0" : "+v"(ol) : "v"(av), "s"(rsw) : "memory");
          STAMP(10);
          mine += 0x80000000ull;
          KEEP(mine);
          asm volatile("s_waitcnt vmcnt(0)" : "+v"(ol) :: "memory");
          STAMP(11);
          const u64 tt = __builtin_bit_cast(u64, ol) + mine;
          const u32 th = (u32)(tt >> 32);
          const bool last = ((th >> 22) & 31u) == NP;
          float r = xo + __builtin_fmaf((float)(int)((u32)tt ^ 0x80000000u), 2.3283064365386963e-10f, (float)(int)((th & 0x3fffffu) - NP * 131072u));
          if (__builtin_expect(__any(last && ((th >> 27) != 0u || __builtin_fabsf(r) < thr)), 0)) {
            float p[NP];
            int spins = 0;
            bool got;
            do {
              CBAR();
              got = true;
#pragma unroll
              for (int g = 0; g < (int)NP; ++g) {
                const u32x2 v = __builtin_amdgcn_raw_buffer_load_b64(rs_ws, last ? yv - tg * 64 + g * 64 : OOB, 0, 16);
                p[g] = __uint_as_float(v.x);
                got = got && v.y == tag;
              }
            } while (__any(last && !got) && ++spins < SPIN_CAP);
            float a = p[0];
#pragma unroll
            for (int g = 1; g < (int)NP; ++g) a += p[g];
            if ((th >> 27) != 0u || __builtin_fabsf(r) < thr) r = xo + a;
          }
          const auto rs_out = RSRC(out, D * 2);
          __builtin_amdgcn_raw_buffer_store_b16((u16)bfbits(r), rs_out, last ? ov : OOB, 0, 0);
          if (!EV) __builtin_amdgcn_raw_buffer_store_b32(tag, rs_ws, last ? ev : OOB, 0, 16);
        } else {
          const u32x2 un = {__float_as_uint(es), tag};
          __builtin_amdgcn_raw_buffer_store_b64(un, rs_ws, yv, 0, 16);
          if (!YAT) STAMP(10);
        }
      }
      asm volatile("s_waitcnt vmcnt(0)" :: "{v[200:215]}"(R0), "{v[216:231]}"(R1), "{v[232:247]}"(R2) : "memory");
    }
  } else if (!YAT && role < NROLE) {
    const u32 ri = (role - NTILE) * 8u + w;
    const u32 rcol = 8 * ri + 2 * (lane & 3) + (lane >> 5);
    u32 xo = x[rcol];
    u32 ov = (lane & 28) ? OOB : rcol * 2;
    u32 r0 = lane < 4 * NP ? WS_Y + ri * 1024 + lane * 16 : OOB;
    u32 ev = (ri == 0 && lane == 0) ? 0u : OOB;
    const auto rs_out = RSRC(out, D * 2);
    KEEP(xo); KEEP(ov); KEEP(ev);
    {
      u32x16 R0;
      u32 c[2] = {0, 0};
      asm volatile(
PL(232, 235, r0) "s_sleep " STR(GAPY0) "\n"
PL(236, 239, r0) "s_sleep " STR(GAPY0) "\n"
PL(240, 243, r0) "s_sleep " STR(GAPY0) "\n"
PL(244, 247, r0)
"1:\n"
P1_CK(GAPY, 232, 233, 234, 235)
P1_CK(GAPY, 236, 237, 238, 239)
P1_CK(GAPY, 240, 241, 242, 243)
P1_CK(GAPY, 244, 245, 246, 247)
"s_sub_u32 %[cnt], %[cnt], 1\n"
"s_cbranch_scc0 1b\n"
"2:\n"
: [c0] "+v"(c[0]), [c1] "+v"(c[1]), [r0] "+v"(r0), [cnt] "+s"(cnt), [m] "=&s"(m), "=&{v[232:247]}"(R0)
: [rs] "s"(rsw), [tag] "v"(tag), [oob] "v"(voob)
: "vcc", "scc", "memory");
      STAMP(11);
      float v = tsum32(__uint_as_float(c[0]), __uint_as_float(c[1]));
      v = tsum16(v, v);
      v += dpp<ROR8>(v);
      v += dpp<ROR4>(v);
      __builtin_amdgcn_raw_buffer_store_b16((u16)bfbits(v + blo(xo)), rs_out, ov, 0, 0);
      __builtin_amdgcn_raw_buffer_store_b32(tag, rs_ws, ev, 0, 16);
      asm volatile("s_waitcnt vmcnt(0)" :: "{v[232:247]}"(R0) : "memory");
    }
  }
}

extern "C" void kernel_launch(void* const* d_in, const int* in_sizes, int n_in,
                              void* d_out, int out_size, void* d_ws, size_t ws_size,
                              hipStream_t stream) {
  if (ws_size < WS_BYTES) return;
  fused_decode_kernel<<<256, 512, 0, stream>>>(
      (const u16*)d_in[0], (const u16*)d_in[1], (const u16*)d_in[2], (const u16*)d_in[3],
      (const u16*)d_in[4], (const u16*)d_in[6], (const u16*)d_in[7], (u16*)d_out, (u32*)d_ws);
}
```

```cpp
#include <hip/hip_runtime.h>
#include <hip/hip_bf16.h>
#include <stdint.h>

#define PACE 6
#define CL 6
#define KARR 1
#define CN 7
#define TAB 12, 12, 11, 11, 10, 10, 10, 9, 9, 9, 8, 8, 8, 8, 7, 7, 7, 6, 6, 6, 5, 5, 4, 4
#define THR 0
#define GAPT 8
#define SKIP 0
#define NC 8
#define CSTR 256
#define SWZ 1
#define IL 1
#define ROT 0
#define EV 1
#define KS 12
#define MAPI 0
#define GAPS 16
#define GAPS0 8
#define GAPS1 4
#define GAPX0 4
#define GAPX 4
#define GAPO0 2
#define GAPO 1
#define GAPY0 5
#define GAPY 2
#define YAT 1
#define ASTR 64
#define RB 31
#define QS 33
#define PB 2

typedef unsigned int u32;
typedef unsigned short u16;
typedef unsigned long long u64;
using u32x2 = __attribute__((ext_vector_type(2))) unsigned;
using u32x4 = __attribute__((ext_vector_type(4))) unsigned;
using u32x16 = __attribute__((ext_vector_type(16))) unsigned;
using f32x2 = __attribute__((ext_vector_type(2))) float;
using f32x4 = __attribute__((ext_vector_type(4))) float;
using bf16x2 = __attribute__((ext_vector_type(2))) __bf16;

constexpr int D = 640, DH = 192, SK = 32768, WLEN = 4096;
constexpr int NL = 27;
constexpr int NSLOT = 2 * NL;
constexpr u32 TABN[] = {TAB};
constexpr u32 NP = sizeof(TABN) / sizeof(u32);
constexpr u32 NTILE = 10 * NP, NROLE = NTILE + (YAT ? 0 : 10);
static_assert(NP <= (YAT ? 24 : 16), "table");
constexpr u32 YS = NP <= 16 ? 1024u : 2048u;
constexpr u32 FB = NP <= 16 ? 131072u : 65536u;
constexpr u64 tab_pack(int lo, bool first) {
  u64 r = 0;
  u32 c = 0;
  for (int g = 0; g < (int)NP; ++g) {
    if (g >= lo && g < lo + 8) r |= (u64)(first ? c : TABN[g]) << (8 * (g - lo));
    c += TABN[g];
  }
  return r;
}
constexpr bool tab_ok() {
  u32 c = 0;
  for (int g = 0; g < (int)NP; ++g) { if (TABN[g] < 1 || TABN[g] > 24) return false; c += TABN[g]; }
  return c == 192;
}
static_assert(tab_ok(), "tile table");
constexpr u32 tab_max() {
  u32 c = 0;
  for (int g = 0; g < (int)NP; ++g) c = TABN[g] > c ? TABN[g] : c;
  return c;
}
constexpr u32 MAXN = tab_max();
constexpr u32 WS_F = 2560;
constexpr u32 WS_G = 3072;
constexpr u32 WS_S = 4096;
constexpr u32 WS_O = WS_S + 32u * 128u;
constexpr u32 WS_Y = WS_O + 256u * 768u;
constexpr u32 WS_X = WS_Y + 80u * YS;
constexpr u32 WS_C = WS_X + 240u * 512u;
constexpr u32 WS_A = WS_C + NC * CSTR;
constexpr u32 WS_BYTES = WS_A + 80u * ASTR;
constexpr int SPIN_CAP = 200000;
constexpr float LOG2E = 1.4426950408889634f;
constexpr float SCALE = 0.07216878364870322f;

__device__ __forceinline__ float dot2(u32 a, u32 b, float acc) {
  return __builtin_amdgcn_fdot2_f32_bf16(__builtin_bit_cast(bf16x2, a), __builtin_bit_cast(bf16x2, b), acc, false);
}
template <int CTRL> __device__ __forceinline__ float dpp(float x) {
  return __builtin_bit_cast(float, __builtin_amdgcn_mov_dpp(__builtin_bit_cast(int, x), CTRL, 0xf, 0xf, true));
}
constexpr int XOR1 = 0xB1, XOR2 = 0x4E, XOR7 = 0x141, ROR8 = 0x128, ROR4 = 0x124;
__device__ __forceinline__ float sum8(float x) {
  x += dpp<XOR1>(x); x += dpp<XOR2>(x); x += dpp<XOR7>(x); return x;
}
__device__ __forceinline__ float fmx(float a, float b) {
  return __builtin_fmaxf(a, b);
}
__device__ __forceinline__ float max8(float x) {
  x = fmx(x, dpp<XOR1>(x)); x = fmx(x, dpp<XOR2>(x)); x = fmx(x, dpp<XOR7>(x)); return x;
}
__device__ __forceinline__ float xrow_sum(float x) {
  auto s = __builtin_amdgcn_permlane16_swap(__float_as_uint(x), __float_as_uint(x), false, false);
  x = __uint_as_float(s[0]) + __uint_as_float(s[1]);
  auto t = __builtin_amdgcn_permlane32_swap(__float_as_uint(x), __float_as_uint(x), false, false);
  return __uint_as_float(t[0]) + __uint_as_float(t[1]);
}
__device__ __forceinline__ float xrow_max(float x) {
  auto s = __builtin_amdgcn_permlane16_swap(__float_as_uint(x), __float_as_uint(x), false, false);
  x = fmx(__uint_as_float(s[0]), __uint_as_float(s[1]));
  auto t = __builtin_amdgcn_permlane32_swap(__float_as_uint(x), __float_as_uint(x), false, false);
  return fmx(__uint_as_float(t[0]), __uint_as_float(t[1]));
}
__device__ __forceinline__ float tsum32(float a, float b) {
  auto t = __builtin_amdgcn_permlane32_swap(__float_as_uint(a), __float_as_uint(b), false, false);
  return __uint_as_float(t[0]) + __uint_as_float(t[1]);
}
__device__ __forceinline__ float tsum16(float a, float b) {
  auto t = __builtin_amdgcn_permlane16_swap(__float_as_uint(a), __float_as_uint(b), false, false);
  return __uint_as_float(t[0]) + __uint_as_float(t[1]);
}
__device__ __forceinline__ float tsum8(float a, float b, bool b3) {
  const float snd = b3 ? a : b, kp = b3 ? b : a;
  return kp + dpp<ROR8>(snd);
}
__device__ __forceinline__ float tsum_hi8(float v0, float v1, float v2, float v3, float v4, float v5, float v6, float v7, bool b3) {
  return tsum8(tsum16(tsum32(v0, v4), tsum32(v2, v6)), tsum16(tsum32(v1, v5), tsum32(v3, v7)), b3);
}
__device__ __forceinline__ float hi_sum(float x) { x += dpp<ROR8>(x); return xrow_sum(x); }
__device__ __forceinline__ float hi_max(float x) { x = fmx(x, dpp<ROR8>(x)); return xrow_max(x); }
__device__ __forceinline__ float wave_sum(float x) { return hi_sum(sum8(x)); }
__device__ __forceinline__ u32 bfbits(float f) { return (u32)__builtin_bit_cast(u16, (__bf16)f); }
__device__ __forceinline__ float bfr(float f) { return __uint_as_float(bfbits(f) << 16); }
__device__ __forceinline__ u32 pack2(float a, float b) {
  bf16x2 p = {(__bf16)a, (__bf16)b};
  return __builtin_bit_cast(u32, p);
}
__device__ __forceinline__ float blo(u32 v) { return __uint_as_float(v << 16); }
__device__ __forceinline__ float bhi(u32 v) { return __uint_as_float(v & 0xffff0000u); }
__device__ __forceinline__ float ex2(float v) { return __builtin_amdgcn_exp2f(v); }
#define BAR() asm volatile("s_waitcnt lgkmcnt(0)\n\ts_barrier" ::: "memory")
#define PIN() __builtin_amdgcn_sched_barrier(0)
#define CBAR() asm volatile("" ::: "memory")
#define STAMP(i)
#define ESTAMP(i)
#define STR_(x) #x
#define STR(x) STR_(x)
#define KEEP(v) asm volatile("" : "+v"(v))
#define RSRC(p, n) __builtin_amdgcn_make_buffer_rsrc((void*)(p), 0, (n), 0x00020000)
#define LDW(p) __hip_atomic_load((p), __ATOMIC_RELAXED, __HIP_MEMORY_SCOPE_WORKGROUP)
#define STW(p, v) __hip_atomic_store((p), (v), __ATOMIC_RELAXED, __HIP_MEMORY_SCOPE_WORKGROUP)

#define WAITVM(n) __builtin_amdgcn_s_waitcnt(0x0F70 | ((n) & 15) | (((n) >> 4) << 14))

#define ST128(v, vo, so) do { __builtin_amdgcn_raw_buffer_store_b128((v), rs_ws, (vo), (so), 16); asm volatile("s_nop 1" :: "v"(v)); } while (0)

constexpr int L_FL = 0;
constexpr int L_ST = 16;
constexpr int L_QS = L_ST + 32;
constexpr int L_XN = L_QS + 100;
constexpr int L_PART = L_XN + 320 + 40;
constexpr int L_OT = L_PART + 8 * 192;
constexpr int L_WT = L_OT + 8 * 64;
constexpr int L_HD = L_WT + 64;
constexpr int L_Y = L_HD + 96;
constexpr int L_XR = L_Y + 8 * 64;
constexpr int L_END = L_XR + 320;
constexpr u32 OOB = 0x80000000u;

struct Sched { int hi[NSLOT + 1]; };
constexpr int S3 = QS + 9;
constexpr Sched make_sched() {
  Sched t{};
  int j = 0;
  for (int n = S3; n < NSLOT; ++n) {
    int burst = 0;
    while (j < NL && burst < PB && n - (NL + j) >= PACE) { ++j; ++burst; }
    t.hi[n] = j;
  }
  t.hi[NSLOT] = NL;
  return t;
}
constexpr Sched SCH = make_sched();
static_assert(QS >= NL && S3 <= NSLOT - CL && CL >= 1, "schedule");

__device__ __forceinline__ float score8(const u32x4 k0, const u32x4 k1, const u32x4 k2, const u32x4 (&q)[3]) {
  float a = dot2(k0.x, q[0].x, 0.f), c = dot2(k0.y, q[0].y, 0.f), e = dot2(k0.z, q[0].z, 0.f), g = dot2(k0.w, q[0].w, 0.f);
  a = dot2(k1.x, q[1].x, a); c = dot2(k1.y, q[1].y, c); e = dot2(k1.z, q[1].z, e); g = dot2(k1.w, q[1].w, g);
  a = dot2(k2.x, q[2].x, a); c = dot2(k2.y, q[2].y, c); e = dot2(k2.z, q[2].z, e); g = dot2(k2.w, q[2].w, g);
  return bfr(bfr(sum8((a + c) + (e + g))) * SCALE);
}
__device__ __forceinline__ void pv_step(const u32x4 d, const float pp, f32x2 (&acc)[4]) {
#pragma unroll
  for (int c = 0; c < 4; ++c) {
    acc[c] = __builtin_elementwise_fma(f32x2{blo(d[c]), bhi(d[c])}, f32x2{pp, pp}, acc[c]);
    KEEP(acc[c]);
  }
}

#define PL(a, b, r) "buffer_load_dwordx4 v[" #a ":" #b "], %[" #r "], %[rs], 0 offen sc1\n"
#define PC(n, x, y, z, t, ca, cb, r) \
  "s_waitcnt vmcnt(" #n ")\n" \
  "v_cmp_eq_u32 vcc, v" #y ", %[tag]\n" \
  "v_cmp_eq_u32 %[m], v" #t ", %[tag]\n" \
  "s_and_b64 vcc, vcc, %[m]\n" \
  "v_cndmask_b32 %[" #ca "], %[" #ca "], v" #x ", vcc\n" \
  "v_cndmask_b32 %[" #cb "], %[" #cb "], v" #z ", vcc\n" \
  "v_cndmask_b32 %[" #r "], %[" #r "], %[oob], vcc\n"
#define PEND(t, gap) \
  "v_cmp_eq_u32 vcc, %[" #t "], %[oob]\n" \
  "s_cmp_eq_u64 vcc, -1\n" \
  "s_cbranch_scc1 2f\n" \
  "s_sleep " STR(gap) "\n"
#define P3_LD(a0, a3, b0, b3, d0, d3) PL(a0, a3, r0) PL(b0, b3, r1) PL(d0, d3, r2)
#define P3_CK(a0, a1, a2, a3, b0, b1, b2, b3, d0, d1, d2, d3) \
  PC(11, a0, a1, a2, a3, c0, c1, r0) PC(10, b0, b1, b2, b3, c2, c3, r1) PC(9, d0, d1, d2, d3, c4, c5, r2) \
  "v_min3_u32 %[t0], %[r0], %[r1], %[r2]\n" PEND(t0, GAPO) P3_LD(a0, a3, b0, b3, d0, d3)
#define P2_LD(a0, a3, b0, b3) PL(a0, a3, r0) PL(b0, b3, r1)
#define P2_CK(a0, a1, a2, a3, b0, b1, b2, b3) \
  PC(7, a0, a1, a2, a3, c0, c1, r0) PC(6, b0, b1, b2, b3, c2, c3, r1) \
  "v_and_b32 %[t0], %[r0], %[r1]\n" PEND(t0, GAPO) P2_LD(a0, a3, b0, b3)
#define P1_CK(gap, a0, a1, a2, a3) PC(3, a0, a1, a2, a3, c0, c1, r0) PEND(r0, gap) PL(a0, a3, r0)
#define SLP(n) "s_sleep " STR(n) "\n"
#define PLOOP(l) "s_sub_u32 %[cnt], %[cnt], 1\ns_cbranch_scc0 " #l "b\n"

__global__ void __launch_bounds__(512, 2)
fused_decode_kernel(const u16* __restrict__ x, const u16* __restrict__ kc, const u16* __restrict__ vc,
                    const u16* __restrict__ wq, const u16* __restrict__ wo, const u16* __restrict__ w1,
                    const u16* __restrict__ w2, u16* out, u32* __restrict__ ws) {
  __shared__ __attribute__((aligned(16))) float lds[L_END];
  const int tid = threadIdx.x;
  const int lane = tid & 63;
  const int w = __builtin_amdgcn_readfirstlane(tid >> 6);
  const int b = blockIdx.x;
  const int h = b >> 3;
  const int s = b & 7;
  const int l3 = lane >> 3, l7 = lane & 7;
  const bool b3 = lane & 8;
  const int perm8 = 4 * (lane >> 5) + 2 * ((lane >> 4) & 1) + (l3 & 1);

  const auto rs_ws = RSRC(ws, WS_BYTES);
  const u32x4 rsw = {(u32)(size_t)ws, (u32)((size_t)ws >> 32) & 0xffffu, WS_BYTES, 0x00020000u};
  const int sh = SWZ ? (s + h) & 7 : s;
  const size_t kvoff = ((size_t)h * SK + (SK - WLEN) + (IL ? 0 : sh * 512)) * DH;
  const auto rs_k = RSRC(kc + kvoff, (IL ? WLEN : 512) * DH * 2);
  const auto rs_v = RSRC(vc + kvoff, (IL ? WLEN : 512) * DH * 2);
  const int kvo = l3 * 384 + l7 * 16;
  char* const ldb = reinterpret_cast<char*>(lds);
  u32* const fl = reinterpret_cast<u32*>(lds + L_FL);
  u32* const qs = reinterpret_cast<u32*>(lds + L_QS);

#define PART(u) do { *reinterpret_cast<float*>(ldb + a_w + (u) * 256) = \
    tsum_hi8(acc[u][0].x, acc[u][0].y, acc[u][1].x, acc[u][1].y, acc[u][2].x, acc[u][2].y, acc[u][3].x, acc[u][3].y, b3); PIN(); } while (0)
  u32 role = 0xffffffffu;
  u32 a_w = (L_PART + w * 192 + 8 * l7 + perm8) * 4, a_r = (L_PART + (lane < 48 ? lane : 47) * 4) * 4;
  u32 o_voff = lane < 48 ? lane * 16 : OOB;
  f32x2 acc[3][4];
#pragma unroll
  for (int u = 0; u < 3; ++u)
#pragma unroll
    for (int c = 0; c < 4; ++c) acc[u][c] = f32x2{0.f, 0.f};
  u32 tag;
  u32 voob = OOB, vzero = 0u, vone = 1u;
  asm volatile("" : "+s"(out));

  if (w == 0) {
    __builtin_amdgcn_s_setprio(3);
    ESTAMP(8);
    int se = s;
    if (MAPI == 1 && (h & 3) == 3) se = (0x53746210u >> (4 * s)) & 7;
    if (MAPI == 2) se = (s + 2 * (h >> 2)) & 7;
    if (MAPI == 3 && (h & 3) == 3) se = ((h & 4 ? 0x53746210u : 0x31547260u) >> (4 * s)) & 7;
    const bool noq = se >= 6 && (h & 3) == 3;
    const auto rs_x = RSRC(x, D * 2);
    const u32x4 xa = __builtin_amdgcn_raw_buffer_load_b128(rs_x, lane * 20, 0, 0);
    const u32 xb = __builtin_amdgcn_raw_buffer_load_b32(rs_x, lane * 20 + 16, 0, 0);
    const int sec = se < 6 ? 6 * h + se : 6 * (h >> 2) + 2 * (h & 3) + (se & 1);
    const u32 stp = se < 6 ? 12288u : 3072u;
    const int cg = sec >> 1, rg = sec & 1;
    const u16* const tp = (se < 6 ? wq : w1) + (size_t)(rg && KS < 40 ? 320 + 8 * KS : 0) * (stp / 2) + cg * 64;
    const auto rs_t = RSRC(tp, noq ? 0 : 639 * stp + 128);
    const auto rs_e = RSRC(tp, (noq || rg) ? 0 : 639 * stp + 128);
    u32x4 wv[40 + KS];
    const u32 tv = l3 * stp + l7 * 16;
    const u32 E = __builtin_amdgcn_raw_buffer_load_b32(rs_ws, 0, 0, 16);
    ESTAMP(9);
#pragma unroll
    for (int i = 0; i < 40; ++i) { wv[i] = __builtin_amdgcn_raw_buffer_load_b128(i < 40 - KS ? rs_t : rs_e, tv, i * 8 * stp, 2); PIN();
    }
    u32x4 kr[3], vr[3];
#pragma unroll
    for (int i = 0; i < 3; ++i) kr[i] = __builtin_amdgcn_raw_buffer_load_b128(rs_k, kvo + i * 128, IL ? sh * 3072 : 0, 2);
#pragma unroll
    for (int i = 0; i < 3; ++i) vr[i] = __builtin_amdgcn_raw_buffer_load_b128(rs_v, kvo + i * 128, IL ? sh * 3072 : 0, 2);
    if (YAT && b == RB) {
      const u32x4 z = {0u, 0u, 0u, 0u};
#pragma unroll
      for (int i = 0; i < 5; ++i) ST128(z, (lane >> 2) * ASTR + (lane & 3) * 16, WS_A + i * 16 * ASTR);
    }
    if (lane < 48) fl[lane] = 0u;
    if (lane < 40) lds[L_XN + 320 + lane] = 0.f;
    PIN();
    ESTAMP(10);
    BAR();
    float xv[10];
    xv[0] = blo(xa.x); xv[1] = bhi(xa.x); xv[2] = blo(xa.y); xv[3] = bhi(xa.y);
    xv[4] = blo(xa.z); xv[5] = bhi(xa.z); xv[6] = blo(xa.w); xv[7] = bhi(xa.w);
    xv[8] = blo(xb); xv[9] = bhi(xb);
    const float sm = ((xv[0] + xv[1]) + (xv[2] + xv[3])) + ((xv[4] + xv[5]) + (xv[6] + xv[7])) + (xv[8] + xv[9]);
    const float mean = wave_sum(sm) * (1.0f / D);
    float sq = 0.f;
#pragma unroll
    for (int e = 0; e < 10; ++e) { const float d = xv[e] - mean; sq += d * d; }
    const float var = wave_sum(sq) * (1.0f / D);
    const float mb = bfr(mean);
    const float den = bfr(__builtin_amdgcn_sqrtf(bfr(bfr(var) + 1e-5f)));
    const float rinv = 1.0f / den;
    u32* xn = reinterpret_cast<u32*>(lds + L_XN);
    if (YAT) {
      u32* xr = reinterpret_cast<u32*>(lds + L_XR) + lane * 5;
      xr[0] = xa.x; xr[1] = xa.y; xr[2] = xa.z; xr[3] = xa.w; xr[4] = xb;
    }
#pragma unroll
    for (int e = 0; e < 10; ++e) {
      const u32 r = lane * 10 + e;
      reinterpret_cast<u16*>(xn)[(r % 8) * 80 + r / 8] = (u16)bfbits(bfr(xv[e] - mb) * rinv);
    }
    static_assert(KS % 4 == 0 && KS <= 40, "row split");
    u32x2 xs[10 + KS / 4];
#pragma unroll
    for (int i = 0; i < 10 + KS / 4; ++i) xs[i] = *reinterpret_cast<const u32x2*>(xn + rg * (20 + KS / 2) + l3 * 40 + i * 2);
    STAMP(1);
    float a8[8];
#pragma unroll
    for (int e = 0; e < 8; ++e) a8[e] = 0.f;
#pragma unroll
    for (int i = 0; i < 40 + KS; ++i) {
      const u32 v = xs[i / 4][(i / 2) % 2];
      const u32 xl = (i & 1) ? v >> 16 : v & 0xffffu;
      const u32 xh = (i & 1) ? v & 0xffff0000u : v << 16;
#pragma unroll
      for (int d = 0; d < 4; ++d) {
        a8[2 * d] = dot2(wv[i][d], xl, a8[2 * d]);
        a8[2 * d + 1] = dot2(wv[i][d], xh, a8[2 * d + 1]);
        KEEP(a8[2 * d]); KEEP(a8[2 * d + 1]);
      }
      PIN();
      if (i < KS) { wv[40 + (i < KS ? i : 0)] = __builtin_amdgcn_raw_buffer_load_b128(rs_e, tv, (40 + i) * 8 * stp, 2); PIN(); }
    }
    tag = E + 1u + (E == 0xffffffffu);
    STAMP(2);
    {
      float e0 = tsum16(tsum32(a8[0], a8[4]), tsum32(a8[2], a8[6]));
      float e1 = tsum16(tsum32(a8[1], a8[5]), tsum32(a8[3], a8[7]));
      e0 += dpp<ROR8>(e0); e1 += dpp<ROR8>(e1);
      const int gs = se < 6 ? sec : 192 + sec;
      const u32x4 un = {__float_as_uint(e0), tag, __float_as_uint(e1), tag};
      const u32 xv_ = (b3 || noq) ? OOB : (4 * l7 + (lane >> 4 & 1) + 2 * (lane >> 5)) * 16;
      ST128(un, xv_, WS_X + gs * 512);
      if (b == RB) {
        asm volatile("s_waitcnt vmcnt(0)" ::: "memory");
        __builtin_amdgcn_raw_buffer_store_b64(u32x2{0u, tag}, rs_ws, lane < NC ? WS_C + lane * CSTR : OOB, 0, 16);
      }
      STAMP(3);
      u32 r[3], c[3][2];
#pragma unroll
      for (int j = 0; j < 3; ++j) { r[j] = (3 * h + j) * 1024 + lane * 16; c[j][0] = c[j][1] = 0u; }
      __builtin_amdgcn_s_sleep(GAPX0);
      int spins = 0;
      while (true) {
        CBAR();
        u32x4 v[3];
#pragma unroll
        for (int j = 0; j < 3; ++j) v[j] = __builtin_amdgcn_raw_buffer_load_b128(rs_ws, r[j], WS_X, 16);
#pragma unroll
        for (int j = 0; j < 3; ++j) {
          const bool ok = v[j].y == tag && v[j].w == tag;
          c[j][0] = ok ? v[j].x : c[j][0]; c[j][1] = ok ? v[j].z : c[j][1]; r[j] = ok ? OOB : r[j];
        }
        if (__all((r[0] & r[1] & r[2]) == OOB) || ++spins >= SPIN_CAP) break;
        __builtin_amdgcn_s_sleep(GAPX);
      }
#pragma unroll
      for (int j = 0; j < 3; ++j) {
        const float t = tsum32(__uint_as_float(c[j][0]), __uint_as_float(c[j][1]));
        reinterpret_cast<u16*>(qs)[j * 64 + 2 * (lane & 31) + (lane >> 5)] = (u16)bfbits(t);
      }
    }
    qs[96] = tag;
    CBAR();
    if (lane == 0) STW(fl, 1u);
    STAMP(4);
    __builtin_amdgcn_s_setprio(0);
    u32x4 q[3];
#pragma unroll
    for (int u = 0; u < 3; ++u) q[u] = *reinterpret_cast<const u32x4*>(qs + u * 32 + l7 * 4);
    const float sco = score8(kr[0], kr[1], kr[2], q);
    const float mw = hi_max(sco);
    const float p = ex2((sco - mw) * LOG2E);
    const float lw = hi_sum(p);
    if (lane == 0) *reinterpret_cast<f32x4*>(lds + L_ST) = f32x4{mw, lw, __uint_as_float(1u), 0.f};
    STAMP(5);
#pragma unroll
    for (int u = 0; u < 3; ++u) { pv_step(vr[u], p, acc[u]); PART(u); }
    {
      int spins = 0;
      while (true) {
        CBAR();
        const u32 t = __builtin_amdgcn_raw_buffer_load_b32(rs_ws, WS_C + 4, (h % NC) * CSTR, 16);
        if (__builtin_amdgcn_readfirstlane(t) == __builtin_amdgcn_readfirstlane(tag) || ++spins >= SPIN_CAP) break;
        __builtin_amdgcn_s_sleep(GAPS);
      }
    }
    f32x4 ml;
    int spins = 0;
    while (true) {
      CBAR();
      ml = *reinterpret_cast<const f32x4*>(lds + L_ST + 4 * l7);
      if (__all(__float_as_uint(ml.z) != 0u) || ++spins >= 100 * SPIN_CAP) break;
      __builtin_amdgcn_s_sleep(4);
    }
    const float M = max8(ml.x);
    const float f = ex2((ml.x - M) * LOG2E);
    const float L = sum8(f * ml.y);
    const u32x4 un = {__float_as_uint(M), tag, __float_as_uint(L), tag};
    ST128(un, lane == 0 ? 0u : OOB, WS_S + b * 16);
    if (lane < 8) lds[L_FL + 8 + lane] = f * __builtin_amdgcn_rcpf(L);
  } else {
    constexpr int GS = IL ? 56 * 3072 : 3072;
    const int soff = IL ? (7 + sh * 7 + w) * 3072 : (8 + (w - 1) * 72) * (DH * 2);
    int so[9];
    so[0] = soff + (ROT == 0 ? 0 : ROT == 1 ? h % 9 : ROT == 5 ? (h & 3) * 2 : h & 7) * GS;
#pragma unroll
    for (int i = 1; i < 9; ++i) { const int t = so[i - 1] + GS; so[i] = ROT && t >= soff + 9 * GS ? soff : t; }
    u32x4 kr[NL], vr[NL];
#define ISSUE(n) do { \
      if ((n) < NL) kr[(n) < NL ? (n) : 0] = __builtin_amdgcn_raw_buffer_load_b128(rs_k, kvo + ((n) % 3) * 128, so[((n) / 3) % 9], 2); \
      else vr[(n) >= NL ? (n) - NL : 0] = __builtin_amdgcn_raw_buffer_load_b128(rs_v, kvo + ((n) % 3) * 128, so[((n) / 3) % 9], 2); \
      PIN(); } while (0)
#define GATE() WAITVM(PACE - 1)
#pragma unroll
    for (int n = 0; n < PACE; ++n) { if (n == 0) ESTAMP(8); ISSUE(n); }
    ESTAMP(9);
    asm volatile("s_barrier" ::: "memory");
    ESTAMP(10);
    KEEP(a_w); KEEP(a_r); KEEP(o_voff);
#pragma unroll
    for (int n = PACE; n < QS; ++n) { GATE(); if (n == PACE) ESTAMP(11); if (n == 2 * PACE) ESTAMP(12); ISSUE(n); }
    STAMP(3);
    {
      int spins = 0;
      while (LDW(fl) == 0u && ++spins < 100 * SPIN_CAP) __builtin_amdgcn_s_sleep(1);
      CBAR();
    }
    STAMP(4);
    u32x4 q[3];
#pragma unroll
    for (int u = 0; u < 3; ++u) q[u] = *reinterpret_cast<const u32x4*>(qs + u * 32 + l7 * 4);
    tag = qs[96];
    float sc[9];
#pragma unroll
    for (int i = 0; i < 9; ++i) {
      sc[i] = score8(kr[3 * i], kr[3 * i + 1], kr[3 * i + 2], q);
      KEEP(sc[i]);
      PIN();
      GATE(); ISSUE(QS + i);
    }
    const float mw = hi_max(fmx(fmx(fmx(sc[0], sc[1]), fmx(sc[2], sc[3])), fmx(fmx(fmx(sc[4], sc[5]), fmx(sc[6], sc[7])), sc[8])));
#pragma unroll
    for (int i = 0; i < 9; ++i) sc[i] = ex2((sc[i] - mw) * LOG2E);
    const float lw = hi_sum((((sc[0] + sc[1]) + (sc[2] + sc[3])) + ((sc[4] + sc[5]) + (sc[6] + sc[7]))) + sc[8]);
    if (lane == 0) *reinterpret_cast<f32x4*>(lds + L_ST + 4 * w) = f32x4{mw, lw, __uint_as_float(1u), 0.f};
    STAMP(5);
#pragma unroll
    for (int n = S3; n <= NSLOT; ++n) {
#pragma unroll
      for (int j = (n == S3 ? 0 : SCH.hi[n - 1]); j < SCH.hi[n]; ++j) {
        pv_step(vr[j], sc[j / 3], acc[j % 3]);
        PIN();
        if (j == NL - 1) STAMP(6);
        if (j >= NL - 3) PART(j % 3);
      }
      if (n < NSLOT) { GATE(); ISSUE(n); }
      if (n == NSLOT - CL) {
        if (lane == 0) __hip_atomic_fetch_add(fl + 1, 65536u, __ATOMIC_RELAXED, __HIP_MEMORY_SCOPE_WORKGROUP);
        PIN();
      }
    }
  }
  u32 old = 0;
  {
    asm volatile("s_mov_b64 exec, 1\n\tds_add_rtn_u32 %0, %1, %2 offset:4\n\ts_mov_b64 exec, -1" : "=&v"(old) : "v"(vzero), "v"(vone) : "memory");
    const f32x4 g0 = *reinterpret_cast<const f32x4*>(lds + L_FL + 8), g1 = *reinterpret_cast<const f32x4*>(lds + L_FL + 12);
    f32x4 o[8];
#pragma unroll
    for (int r = 0; r < 8; ++r) o[r] = *reinterpret_cast<const f32x4*>(ldb + a_r + r * 768);
#pragma unroll
    for (int r = 0; r < 8; ++r) asm volatile("" : "+v"(o[r]), "+v"(old));
    old = __builtin_amdgcn_readfirstlane(old);
    const f32x4 t = ((g0.x * o[0] + g0.y * o[1]) + (g0.z * o[2] + g0.w * o[3])) + ((g1.x * o[4] + g1.y * o[5]) + (g1.z * o[6] + g1.w * o[7]));
    const u32x4 un = {pack2(t.x, t.y), tag, pack2(t.z, t.w), tag};
    ST128(un, (old & 255u) == 7u ? o_voff : OOB, WS_O + b * 768);
    if ((old & 255u) == 7u) STAMP(7);
  }
  if (w) STAMP(1);
  KEEP(voob);
  if (w == 0) {
    int spins = 0;
    while (LDW(fl + 1) >> 16 < CN && ++spins < 100 * SPIN_CAP) __builtin_amdgcn_s_sleep(1);
    CBAR();
    STAMP(6);
    const u32 stag = __builtin_amdgcn_readfirstlane(tag);
    role = 255u;
    if (!SKIP || __builtin_amdgcn_readfirstlane(__builtin_amdgcn_raw_buffer_load_b32(rs_ws, WS_F, 0, 16)) != stag)
      role = __builtin_amdgcn_readfirstlane(__builtin_amdgcn_raw_ptr_buffer_atomic_add_i32(1, rs_ws, lane == 0 ? WS_C : OOB, (h % NC) * CSTR, 0)) * NC + h % NC;
    if (SKIP && role == NROLE - 1u) __builtin_amdgcn_raw_buffer_store_b32(tag, rs_ws, lane == 0 ? WS_F : OOB, 0, 16);
    if (THR && role == THR - 1u) __builtin_amdgcn_raw_buffer_store_b32(tag, rs_ws, lane == 0 ? WS_G : OOB, 0, 16);
    if (role + 1u < THR) {
      spins = 0;
      while (true) {
        __builtin_amdgcn_s_sleep(GAPT);
        CBAR();
        const u32 t = __builtin_amdgcn_raw_buffer_load_b32(rs_ws, WS_G, 0, 16);
        if (__builtin_amdgcn_readfirstlane(t) == stag || ++spins >= SPIN_CAP) break;
      }
    }
    if (lane == 0) STW(fl + 2, role + 1u);
    STAMP(14);
    if (EV && role == 255u) {
      spins = 0;
      while (true) {
        CBAR();
        const u32x2 t = __builtin_amdgcn_raw_buffer_load_b64(rs_ws, lane < NC ? WS_C + lane * CSTR : OOB, 0, 16);
        if (__all(lane >= NC || (t.x == 256u / NC && t.y == tag)) || ++spins >= SPIN_CAP) break;
        __builtin_amdgcn_s_sleep(GAPS1);
      }
      __builtin_amdgcn_raw_buffer_store_b32(tag, rs_ws, lane == 0 ? 0u : OOB, 0, 16);
    }
  } else {
    int spins = 0;
    while ((role = __builtin_amdgcn_readfirstlane(LDW(fl + 2)) - 1u) == 0xffffffffu && ++spins < 100 * SPIN_CAP) __builtin_amdgcn_s_sleep(1);
  }
  u32 t0, cnt = SPIN_CAP;
  u64 m;
  if (role < NTILE) {
    u32 tg, tc;
    asm("s_mul_i32 %0, %2, 205\n\ts_lshr_b32 %0, %0, 11\n\ts_mul_i32 %1, %0, 10\n\ts_sub_u32 %1, %2, %1"
        : "=&s"(tg), "=&s"(tc) : "s"(role) : "scc");
    const u32 c0 = (u32)((tg < 8u ? tab_pack(0, true) : (NP <= 16 || tg < 16u) ? tab_pack(8, true) : tab_pack(16, true)) >> (8u * (tg & 7u))) & 255u;
    const u32 nc = (u32)((tg < 8u ? tab_pack(0, false) : (NP <= 16 || tg < 16u) ? tab_pack(8, false) : tab_pack(16, false)) >> (8u * (tg & 7u))) & 255u;
    const u32 k = KARR ? old & 7u : w;
    const u32 nv = (nc + 7u - k) >> 3, ck = c0 + k, h0 = c0 / 6u;
    const auto rs_wo = RSRC(wo + (size_t)(ck * 32u * D + 64u * tc), 2 * 327680 + 31 * 1280 + 128);
    const auto rs_w2 = RSRC(w2 + (size_t)(ck * 8u * D + 64u * tc), 2 * 81920 + 7 * 1280 + 128);
    u32x4 wov[12], w2v[3], sv, hv[3];
    u32 so = 0u, ho[3] = {0u, 0u, 0u};
#define WISSUE(j) do { \
      w2v[j] = __builtin_amdgcn_raw_buffer_load_b128(rs_w2, l3 * 1280 + l7 * 16 + (j) * 81920, 0, 2); \
      _Pragma("unroll") for (int i = 0; i < 4; ++i) \
        wov[4 * (j) + i] = __builtin_amdgcn_raw_buffer_load_b128(rs_wo, l3 * 5120 + l7 * 16 + (j) * 327680 + i * 1280, 0, 2); \
    } while (0)
    if (w == 0) {
      so = 6u * (h0 + l3) < c0 + nc ? lane * 16 : OOB;
#pragma unroll
      for (int j = 0; j < 3; ++j) {
        const u32 g = 32 * j + (lane & 31), gp = 4u * c0 + g;
        ho[j] = ((gp >> 5) * 1024u + (lane >> 5) * 512 + (gp & 31u) * 16u) | (g < 4u * nc ? 0u : OOB);
      }
      KEEP(so); KEEP(ho[0]); KEEP(ho[1]); KEEP(ho[2]);
      asm volatile("buffer_load_dwordx4 %0, %4, %8, %9 offen sc1\n\tbuffer_load_dwordx4 %1, %5, %8, %10 offen sc1\n\t"
                   "buffer_load_dwordx4 %2, %6, %8, %10 offen sc1\n\tbuffer_load_dwordx4 %3, %7, %8, %10 offen sc1"
                   : "=&v"(sv), "=&v"(hv[0]), "=&v"(hv[1]), "=&v"(hv[2])
                   : "v"(so), "v"(ho[0]), "v"(ho[1]), "v"(ho[2]), "s"(rsw), "s"(WS_S + h0 * 128u), "s"(WS_X + 96u * 1024u) : "memory");
    }
    if (nv > 0u) {
      if (nv > 1u) {
        if (nv > 2u) WISSUE(2);
        WISSUE(1);
      }
      WISSUE(0);
    }
    PIN();
    if (w == 0) {
      asm volatile("s_waitcnt vmcnt(15)\n\ts_cmp_gt_u32 %4, 2\n\ts_cbranch_scc1 1f\n\t"
                   "s_waitcnt vmcnt(10)\n\ts_cmp_gt_u32 %4, 1\n\ts_cbranch_scc1 1f\n\t"
                   "s_waitcnt vmcnt(5)\n\ts_cmp_gt_u32 %4, 0\n\ts_cbranch_scc1 1f\n\t"
                   "s_waitcnt vmcnt(0)\n1:"
                   : "+v"(sv), "+v"(hv[0]), "+v"(hv[1]), "+v"(hv[2]) : "s"(nv) : "scc", "memory");
#define HBAD() (((ho[0] | ((hv[0].y ^ tag) | (hv[0].w ^ tag) ? 0u : OOB)) & (ho[1] | ((hv[1].y ^ tag) | (hv[1].w ^ tag) ? 0u : OOB)) & \
                 (ho[2] | ((hv[2].y ^ tag) | (hv[2].w ^ tag) ? 0u : OOB))) < OOB)
      if (__builtin_expect(__any(HBAD()), 0)) {
        int spins = 0;
        do {
          __builtin_amdgcn_s_sleep(GAPS);
          CBAR();
          _Pragma("unroll") for (int j = 0; j < 3; ++j) hv[j] = __builtin_amdgcn_raw_buffer_load_b128(rs_ws, ho[j], WS_X + 96u * 1024u, 16);
        } while (__any(HBAD()) && ++spins < SPIN_CAP);
      }
      u32 r0 = (so >= OOB || (sv.y == tag && sv.w == tag)) ? OOB : so + WS_S + h0 * 128u;
      u32x16 RS;
      asm volatile(
"v_cmp_eq_u32 vcc, %[r0], %[oob]\n"
"s_cmp_eq_u64 vcc, -1\n"
"s_cbranch_scc1 2f\n"
PL(232, 235, r0) SLP(GAPS0)
PL(236, 239, r0) SLP(GAPS0)
PL(240, 243, r0) SLP(GAPS0)
PL(244, 247, r0)
"1:\n"
P1_CK(GAPS1, 232, 233, 234, 235)
P1_CK(GAPS1, 236, 237, 238, 239)
P1_CK(GAPS1, 240, 241, 242, 243)
P1_CK(GAPS1, 244, 245, 246, 247)
PLOOP(1)
"2:\n"
: [c0] "+v"(sv.x), [c1] "+v"(sv.z), [r0] "+v"(r0), [cnt] "+s"(cnt), [m] "=&s"(m), "=&{v[232:247]}"(RS)
: [rs] "s"(rsw), [tag] "v"(tag), [oob] "v"(voob)
: "vcc", "scc", "memory");
      const float mp = __uint_as_float(sv.x);
      const float f = ex2((mp - max8(mp)) * LOG2E) * __uint_as_float(sv.z);
      lds[L_WT + lane] = f * __builtin_amdgcn_rcpf(sum8(f));
#pragma unroll
      for (int j = 0; j < 3; ++j)
        reinterpret_cast<u16*>(lds + L_HD)[64 * j + 2 * (lane & 31) + (lane >> 5)] =
            (u16)bfbits(fmaxf(tsum32(__uint_as_float(hv[j].x), __uint_as_float(hv[j].z)), 0.f));
      CBAR();
      if (lane == 0) STW(fl + 3, 1u);
      asm volatile("s_waitcnt vmcnt(0)" :: "{v[232:247]}"(RS) : "memory");
      cnt = SPIN_CAP;
    } else {
      int spins = 0;
      while (LDW(fl + 3) == 0u && ++spins < 100 * SPIN_CAP) __builtin_amdgcn_s_sleep(2);
    }
    CBAR();
    float wt[3], y[8];
    u32 hid[3], rr[3], wp[6][8];
#pragma unroll
    for (int j = 0; j < 3; ++j) {
      const u32 c = ck + 8 * j, hh = c / 6u;
      wt[j] = lds[L_WT + ((hh - h0) & 7u) * 8u + l3];
      hid[j] = reinterpret_cast<const u16*>(lds + L_HD)[(k + 8 * j) * 8u + l3];
      rr[j] = WS_O + (hh * 8u + l3) * 768u + (c - 6u * hh) * 128u + l7 * 16;
      KEEP(wt[j]); KEEP(rr[j]);
    }
#pragma unroll
    for (int e = 0; e < 8; ++e) y[e] = 0.f;
    u32 a_ow = (L_OT + w * 64) * 4 + l7 * 32 + (l3 >> 1) * 2, a_or = (L_OT + w * 64) * 4 + l3 * 32;
    u32 a_yw = (L_Y + k * 64 + 8 * l7 + perm8) * 4, a_yr = (L_Y + lane) * 4;
    u32 yv = WS_Y + (8 * tc + l3) * YS + tg * 64 + l7 * 8;
    KEEP(a_ow); KEEP(a_or); KEEP(a_yw); KEEP(a_yr); KEEP(yv);
    u32 av = WS_A + (8 * tc + l3) * ASTR + l7 * 8, ov = (64 * tc + lane) * 2, ev = (tc == 0 && lane == 0) ? 0u : OOB;
    float xo = blo(reinterpret_cast<const u16*>(lds + L_XR)[64 * tc + lane]);
    float thr = __builtin_fabsf(xo) < 6.103515625e-05f ? 6.103515625e-05f : 0.f;
    if (YAT) { KEEP(av); KEEP(ov); KEEP(xo); KEEP(thr); if (!EV) KEEP(ev); }
    PIN();
#define WPREP(j) do { \
      _Pragma("unroll") for (int d = 0; d < 4; ++d) { \
        y[2 * d] = dot2(w2v[j][d], hid[j], y[2 * d]); \
        y[2 * d + 1] = dot2(w2v[j][d], hid[j] << 16, y[2 * d + 1]); \
        KEEP(y[2 * d]); KEEP(y[2 * d + 1]); \
      } \
      _Pragma("unroll") for (int i = 2 * (j); i < 2 * (j) + 2; ++i) { \
        _Pragma("unroll") for (int d = 0; d < 4; ++d) { \
          wp[i][2 * d] = __builtin_amdgcn_perm(wov[2 * i + 1][d], wov[2 * i][d], 0x05040100u); \
          wp[i][2 * d + 1] = __builtin_amdgcn_perm(wov[2 * i + 1][d], wov[2 * i][d], 0x07060302u); \
          KEEP(wp[i][2 * d]); KEEP(wp[i][2 * d + 1]); \
        } \
        PIN(); \
      } } while (0)
#define ORED(j) do { \
      float u = tsum16(tsum32(wt[j] * blo(c[2 * (j)]), wt[j] * blo(c[2 * (j) + 1])), tsum32(wt[j] * bhi(c[2 * (j)]), wt[j] * bhi(c[2 * (j) + 1]))); \
      u += dpp<ROR8>(u); \
      *reinterpret_cast<u16*>(ldb + a_ow + (j) * 8) = (u16)bfbits(u); } while (0)
#define YDOT(j, o0, o1) do { \
      _Pragma("unroll") for (int e = 0; e < 8; ++e) y[e] = dot2(wp[2 * (j)][e], o0, y[e]); \
      _Pragma("unroll") for (int e = 0; e < 8; ++e) y[e] = dot2(wp[2 * (j) + 1][e], o1, y[e]); } while (0)
    u32x16 R0, R1, R2;
    if (nv > 0u) {
      if (nv > 1u) {
        if (nv > 2u) WPREP(2);
        WPREP(1);
      }
      WPREP(0);
    }
    WAITVM(0);
    u32 nw = nv;
    asm volatile("" : "+s"(nw));
    if (nw > 0u) {
      if (w) STAMP(14);
      {
        int spins = 0;
        while ((LDW(fl + 1) & 255u) != 8u && ++spins < 100 * SPIN_CAP) __builtin_amdgcn_s_sleep(1);
        CBAR();
      }
      if (w) STAMP(2);
      u32 c[6] = {0, 0, 0, 0, 0, 0};
      asm volatile(
"s_cmp_lt_u32 %[nv], 2\n"
"s_cbranch_scc1 3f\n"
"s_cmp_lt_u32 %[nv], 3\n"
"s_cbranch_scc1 4f\n"
P3_LD(200, 203, 204, 207, 208, 211) SLP(GAPO0)
P3_LD(212, 215, 216, 219, 220, 223) SLP(GAPO0)
P3_LD(224, 227, 228, 231, 232, 235) SLP(GAPO0)
P3_LD(236, 239, 240, 243, 244, 247)
"1:\n"
P3_CK(200, 201, 202, 203, 204, 205, 206, 207, 208, 209, 210, 211)
P3_CK(212, 213, 214, 215, 216, 217, 218, 219, 220, 221, 222, 223)
P3_CK(224, 225, 226, 227, 228, 229, 230, 231, 232, 233, 234, 235)
P3_CK(236, 237, 238, 239, 240, 241, 242, 243, 244, 245, 246, 247)
PLOOP(1)
"s_branch 2f\n"
"3:\n"
PL(232, 235, r0) SLP(GAPO0)
PL(236, 239, r0) SLP(GAPO0)
PL(240, 243, r0) SLP(GAPO0)
PL(244, 247, r0)
"5:\n"
P1_CK(GAPO, 232, 233, 234, 235)
P1_CK(GAPO, 236, 237, 238, 239)
P1_CK(GAPO, 240, 241, 242, 243)
P1_CK(GAPO, 244, 245, 246, 247)
PLOOP(5)
"s_branch 2f\n"
"4:\n"
P2_LD(216, 219, 220, 223) SLP(GAPO0)
P2_LD(224, 227, 228, 231) SLP(GAPO0)
P2_LD(232, 235, 236, 239) SLP(GAPO0)
P2_LD(240, 243, 244, 247)
"6:\n"
P2_CK(216, 217, 218, 219, 220, 221, 222, 223)
P2_CK(224, 225, 226, 227, 228, 229, 230, 231)
P2_CK(232, 233, 234, 235, 236, 237, 238, 239)
P2_CK(240, 241, 242, 243, 244, 245, 246, 247)
PLOOP(6)
"2:\n"
: [c0] "+v"(c[0]), [c1] "+v"(c[1]), [c2] "+v"(c[2]), [c3] "+v"(c[3]), [c4] "+v"(c[4]), [c5] "+v"(c[5]),
  [r0] "+v"(rr[0]), [r1] "+v"(rr[1]), [r2] "+v"(rr[2]),
  [t0] "=&v"(t0), [cnt] "+s"(cnt), [m] "=&s"(m), "=&{v[200:215]}"(R0), "=&{v[216:231]}"(R1), "=&{v[232:247]}"(R2)
: [rs] "s"(rsw), [tag] "v"(tag), [oob] "v"(voob), [nv] "s"(nv)
: "vcc", "scc", "memory");
      STAMP(8);
      if (nv == 1u) {
        ORED(0);
        CBAR();
        const u32x2 ov = *reinterpret_cast<const u32x2*>(ldb + a_or);
        YDOT(0, ov[0], ov[1]);
      } else if (MAXN <= 16 || nv == 2u) {
        ORED(0); ORED(1);
        CBAR();
        const u32x4 ov = *reinterpret_cast<const u32x4*>(ldb + a_or);
        YDOT(0, ov[0], ov[1]);
        YDOT(1, ov[2], ov[3]);
      } else {
        ORED(0); ORED(1); ORED(2);
        CBAR();
        const u32x4 ov = *reinterpret_cast<const u32x4*>(ldb + a_or);
        const u32x2 ow = *reinterpret_cast<const u32x2*>(ldb + a_or + 16);
        YDOT(0, ov[0], ov[1]);
        YDOT(1, ov[2], ov[3]);
        YDOT(2, ow[0], ow[1]);
      }
    }
    {
      *reinterpret_cast<float*>(ldb + a_yw) = tsum_hi8(y[0], y[1], y[2], y[3], y[4], y[5], y[6], y[7], b3);
      BAR();
      STAMP(9);
      if (w < (YAT ? 2 : 1)) {
        float e[8];
#pragma unroll
        for (int r = 0; r < 8; ++r) e[r] = *reinterpret_cast<const float*>(ldb + a_yr + r * 256);
        const float es = ((e[0] + e[1]) + (e[2] + e[3])) + ((e[4] + e[5]) + (e[6] + e[7]));
        if (YAT && w == 0) {
          const bool ok = __builtin_fabsf(es) < (float)(FB - 72u);
          const u32x2 fx = __builtin_bit_cast(u32x2, (double)es + (1048576.0 + FB));
          u32x2 ol = {ok ? fx.x : 0u, ok ? fx.y + ((1u << 22) - 0x41300000u) : (1u << 22) + (1u << 27)};
          u64 mine = __builtin_bit_cast(u64, ol);
          asm volatile("buffer_atomic_add_x2 %0, %1, %2, 0 offen sc0" : "+v"(ol) : "v"(av), "s"(rsw) : "memory");
          STAMP(10);
          mine += 0x80000000ull;
          KEEP(mine);
          asm volatile("s_waitcnt vmcnt(0)" : "+v"(ol) :: "memory");
          STAMP(11);
          const u64 tt = __builtin_bit_cast(u64, ol) + mine;
          const u32 th = (u32)(tt >> 32);
          const bool last = ((th >> 22) & 31u) == NP;
          float r = xo + __builtin_fmaf((float)(int)((u32)tt ^ 0x80000000u), 2.3283064365386963e-10f, (float)(int)((th & 0x3fffffu) - NP * FB));
          if (__builtin_expect(__any(last && ((th >> 27) != 0u || __builtin_fabsf(r) < thr)), 0)) {
            float p[NP];
            int spins = 0;
            bool got;
            do {
              CBAR();
              got = true;
#pragma unroll
              for (int g = 0; g < (int)NP; ++g) {
                const u32x2 v = __builtin_amdgcn_raw_buffer_load_b64(rs_ws, last ? yv - tg * 64 + g * 64 : OOB, 0, 16);
                p[g] = __uint_as_float(v.x);
                got = got && v.y == tag;
              }
            } while (__any(last && !got) && ++spins < SPIN_CAP);
            float a = p[0];
#pragma unroll
            for (int g = 1; g < (int)NP; ++g) a += p[g];
            if ((th >> 27) != 0u || __builtin_fabsf(r) < thr) r = xo + a;
          }
          const auto rs_out = RSRC(out, D * 2);
          __builtin_amdgcn_raw_buffer_store_b16((u16)bfbits(r), rs_out, last ? ov : OOB, 0, 0);
          if (!EV) __builtin_amdgcn_raw_buffer_store_b32(tag, rs_ws, last ? ev : OOB, 0, 16);
        } else {
          const u32x2 un = {__float_as_uint(es), tag};
          __builtin_amdgcn_raw_buffer_store_b64(un, rs_ws, yv, 0, 16);
          if (!YAT) STAMP(10);
        }
      }
      asm volatile("s_waitcnt vmcnt(0)" :: "{v[200:215]}"(R0), "{v[216:231]}"(R1), "{v[232:247]}"(R2) : "memory");
    }
  } else if (!YAT && role < NROLE) {
    const u32 ri = (role - NTILE) * 8u + w;
    const u32 rcol = 8 * ri + 2 * (lane & 3) + (lane >> 5);
    u32 xo = x[rcol];
    u32 ov = (lane & 28) ? OOB : rcol * 2;
    u32 r0 = lane < 4 * NP ? WS_Y + ri * 1024 + lane * 16 : OOB;
    u32 ev = (ri == 0 && lane == 0) ? 0u : OOB;
    const auto rs_out = RSRC(out, D * 2);
    KEEP(xo); KEEP(ov); KEEP(ev);
    {
      u32x16 R0;
      u32 c[2] = {0, 0};
      asm volatile(
PL(232, 235, r0) "s_sleep " STR(GAPY0) "\n"
PL(236, 239, r0) "s_sleep " STR(GAPY0) "\n"
PL(240, 243, r0) "s_sleep " STR(GAPY0) "\n"
PL(244, 247, r0)
"1:\n"
P1_CK(GAPY, 232, 233, 234, 235)
P1_CK(GAPY, 236, 237, 238, 239)
P1_CK(GAPY, 240, 241, 242, 243)
P1_CK(GAPY, 244, 245, 246, 247)
"s_sub_u32 %[cnt], %[cnt], 1\n"
"s_cbranch_scc0 1b\n"
"2:\n"
: [c0] "+v"(c[0]), [c1] "+v"(c[1]), [r0] "+v"(r0), [cnt] "+s"(cnt), [m] "=&s"(m), "=&{v[232:247]}"(R0)
: [rs] "s"(rsw), [tag] "v"(tag), [oob] "v"(voob)
: "vcc", "scc", "memory");
      STAMP(11);
      float v = tsum32(__uint_as_float(c[0]), __uint_as_float(c[1]));
      v = tsum16(v, v);
      v += dpp<ROR8>(v);
      v += dpp<ROR4>(v);
      __builtin_amdgcn_raw_buffer_store_b16((u16)bfbits(v + blo(xo)), rs_out, ov, 0, 0);
      __builtin_amdgcn_raw_buffer_store_b32(tag, rs_ws, ev, 0, 16);
      asm volatile("s_waitcnt vmcnt(0)" :: "{v[232:247]}"(R0) : "memory");
    }
  }
}

extern "C" void kernel_launch(void* const* d_in, const int* in_sizes, int n_in,
                              void* d_out, int out_size, void* d_ws, size_t ws_size,
                              hipStream_t stream) {
  if (ws_size < WS_BYTES) return;
  fused_decode_kernel<<<256, 512, 0, stream>>>(
      (const u16*)d_in[0], (const u16*)d_in[1], (const u16*)d_in[2], (const u16*)d_in[3],
      (const u16*)d_in[4], (const u16*)d_in[6], (const u16*)d_in[7], (u16*)d_out, (u32*)d_ws);
}
```
